```python
import torch

def _softmax(x, axis=-1):
    return torch.softmax(x, dim=axis)
def _gelu(x):
    return torch.nn.functional.gelu(x, approximate="tanh")
def _silu(x):
    return torch.nn.functional.silu(x)
def _sigmoid(x):
    return torch.sigmoid(x)
def _layernorm(x, axis=-1, eps=1e-5):
    m = x.mean(dim=axis, keepdim=True)
    v = x.var(dim=axis, keepdim=True, unbiased=False)
    return (x - m) / torch.sqrt(v + eps)
def _scatter_add(tgt, idx, src, axis=0):
    return tgt.clone().index_add_(axis, idx, src)
def _segment_sum(src, seg, n):
    out = torch.zeros((n,) + src.shape[1:], dtype=src.dtype)
    return out.index_add_(0, seg, src)
def _pad(x, pw):
    flat = tuple(v for pair in reversed(pw) for v in pair)
    return torch.nn.functional.pad(x, flat)

kernel_name = 'standalone__mha_block_cached__near_ridge'

def setup_inputs():

    _w = (lambda a: (lambda t: t.to(torch.bfloat16) if t.is_floating_point() else t)(torch.as_tensor(a)))
    B, Sq, D = 8, 256, 512
    H, Sk, Dh = 32, 1024, 128
    return {
        "x": _w((lambda *s: torch.randn(*s, dtype=torch.bfloat16) * 0.8)(B, Sq, D)),
        "k_cache": _w((lambda *s: torch.randn(*s, dtype=torch.bfloat16) * 0.8)(B, H, Sk, Dh)),
        "v_cache": _w((lambda *s: torch.randn(*s, dtype=torch.bfloat16) * 0.05)(B, H, Sk, Dh)),
        "wq": _w((lambda *s: torch.randn(*s, dtype=torch.bfloat16) * 0.8)(D, H * Dh) * (D ** -0.5)),
        "wo": _w((lambda *s: torch.randn(*s, dtype=torch.bfloat16) * 0.05)(H * Dh, D) * ((H * Dh) ** -0.5)),
    }

def reference(x, k_cache, v_cache, wq, wo):
    B, Sq, D = x.shape
    H, Dh = 32, 128
    q = (lambda x, *d: x.permute(*d))((x @ wq).reshape(B, Sq, H, Dh), 0, 2, 1, 3)
    scores = torch.einsum("bhqd,bhkd->bhqk", q, k_cache) * (Dh ** -0.5)
    p = _softmax(scores, axis=-1)
    o = torch.einsum("bhqk,bhkd->bhqd", p, v_cache)
    return (lambda x, *d: x.permute(*d))(o, 0, 2, 1, 3).reshape(B, Sq, H * Dh) @ wo
```

```cpp
#include <hip/hip_runtime.h>
#include <hip/hip_bf16.h>
typedef unsigned short u16;
__device__ inline float b2f(u16 x){ return __uint_as_float(((unsigned)x)<<16); }
__device__ inline u16 f2b(float f){ unsigned u=__float_as_uint(f); return (u16)((u+0x7FFF+((u>>16)&1))>>16); }
__global__ void gemm(const u16* A,const u16* B,u16* C,int M,int N,int K){
  __shared__ float As[16][17],Bs[16][17];
  int tx=threadIdx.x&15,ty=threadIdx.x>>4; int r=blockIdx.y*16+ty,c=blockIdx.x*16+tx; float acc=0;
  for(int k=0;k<K;k+=16){ As[ty][tx]=b2f(A[(size_t)r*K+k+tx]); Bs[ty][tx]=b2f(B[(size_t)(k+ty)*N+c]); __syncthreads();
    for(int j=0;j<16;j++) acc+=As[ty][j]*Bs[j][tx]; __syncthreads(); }
  C[(size_t)r*N+c]=f2b(acc);
}
__global__ void attn(const u16* Q,const u16* K,const u16* V,u16* O){
  __shared__ float q[128],s[1024],red[128];
  int bq=blockIdx.x; int h=blockIdx.y; int b=bq>>8; int t=threadIdx.x;
  q[t]=b2f(Q[(size_t)bq*4096+h*128+t]); __syncthreads();
  const u16* Kb=K+((size_t)(b*32+h))*1024*128; const u16* Vb=V+((size_t)(b*32+h))*1024*128;
  float mx=-1e30f;
  for(int k=t;k<1024;k+=128){ float a=0; for(int d=0;d<128;d++) a+=q[d]*b2f(Kb[k*128+d]); a*=0.08838834764f; s[k]=a; mx=fmaxf(mx,a);}
  red[t]=mx; __syncthreads(); for(int o=64;o>0;o>>=1){ if(t<o) red[t]=fmaxf(red[t],red[t+o]); __syncthreads(); } mx=red[0]; __syncthreads();
  float sm=0; for(int k=t;k<1024;k+=128){ float p=__expf(s[k]-mx); s[k]=p; sm+=p; }
  red[t]=sm; __syncthreads(); for(int o=64;o>0;o>>=1){ if(t<o) red[t]+=red[t+o]; __syncthreads(); } sm=red[0];
  float a=0; for(int k=0;k<1024;k++) a+=s[k]*b2f(Vb[k*128+t]);
  O[(size_t)bq*4096+h*128+t]=f2b(a/sm);
}
extern "C" void kernel_launch(void* const* d_in, const int* in_sizes, int n_in, void* d_out, int out_size, void* d_ws, size_t ws_size, hipStream_t stream) {
  const u16* x=(const u16*)d_in[0]; const u16* k=(const u16*)d_in[1]; const u16* v=(const u16*)d_in[2]; const u16* wq=(const u16*)d_in[3]; const u16* wo=(const u16*)d_in[4];
  u16* Q=(u16*)d_ws; u16* O=Q+(size_t)2048*4096;
  gemm<<<dim3(4096/16,2048/16),256,0,stream>>>(x,wq,Q,2048,4096,512);
  attn<<<dim3(2048,32),128,0,stream>>>(Q,k,v,O);
  gemm<<<dim3(512/16,2048/16),256,0,stream>>>(O,wo,(u16*)d_out,2048,512,4096);
}
```

```cpp
#include <hip/hip_runtime.h>

__global__ __launch_bounds__(256, 1) void mha_fused(const void* x, const void* k, const void* v, const void* wq, const void* wo, void* out, void* ws) {
  __shared__ __attribute__((aligned(16))) char lds[163840];
  unsigned tid = threadIdx.x;
  asm volatile(
    "s_load_dwordx2 s[8:9], %1, 0\n"
    "s_load_dwordx2 s[12:13], %1, 24\n"
    "s_load_dwordx2 s[16:17], %1, 8\n"
    "s_load_dwordx2 s[20:21], %1, 16\n"
    "s_load_dwordx2 s[24:25], %1, 32\n"
    "s_load_dwordx2 s[40:41], %1, 40\n"
    "s_load_dwordx2 s[28:29], %1, 48\n"
    "s_memrealtime s[64:65]\n"
    "s_getpc_b64 s[96:97]\n"
    "Lpc%=:\n"
    "s_lshr_b32 s52, %2, 3\n"
    "s_mul_i32 s52, s52, 2304\n"
    "s_min_u32 s52, s52, Lend%=-Lpc%=-4096\n"
    "s_add_u32 s96, s96, s52\n"
    "s_addc_u32 s97, s97, 0\n"
    "v_mul_u32_u24 v250, 12, v0\n"
    "global_load_dword v251, v250, s[96:97]\n"
    "v_lshrrev_b32 v137, 6, v0\n"
    "v_and_b32 v136, 63, v0\n"
    "v_readfirstlane_b32 s44, v137\n"
    "s_and_b32 s52, %2, 7\n"
    "s_lshr_b32 s53, %2, 3\n"
    "s_and_b32 s49, s52, 3\n"
    "s_and_b32 s48, s53, 7\n"
    "s_lshr_b32 s50, s53, 3\n"
    "s_and_b32 s45, s52, 4\n"
    "s_add_u32 s45, s45, s50\n"
    "s_lshl_b32 s46, s49, 3\n"
    "s_add_u32 s46, s46, s48\n"
    "s_add_u32 s47, s50, s48\n"
    "s_lshl_b32 s47, s47, 2\n"
    "s_lshl_b32 s51, s44, 12\n"
    "s_lshl_b32 s54, s44, 16\n"
    "s_lshl_b32 s55, s44, 17\n"
    "s_lshl_b32 s52, s46, 8\n"
    "s_add_u32 s55, s55, s52\n"
    "s_add_u32 s52, s47, 0\n"
    "s_and_b32 s52, s52, 7\n"
    "s_lshl_b32 s68, s52, 7\n"
    "s_add_u32 s68, s68, s54\n"
    "s_lshl_b32 s76, s52, 19\n"
    "s_add_u32 s76, s76, s55\n"
    "s_xor_b32 s84, s49, 3\n"
    "s_lshl_b32 s84, s84, 14\n"
    "s_xor_b32 s85, s49, 2\n"
    "s_lshl_b32 s85, s85, 14\n"
    "s_xor_b32 s86, s49, 1\n"
    "s_lshl_b32 s86, s86, 14\n"
    "s_xor_b32 s87, s49, 0\n"
    "s_lshl_b32 s87, s87, 14\n"
    "v_and_b32 v144, 3, v136\n"
    "v_bfe_u32 v138, v136, 2, 3\n"
    "v_lshrrev_b32 v142, 4, v136\n"
    "v_lshlrev_b32 v139, 5, v142\n"
    "v_lshlrev_b32 v140, 4, v144\n"
    "v_xor_b32 v139, v139, v140\n"
    "v_lshl_add_u32 v128, v138, 11, v139\n"
    "v_bfe_u32 v143, v136, 2, 2\n"
    "v_lshl_add_u32 v140, v142, 6, v140\n"
    "v_lshl_add_u32 v132, v143, 13, v140\n"
    "v_add_u32 v133, 31744, v132\n"
    "v_add_u32 v134, 63488, v132\n"
    "v_add_u32 v135, 95232, v132\n"
    "s_waitcnt lgkmcnt(0)\n"
    "s_load_dword s67, s[12:13], s76\n"
    "s_xor_b32 s53, s76, 2097152\n"
    "s_load_dword s67, s[12:13], s53\n"
    "s_lshl_b32 s52, s45, 18\n"
    "s_add_u32 s8, s8, s52\n"
    "s_addc_u32 s9, s9, 0\n"
    "s_and_b32 s9, s9, 0xffff\n"
    "s_mov_b32 s10, 262144\n"
    "s_mov_b32 s11, 0x20000\n"
    "s_and_b32 s13, s13, 0xffff\n"
    "s_mov_b32 s14, 4194304\n"
    "s_mov_b32 s15, 0x20000\n"
    "s_add_u32 m0, s51, 32768\n"
    "s_add_u32 s52, s68, s84\n"
    "buffer_load_dwordx4 v128, s[8:11], s52 offen offset:0  lds\n"
    "buffer_load_dwordx4 v128, s[8:11], s52 offen offset:1024  lds\n"
    "s_add_u32 m0, s51, 34816\n"
    "s_add_u32 s52, s68, s85\n"
    "buffer_load_dwordx4 v128, s[8:11], s52 offen offset:0  lds\n"
    "buffer_load_dwordx4 v128, s[8:11], s52 offen offset:1024  lds\n"
    "s_add_u32 m0, s51, 98304\n"
    "s_nop 0\n"
    "buffer_load_dwordx4 v132, s[12:15], s76 offen offset:0  lds\n"
    "buffer_load_dwordx4 v133, s[12:15], s76 offen offset:1024  lds\n"
    "buffer_load_dwordx4 v134, s[12:15], s76 offen offset:2048  lds\n"
    "buffer_load_dwordx4 v135, s[12:15], s76 offen offset:3072  lds\n"
    "s_add_u32 m0, s51, 49152\n"
    "s_add_u32 s52, s68, s86\n"
    "buffer_load_dwordx4 v128, s[8:11], s52 offen offset:0  lds\n"
    "buffer_load_dwordx4 v128, s[8:11], s52 offen offset:1024  lds\n"
    "s_add_u32 m0, s51, 51200\n"
    "s_add_u32 s52, s68, s87\n"
    "buffer_load_dwordx4 v128, s[8:11], s52 offen offset:0  lds\n"
    "buffer_load_dwordx4 v128, s[8:11], s52 offen offset:1024  lds\n"
    "s_lshl_b32 s53, s45, 5\n"
    "s_add_u32 s53, s53, s46\n"
    "s_lshl_b32 s53, s53, 18\n"
    "s_load_dword s67, s[16:17], s53\n"
    "s_load_dword s67, s[20:21], s53\n"
    "s_lshl_b32 s90, s46, 2\n"
    "s_add_u32 s90, s90, s44\n"
    "s_lshl_b32 s92, s90, 12\n"
    "s_add_u32 s52, s47, 1\n"
    "s_and_b32 s52, s52, 7\n"
    "s_lshl_b32 s69, s52, 7\n"
    "s_add_u32 s69, s69, s54\n"
    "s_lshl_b32 s77, s52, 19\n"
    "s_add_u32 s77, s77, s55\n"
    "s_add_u32 s52, s47, 2\n"
    "s_and_b32 s52, s52, 7\n"
    "s_lshl_b32 s70, s52, 7\n"
    "s_add_u32 s70, s70, s54\n"
    "s_lshl_b32 s78, s52, 19\n"
    "s_add_u32 s78, s78, s55\n"
    "s_add_u32 s52, s47, 3\n"
    "s_and_b32 s52, s52, 7\n"
    "s_lshl_b32 s71, s52, 7\n"
    "s_add_u32 s71, s71, s54\n"
    "s_lshl_b32 s79, s52, 19\n"
    "s_add_u32 s79, s79, s55\n"
    "s_add_u32 s52, s47, 4\n"
    "s_and_b32 s52, s52, 7\n"
    "s_lshl_b32 s72, s52, 7\n"
    "s_add_u32 s72, s72, s54\n"
    "s_lshl_b32 s80, s52, 19\n"
    "s_add_u32 s80, s80, s55\n"
    "s_add_u32 s52, s47, 5\n"
    "s_and_b32 s52, s52, 7\n"
    "s_lshl_b32 s73, s52, 7\n"
    "s_add_u32 s73, s73, s54\n"
    "s_lshl_b32 s81, s52, 19\n"
    "s_add_u32 s81, s81, s55\n"
    "s_add_u32 s52, s47, 6\n"
    "s_and_b32 s52, s52, 7\n"
    "s_lshl_b32 s74, s52, 7\n"
    "s_add_u32 s74, s74, s54\n"
    "s_lshl_b32 s82, s52, 19\n"
    "s_add_u32 s82, s82, s55\n"
    "s_add_u32 s52, s47, 7\n"
    "s_and_b32 s52, s52, 7\n"
    "s_lshl_b32 s75, s52, 7\n"
    "s_add_u32 s75, s75, s54\n"
    "s_lshl_b32 s83, s52, 19\n"
    "s_add_u32 s83, s83, s55\n"
    "s_lshl_b32 s53, s45, 5\n"
    "s_add_u32 s53, s53, s46\n"
    "s_lshl_b32 s52, s53, 18\n"
    "s_add_u32 s16, s16, s52\n"
    "s_addc_u32 s17, s17, 0\n"
    "s_and_b32 s17, s17, 0xffff\n"
    "s_mov_b32 s18, 262144\n"
    "s_mov_b32 s19, 0x20000\n"
    "s_lshl_b32 s52, s53, 18\n"
    "s_add_u32 s20, s20, s52\n"
    "s_addc_u32 s21, s21, 0\n"
    "s_and_b32 s21, s21, 0xffff\n"
    "s_mov_b32 s22, 262144\n"
    "s_mov_b32 s23, 0x20000\n"
    "v_lshl_add_u32 v231, v143, 8, v140\n"
    "v_xor_b32 v232, 32, v231\n"
    "v_lshlrev_b32 v233, 4, v136\n"
    "v_add_u32 v139, s44, v142\n"
    "v_and_b32 v139, 3, v139\n"
    "v_lshlrev_b32 v139, 6, v139\n"
    "v_lshl_add_u32 v139, v144, 4, v139\n"
    "v_lshl_add_u32 v234, v143, 10, v139\n"
    "v_xor_b32 v139, 32, v234\n"
    "v_add_u32 v235, 3072, v139\n"
    "v_add_u32 v236, 6144, v234\n"
    "v_add_u32 v237, 9216, v139\n"
    "v_and_b32 v138, 1, v143\n"
    "v_lshlrev_b32 v138, 1, v138\n"
    "v_xor_b32 v138, v138, v142\n"
    "v_lshlrev_b32 v138, 4, v138\n"
    "v_lshl_add_u32 v139, v144, 6, v138\n"
    "v_lshl_add_u32 v228, v143, 10, v139\n"
    "v_and_b32 v139, 7, v136\n"
    "v_lshl_add_u32 v139, v139, 6, v138\n"
    "v_bfe_u32 v141, v136, 3, 1\n"
    "v_lshl_add_u32 v139, v141, 10, v139\n"
    "v_add_u32 v129, s51, v139\n"
    "v_add_u32 v130, 131072, v129\n"
    "v_lshlrev_b32 v139, 11, v142\n"
    "v_lshl_add_u32 v139, v143, 6, v139\n"
    "v_lshl_add_u32 v139, v144, 4, v139\n"
    "v_add_u32 v131, 65536, v139\n"
    "v_lshlrev_b32 v139, 10, v142\n"
    "v_lshl_add_u32 v139, v143, 6, v139\n"
    "v_lshl_add_u32 v139, v144, 3, v139\n"
    "v_and_b32 v141, 1, v142\n"
    "v_lshl_add_u32 v139, v141, 5, v139\n"
    "v_add_u32 v229, 65536, v139\n"
    "v_xor_b32 v230, 32, v229\n"
    "v_add_u32 v238, s51, v139\n"
    "v_xor_b32 v239, 32, v238\n"
    "v_add_u32 v240, 65536, v238\n"
    "v_add_u32 v241, 65536, v239\n"
    "v_add_u32 v242, 131072, v238\n"
    "v_add_u32 v243, 131072, v239\n"
    "v_cmp_eq_u32 vcc, 0, v144\n"
    "v_mov_b32 v141, 0x3f803f80\n"
    "s_mov_b32 s62, 0x3e0293ee\n"
    "s_mov_b32 s63, 0x3e0293ee\n"
    "v_cndmask_b32 v141, 0, v141, vcc\n"
    "v_accvgpr_write_b32 a112, v141\n"
    "v_accvgpr_write_b32 a113, v141\n"
    "v_accvgpr_write_b32 a114, v141\n"
    "v_accvgpr_write_b32 a115, v141\n"
    "v_mov_b32 v141, 2147418112\n"
    "s_lshr_b32 s52, s44, 1\n"
    "s_lshl_b32 s53, s49, 1\n"
    "s_add_u32 s52, s52, s53\n"
    "s_lshl_b32 s52, s52, 4\n"
    "s_lshr_b32 s53, s48, 1\n"
    "s_add_u32 s52, s52, s53\n"
    "v_lshl_add_u32 v138, v136, 2, s52\n"
    "s_lshl_b32 s52, s90, 9\n"
    "v_lshl_add_u32 v247, v136, 2, s52\n"
    "s_xor_b32 s52, s49, 7\n"
    "v_xor_b32 v139, s52, v136\n"
    "s_and_b32 s52, s90, 31\n"
    "v_lshl_add_u32 v139, v139, 5, s52\n"
    "v_lshlrev_b32 v137, 7, v136\n"
    "v_add_u32 v137, 65024, v137\n"
    "v_lshl_add_u32 v248, v139, 9, v137\n"
    "v_cmp_gt_u32 vcc, 4, v136\n"
    "s_nop 1\n"
    "v_cndmask_b32 v138, v139, v138, vcc\n"
    "v_cndmask_b32 v247, v141, v247, vcc\n"
    "v_cndmask_b32 v248, v248, v141, vcc\n"
    "v_lshlrev_b32 v138, 7, v138\n"
    "v_add_u32 v138, 131072, v138\n"
    "v_cmp_gt_u32 vcc, 7, v136\n"
    "s_nop 1\n"
    "v_cndmask_b32 v245, v141, v138, vcc\n"
    "v_cndmask_b32 v248, v141, v248, vcc\n"
    "s_lshl_b32 s52, s90, 7\n"
    "s_add_u32 s52, s52, 131072\n"
    "v_mov_b32 v138, s52\n"
    "v_cmp_eq_u32 vcc, 0, v136\n"
    "s_nop 1\n"
    "v_cndmask_b32 v225, v141, v138, vcc\n"
    "s_lshl_b32 s52, s49, 2\n"
    "s_add_u32 s52, s52, s44\n"
    "s_lshl_b32 s52, s52, 12\n"
    "s_lshr_b32 s53, s48, 2\n"
    "s_lshl_b32 s53, s53, 10\n"
    "s_add_u32 s52, s52, s53\n"
    "s_and_b32 s53, s48, 3\n"
    "s_lshl_b32 s53, s53, 2\n"
    "s_add_u32 s52, s52, s53\n"
    "v_and_b32 v138, 7, v136\n"
    "v_lshl_add_u32 v138, v138, 7, s52\n"
    "v_bfe_u32 v139, v136, 3, 1\n"
    "v_lshl_add_u32 v138, v139, 11, v138\n"
    "s_lshl_b32 s52, s90, 9\n"
    "s_add_u32 s52, s52, 65536\n"
    "v_lshl_add_u32 v139, v144, 7, s52\n"
    "v_cmp_gt_u32 vcc, 16, v136\n"
    "v_and_b32 v137, 0x33, v136\n"
    "s_nop 0\n"
    "v_cndmask_b32 v139, v139, v138, vcc\n"
    "v_cmp_gt_u32 vcc, 19, v136\n"
    "s_nop 1\n"
    "v_cndmask_b32 v254, v141, v139, vcc\n"
    "v_cmp_eq_u32 vcc, 0, v137\n"
    "s_nop 1\n"
    "v_cndmask_b32 v244, v141, v138, vcc\n"
    "v_mov_b32 v253, v136\n"
    "s_lshr_b32 s54, s48, 2\n"
    "s_lshl_b32 s55, s50, 1\n"
    "s_add_u32 s55, s55, s54\n"
    "s_lshl_b32 s47, s55, 13\n"
    "s_mov_b32 s55, 0\n"
    "s_lshl_b32 s52, s49, 2\n"
    "s_add_u32 s52, s52, s44\n"
    "s_lshl_b32 s88, s52, 18\n"
    "s_lshl_b32 s53, s55, 15\n"
    "s_add_u32 s88, s88, s53\n"
    "s_and_b32 s53, s48, 3\n"
    "s_lshl_b32 s53, s53, 8\n"
    "s_add_u32 s88, s88, s53\n"
    "s_xor_b32 s53, s88, 196608\n"
    "s_add_u32 s47, s47, s53\n"
    "s_lshl_b32 s89, s52, 15\n"
    "s_lshl_b32 s53, s54, 13\n"
    "s_add_u32 s89, s89, s53\n"
    "s_lshr_b32 s53, s55, 2\n"
    "s_lshl_b32 s53, s53, 14\n"
    "s_add_u32 s89, s89, s53\n"
    "s_and_b32 s53, s55, 3\n"
    "s_lshl_b32 s53, s53, 10\n"
    "s_add_u32 s89, s89, s53\n"
    "s_lshl_b32 s93, s90, 10\n"
    "s_lshr_b32 s61, %2, 3\n"
    "s_lshl_b32 s52, %2, 1\n"
    "s_add_u32 s61, s61, s52\n"
    "s_and_b32 s61, s61, 15\n"
    "s_lshl_b32 s61, s61, 14\n"
    "s_add_u32 s61, s61, s51\n"
    "s_add_u32 s60, s64, 1010\n"
    "s_mov_b64 s[64:65], -1\n"
    "s_add_u32 s56, s51, 49152\n"
    "s_add_u32 s58, s61, 49152\n"
    "s_and_b32 s58, s58, 0x3ffff\n"
    "s_add_u32 s57, s51, 98304\n"
    "s_add_u32 s59, s61, 32768\n"
    "s_and_b32 s59, s59, 0x3ffff\n"
    "v_mov_b32 v224, 0\n"
    "v_mov_b32 v226, 0\n"
    "s_mov_b64 exec, 0\n"
    "s_cmp_eq_u32 s44, 0\n"
    "s_cbranch_scc1 Lpg1%=\n"
    "s_cmp_eq_u32 s44, 1\n"
    "s_cbranch_scc1 Lpg2%=\n"
    "s_cmp_eq_u32 s44, 2\n"
    "s_cbranch_scc1 Lpg3%=\n"
    "s_branch Lpg4%=\n"
    "Lwb0%=:\n"
    "s_mov_b64 exec, -1\n"
    "s_add_u32 m0, s51, 131072\n"
    "s_add_u32 s52, s69, s84\n"
    "buffer_load_dwordx4 v128, s[8:11], s52 offen offset:0  lds\n"
    "buffer_load_dwordx4 v128, s[8:11], s52 offen offset:1024  lds\n"
    "s_add_u32 m0, s51, 133120\n"
    "s_add_u32 s52, s69, s85\n"
    "buffer_load_dwordx4 v128, s[8:11], s52 offen offset:0  lds\n"
    "buffer_load_dwordx4 v128, s[8:11], s52 offen offset:1024  lds\n"
    "s_add_u32 m0, s51, 147456\n"
    "s_add_u32 s52, s69, s86\n"
    "buffer_load_dwordx4 v128, s[8:11], s52 offen offset:0  lds\n"
    "buffer_load_dwordx4 v128, s[8:11], s52 offen offset:1024  lds\n"
    "s_add_u32 m0, s51, 149504\n"
    "s_add_u32 s52, s69, s87\n"
    "buffer_load_dwordx4 v128, s[8:11], s52 offen offset:0  lds\n"
    "buffer_load_dwordx4 v128, s[8:11], s52 offen offset:1024  lds\n"
    "s_add_u32 m0, s51, 114688\n"
    "s_nop 0\n"
    "buffer_load_dwordx4 v132, s[12:15], s77 offen offset:0  lds\n"
    "buffer_load_dwordx4 v133, s[12:15], s77 offen offset:1024  lds\n"
    "buffer_load_dwordx4 v134, s[12:15], s77 offen offset:2048  lds\n"
    "buffer_load_dwordx4 v135, s[12:15], s77 offen offset:3072  lds\n"
    "s_lshl_b32 s52, s45, 18\n"
    "s_add_u32 s52, s52, 50331648\n"
    "s_add_u32 s36, s28, s52\n"
    "s_addc_u32 s37, s29, 0\n"
    "s_and_b32 s37, s37, 0xffff\n"
    "s_mov_b32 s38, 147456\n"
    "s_mov_b32 s39, 0x20000\n"
    "s_lshl_b32 s52, s45, 20\n"
    "s_add_u32 s52, s52, 16777216\n"
    "s_add_u32 s32, s28, s52\n"
    "s_addc_u32 s33, s29, 0\n"
    "s_and_b32 s33, s33, 0xffff\n"
    "s_mov_b32 s34, 786432\n"
    "s_mov_b32 s35, 0x20000\n"
    "s_lshl_b32 s52, s45, 21\n"
    "s_add_u32 s28, s28, s52\n"
    "s_addc_u32 s29, s29, 0\n"
    "s_and_b32 s29, s29, 0xffff\n"
    "s_mov_b32 s30, 2097152\n"
    "s_mov_b32 s31, 0x20000\n"
    "s_mov_b64 s[4:5], s[28:29]\n"
    "s_mov_b64 s[6:7], s[30:31]\n"
    "s_and_b32 s25, s25, 0xffff\n"
    "s_mov_b32 s26, 4194304\n"
    "s_mov_b32 s27, 0x20000\n"
    "s_lshl_b32 s52, s45, 18\n"
    "s_add_u32 s40, s40, s52\n"
    "s_addc_u32 s41, s41, 0\n"
    "s_and_b32 s41, s41, 0xffff\n"
    "s_mov_b32 s42, 262144\n"
    "s_mov_b32 s43, 0x20000\n"
    "s_waitcnt vmcnt(12)\n"
    "ds_read_b128 a[128:131], v129 offset:32768\n"
    "ds_read_b128 a[132:135], v129 offset:33280\n"
    "ds_read_b128 a[136:139], v129 offset:34816\n"
    "ds_read_b128 a[140:143], v129 offset:35328\n"
    "ds_read_b128 a[144:147], v129 offset:49152\n"
    "ds_read_b128 a[148:151], v129 offset:49664\n"
    "ds_read_b128 a[152:155], v129 offset:51200\n"
    "ds_read_b128 a[156:159], v129 offset:51712\n"
    "s_waitcnt vmcnt(16) lgkmcnt(0)\n"
    "s_barrier\n"
    "ds_read_b64_tr_b16 a[192:193], v131 offset:32768\n"
    "ds_read_b64_tr_b16 a[194:195], v131 offset:33792\n"
    "ds_read_b64_tr_b16 a[196:197], v131 offset:40960\n"
    "ds_read_b64_tr_b16 a[198:199], v131 offset:41984\n"
    "ds_read_b64_tr_b16 a[200:201], v131 offset:32776\n"
    "ds_read_b64_tr_b16 a[202:203], v131 offset:33800\n"
    "ds_read_b64_tr_b16 a[204:205], v131 offset:40968\n"
    "ds_read_b64_tr_b16 a[206:207], v131 offset:41992\n"
    "s_waitcnt lgkmcnt(6)\n"
    "v_mfma_f32_16x16x32_bf16 v[0:3], a[192:195], a[128:131], 0\n"
    "s_waitcnt lgkmcnt(4)\n"
    "ds_read_b64_tr_b16 a[208:209], v131 offset:33024\n"
    "ds_read_b64_tr_b16 a[210:211], v131 offset:34048\n"
    "v_mfma_f32_16x16x32_bf16 v[0:3], a[196:199], a[132:135], v[0:3]\n"
    "s_add_u32 m0, s51, 32768\n"
    "v_mfma_f32_16x16x32_bf16 v[4:7], a[192:195], a[136:139], 0\n"
    "s_add_u32 s52, s70, s84\n"
    "v_mfma_f32_16x16x32_bf16 v[4:7], a[196:199], a[140:143], v[4:7]\n"
    "buffer_load_dwordx4 v128, s[8:11], s52 offen offset:0  lds\n"
    "v_mfma_f32_16x16x32_bf16 v[8:11], a[192:195], a[144:147], 0\n"
    "ds_read_b64_tr_b16 a[212:213], v131 offset:41216\n"
    "ds_read_b64_tr_b16 a[214:215], v131 offset:42240\n"
    "v_mfma_f32_16x16x32_bf16 v[8:11], a[196:199], a[148:151], v[8:11]\n"
    "v_mfma_f32_16x16x32_bf16 v[12:15], a[192:195], a[152:155], 0\n"
    "v_mfma_f32_16x16x32_bf16 v[12:15], a[196:199], a[156:159], v[12:15]\n"
    "s_waitcnt lgkmcnt(6)\n"
    "v_mfma_f32_16x16x32_bf16 v[16:19], a[200:203], a[128:131], 0\n"
    "s_waitcnt lgkmcnt(4)\n"
    "ds_read_b64_tr_b16 a[216:217], v131 offset:33032\n"
    "ds_read_b64_tr_b16 a[218:219], v131 offset:34056\n"
    "v_mfma_f32_16x16x32_bf16 v[16:19], a[204:207], a[132:135], v[16:19]\n"
    "buffer_load_dwordx4 v128, s[8:11], s52 offen offset:1024  lds\n"
    "v_mfma_f32_16x16x32_bf16 v[20:23], a[200:203], a[136:139], 0\n"
    "v_mfma_f32_16x16x32_bf16 v[20:23], a[204:207], a[140:143], v[20:23]\n"
    "s_add_u32 m0, s51, 34816\n"
    "v_mfma_f32_16x16x32_bf16 v[24:27], a[200:203], a[144:147], 0\n"
    "s_add_u32 s52, s70, s85\n"
    "ds_read_b64_tr_b16 a[220:221], v131 offset:41224\n"
    "ds_read_b64_tr_b16 a[222:223], v131 offset:42248\n"
    "v_mfma_f32_16x16x32_bf16 v[24:27], a[204:207], a[148:151], v[24:27]\n"
    "buffer_load_dwordx4 v128, s[8:11], s52 offen offset:0  lds\n"
    "v_mfma_f32_16x16x32_bf16 v[28:31], a[200:203], a[152:155], 0\n"
    "v_mfma_f32_16x16x32_bf16 v[28:31], a[204:207], a[156:159], v[28:31]\n"
    "s_waitcnt lgkmcnt(6)\n"
    "v_mfma_f32_16x16x32_bf16 v[32:35], a[208:211], a[128:131], 0\n"
    "s_waitcnt lgkmcnt(4)\n"
    "ds_read_b64_tr_b16 a[192:193], v131 offset:33280\n"
    "ds_read_b64_tr_b16 a[194:195], v131 offset:34304\n"
    "v_mfma_f32_16x16x32_bf16 v[32:35], a[212:215], a[132:135], v[32:35]\n"
    "buffer_load_dwordx4 v128, s[8:11], s52 offen offset:1024  lds\n"
    "v_mfma_f32_16x16x32_bf16 v[36:39], a[208:211], a[136:139], 0\n"
    "v_mfma_f32_16x16x32_bf16 v[36:39], a[212:215], a[140:143], v[36:39]\n"
    "s_add_u32 m0, s51, 49152\n"
    "v_mfma_f32_16x16x32_bf16 v[40:43], a[208:211], a[144:147], 0\n"
    "s_add_u32 s52, s70, s86\n"
    "ds_read_b64_tr_b16 a[196:197], v131 offset:41472\n"
    "ds_read_b64_tr_b16 a[198:199], v131 offset:42496\n"
    "v_mfma_f32_16x16x32_bf16 v[40:43], a[212:215], a[148:151], v[40:43]\n"
    "buffer_load_dwordx4 v128, s[8:11], s52 offen offset:0  lds\n"
    "v_mfma_f32_16x16x32_bf16 v[44:47], a[208:211], a[152:155], 0\n"
    "v_mfma_f32_16x16x32_bf16 v[44:47], a[212:215], a[156:159], v[44:47]\n"
    "s_waitcnt lgkmcnt(6)\n"
    "v_mfma_f32_16x16x32_bf16 v[48:51], a[216:219], a[128:131], 0\n"
    "s_waitcnt lgkmcnt(4)\n"
    "ds_read_b64_tr_b16 a[200:201], v131 offset:33288\n"
    "ds_read_b64_tr_b16 a[202:203], v131 offset:34312\n"
    "v_mfma_f32_16x16x32_bf16 v[48:51], a[220:223], a[132:135], v[48:51]\n"
    "buffer_load_dwordx4 v128, s[8:11], s52 offen offset:1024  lds\n"
    "v_mfma_f32_16x16x32_bf16 v[52:55], a[216:219], a[136:139], 0\n"
    "v_mfma_f32_16x16x32_bf16 v[52:55], a[220:223], a[140:143], v[52:55]\n"
    "s_add_u32 m0, s51, 51200\n"
    "v_mfma_f32_16x16x32_bf16 v[56:59], a[216:219], a[144:147], 0\n"
    "s_add_u32 s52, s70, s87\n"
    "ds_read_b64_tr_b16 a[204:205], v131 offset:41480\n"
    "ds_read_b64_tr_b16 a[206:207], v131 offset:42504\n"
    "v_mfma_f32_16x16x32_bf16 v[56:59], a[220:223], a[148:151], v[56:59]\n"
    "buffer_load_dwordx4 v128, s[8:11], s52 offen offset:0  lds\n"
    "v_mfma_f32_16x16x32_bf16 v[60:63], a[216:219], a[152:155], 0\n"
    "v_mfma_f32_16x16x32_bf16 v[60:63], a[220:223], a[156:159], v[60:63]\n"
    "s_waitcnt lgkmcnt(6)\n"
    "v_mfma_f32_16x16x32_bf16 v[64:67], a[192:195], a[128:131], 0\n"
    "s_waitcnt lgkmcnt(4)\n"
    "ds_read_b64_tr_b16 a[208:209], v131 offset:33536\n"
    "ds_read_b64_tr_b16 a[210:211], v131 offset:34560\n"
    "v_mfma_f32_16x16x32_bf16 v[64:67], a[196:199], a[132:135], v[64:67]\n"
    "buffer_load_dwordx4 v128, s[8:11], s52 offen offset:1024  lds\n"
    "v_mfma_f32_16x16x32_bf16 v[68:71], a[192:195], a[136:139], 0\n"
    "v_mfma_f32_16x16x32_bf16 v[68:71], a[196:199], a[140:143], v[68:71]\n"
    "v_mfma_f32_16x16x32_bf16 v[72:75], a[192:195], a[144:147], 0\n"
    "ds_read_b64_tr_b16 a[212:213], v131 offset:41728\n"
    "ds_read_b64_tr_b16 a[214:215], v131 offset:42752\n"
    "v_mfma_f32_16x16x32_bf16 v[72:75], a[196:199], a[148:151], v[72:75]\n"
    "s_mov_b64 exec, 0\n"
    "s_cmp_eq_u32 s44, 0\n"
    "s_cbranch_scc1 Lpg9%=\n"
    "s_cmp_eq_u32 s44, 1\n"
    "s_cbranch_scc1 Lpg10%=\n"
    "s_cmp_eq_u32 s44, 2\n"
    "s_cbranch_scc1 Lpg11%=\n"
    "s_branch Lpg12%=\n"
    "Lwb1%=:\n"
    "s_mov_b64 exec, -1\n"
    "s_waitcnt vmcnt(12)\n"
    "v_mfma_f32_16x16x32_bf16 v[76:79], a[192:195], a[152:155], 0\n"
    "ds_read_b128 a[160:163], v130 offset:0\n"
    "v_mfma_f32_16x16x32_bf16 v[76:79], a[196:199], a[156:159], v[76:79]\n"
    "s_waitcnt lgkmcnt(7)\n"
    "ds_read_b128 a[164:167], v130 offset:512\n"
    "v_mfma_f32_16x16x32_bf16 v[80:83], a[200:203], a[128:131], 0\n"
    "s_waitcnt lgkmcnt(6)\n"
    "ds_read_b64_tr_b16 a[216:217], v131 offset:33544\n"
    "ds_read_b64_tr_b16 a[218:219], v131 offset:34568\n"
    "v_mfma_f32_16x16x32_bf16 v[80:83], a[204:207], a[132:135], v[80:83]\n"
    "ds_read_b128 a[168:171], v130 offset:2048\n"
    "v_mfma_f32_16x16x32_bf16 v[84:87], a[200:203], a[136:139], 0\n"
    "ds_read_b128 a[172:175], v130 offset:2560\n"
    "v_mfma_f32_16x16x32_bf16 v[84:87], a[204:207], a[140:143], v[84:87]\n"
    "ds_read_b128 a[176:179], v130 offset:16384\n"
    "v_mfma_f32_16x16x32_bf16 v[88:91], a[200:203], a[144:147], 0\n"
    "ds_read_b64_tr_b16 a[220:221], v131 offset:41736\n"
    "ds_read_b64_tr_b16 a[222:223], v131 offset:42760\n"
    "v_mfma_f32_16x16x32_bf16 v[88:91], a[204:207], a[148:151], v[88:91]\n"
    "ds_read_b128 a[180:183], v130 offset:16896\n"
    "v_mfma_f32_16x16x32_bf16 v[92:95], a[200:203], a[152:155], 0\n"
    "s_add_u32 m0, s51, 81920\n"
    "ds_read_b128 a[184:187], v130 offset:18432\n"
    "v_mfma_f32_16x16x32_bf16 v[92:95], a[204:207], a[156:159], v[92:95]\n"
    "s_waitcnt lgkmcnt(13)\n"
    "ds_read_b128 a[188:191], v130 offset:18944\n"
    "v_mfma_f32_16x16x32_bf16 v[96:99], a[208:211], a[128:131], 0\n"
    "s_waitcnt lgkmcnt(12)\n"
    "buffer_load_dwordx4 v132, s[12:15], s78 offen offset:0  lds\n"
    "v_mfma_f32_16x16x32_bf16 v[96:99], a[212:215], a[132:135], v[96:99]\n"
    "v_mfma_f32_16x16x32_bf16 v[100:103], a[208:211], a[136:139], 0\n"
    "v_mfma_f32_16x16x32_bf16 v[100:103], a[212:215], a[140:143], v[100:103]\n"
    "buffer_load_dwordx4 v133, s[12:15], s78 offen offset:1024  lds\n"
    "v_mfma_f32_16x16x32_bf16 v[104:107], a[208:211], a[144:147], 0\n"
    "v_mfma_f32_16x16x32_bf16 v[104:107], a[212:215], a[148:151], v[104:107]\n"
    "s_waitcnt vmcnt(10) lgkmcnt(0)\n"
    "v_mfma_f32_16x16x32_bf16 v[108:111], a[208:211], a[152:155], 0\n"
    "s_barrier\n"
    "ds_read_b64_tr_b16 a[192:193], v131 offset:49152\n"
    "ds_read_b64_tr_b16 a[194:195], v131 offset:50176\n"
    "v_mfma_f32_16x16x32_bf16 v[108:111], a[212:215], a[156:159], v[108:111]\n"
    "buffer_load_dwordx4 v134, s[12:15], s78 offen offset:2048  lds\n"
    "v_mfma_f32_16x16x32_bf16 v[112:115], a[216:219], a[128:131], 0\n"
    "ds_read_b64_tr_b16 a[196:197], v131 offset:57344\n"
    "ds_read_b64_tr_b16 a[198:199], v131 offset:58368\n"
    "v_mfma_f32_16x16x32_bf16 v[112:115], a[220:223], a[132:135], v[112:115]\n"
    "v_mfma_f32_16x16x32_bf16 v[116:119], a[216:219], a[136:139], 0\n"
    "v_mfma_f32_16x16x32_bf16 v[116:119], a[220:223], a[140:143], v[116:119]\n"
    "ds_read_b64_tr_b16 a[200:201], v131 offset:49160\n"
    "ds_read_b64_tr_b16 a[202:203], v131 offset:50184\n"
    "v_mfma_f32_16x16x32_bf16 v[120:123], a[216:219], a[144:147], 0\n"
    "buffer_load_dwordx4 v135, s[12:15], s78 offen offset:3072  lds\n"
    "v_mfma_f32_16x16x32_bf16 v[120:123], a[220:223], a[148:151], v[120:123]\n"
    "v_mfma_f32_16x16x32_bf16 v[124:127], a[216:219], a[152:155], 0\n"
    "ds_read_b64_tr_b16 a[204:205], v131 offset:57352\n"
    "ds_read_b64_tr_b16 a[206:207], v131 offset:58376\n"
    "v_mfma_f32_16x16x32_bf16 v[124:127], a[220:223], a[156:159], v[124:127]\n"
    "s_waitcnt lgkmcnt(6)\n"
    "v_mfma_f32_16x16x32_bf16 v[0:3], a[192:195], a[160:163], v[0:3]\n"
    "s_waitcnt lgkmcnt(4)\n"
    "ds_read_b64_tr_b16 a[208:209], v131 offset:49408\n"
    "ds_read_b64_tr_b16 a[210:211], v131 offset:50432\n"
    "v_mfma_f32_16x16x32_bf16 v[0:3], a[196:199], a[164:167], v[0:3]\n"
    "s_add_u32 m0, s51, 131072\n"
    "v_mfma_f32_16x16x32_bf16 v[4:7], a[192:195], a[168:171], v[4:7]\n"
    "s_add_u32 s52, s71, s84\n"
    "v_mfma_f32_16x16x32_bf16 v[4:7], a[196:199], a[172:175], v[4:7]\n"
    "buffer_load_dwordx4 v128, s[8:11], s52 offen offset:0  lds\n"
    "v_mfma_f32_16x16x32_bf16 v[8:11], a[192:195], a[176:179], v[8:11]\n"
    "ds_read_b64_tr_b16 a[212:213], v131 offset:57600\n"
    "ds_read_b64_tr_b16 a[214:215], v131 offset:58624\n"
    "v_mfma_f32_16x16x32_bf16 v[8:11], a[196:199], a[180:183], v[8:11]\n"
    "v_mfma_f32_16x16x32_bf16 v[12:15], a[192:195], a[184:187], v[12:15]\n"
    "v_mfma_f32_16x16x32_bf16 v[12:15], a[196:199], a[188:191], v[12:15]\n"
    "s_waitcnt lgkmcnt(6)\n"
    "v_mfma_f32_16x16x32_bf16 v[16:19], a[200:203], a[160:163], v[16:19]\n"
    "s_waitcnt lgkmcnt(4)\n"
    "ds_read_b64_tr_b16 a[216:217], v131 offset:49416\n"
    "ds_read_b64_tr_b16 a[218:219], v131 offset:50440\n"
    "v_mfma_f32_16x16x32_bf16 v[16:19], a[204:207], a[164:167], v[16:19]\n"
    "buffer_load_dwordx4 v128, s[8:11], s52 offen offset:1024  lds\n"
    "v_mfma_f32_16x16x32_bf16 v[20:23], a[200:203], a[168:171], v[20:23]\n"
    "v_mfma_f32_16x16x32_bf16 v[20:23], a[204:207], a[172:175], v[20:23]\n"
    "s_add_u32 m0, s51, 133120\n"
    "v_mfma_f32_16x16x32_bf16 v[24:27], a[200:203], a[176:179], v[24:27]\n"
    "s_add_u32 s52, s71, s85\n"
    "ds_read_b64_tr_b16 a[220:221], v131 offset:57608\n"
    "ds_read_b64_tr_b16 a[222:223], v131 offset:58632\n"
    "v_mfma_f32_16x16x32_bf16 v[24:27], a[204:207], a[180:183], v[24:27]\n"
    "buffer_load_dwordx4 v128, s[8:11], s52 offen offset:0  lds\n"
    "v_mfma_f32_16x16x32_bf16 v[28:31], a[200:203], a[184:187], v[28:31]\n"
    "v_mfma_f32_16x16x32_bf16 v[28:31], a[204:207], a[188:191], v[28:31]\n"
    "s_waitcnt lgkmcnt(6)\n"
    "v_mfma_f32_16x16x32_bf16 v[32:35], a[208:211], a[160:163], v[32:35]\n"
    "s_waitcnt lgkmcnt(4)\n"
    "ds_read_b64_tr_b16 a[192:193], v131 offset:49664\n"
    "ds_read_b64_tr_b16 a[194:195], v131 offset:50688\n"
    "v_mfma_f32_16x16x32_bf16 v[32:35], a[212:215], a[164:167], v[32:35]\n"
    "buffer_load_dwordx4 v128, s[8:11], s52 offen offset:1024  lds\n"
    "v_mfma_f32_16x16x32_bf16 v[36:39], a[208:211], a[168:171], v[36:39]\n"
    "v_mfma_f32_16x16x32_bf16 v[36:39], a[212:215], a[172:175], v[36:39]\n"
    "s_add_u32 m0, s51, 147456\n"
    "v_mfma_f32_16x16x32_bf16 v[40:43], a[208:211], a[176:179], v[40:43]\n"
    "s_add_u32 s52, s71, s86\n"
    "ds_read_b64_tr_b16 a[196:197], v131 offset:57856\n"
    "ds_read_b64_tr_b16 a[198:199], v131 offset:58880\n"
    "v_mfma_f32_16x16x32_bf16 v[40:43], a[212:215], a[180:183], v[40:43]\n"
    "buffer_load_dwordx4 v128, s[8:11], s52 offen offset:0  lds\n"
    "v_mfma_f32_16x16x32_bf16 v[44:47], a[208:211], a[184:187], v[44:47]\n"
    "v_mfma_f32_16x16x32_bf16 v[44:47], a[212:215], a[188:191], v[44:47]\n"
    "s_waitcnt lgkmcnt(6)\n"
    "v_mfma_f32_16x16x32_bf16 v[48:51], a[216:219], a[160:163], v[48:51]\n"
    "s_waitcnt lgkmcnt(4)\n"
    "ds_read_b64_tr_b16 a[200:201], v131 offset:49672\n"
    "ds_read_b64_tr_b16 a[202:203], v131 offset:50696\n"
    "v_mfma_f32_16x16x32_bf16 v[48:51], a[220:223], a[164:167], v[48:51]\n"
    "buffer_load_dwordx4 v128, s[8:11], s52 offen offset:1024  lds\n"
    "v_mfma_f32_16x16x32_bf16 v[52:55], a[216:219], a[168:171], v[52:55]\n"
    "v_mfma_f32_16x16x32_bf16 v[52:55], a[220:223], a[172:175], v[52:55]\n"
    "s_add_u32 m0, s51, 149504\n"
    "v_mfma_f32_16x16x32_bf16 v[56:59], a[216:219], a[176:179], v[56:59]\n"
    "s_add_u32 s52, s71, s87\n"
    "ds_read_b64_tr_b16 a[204:205], v131 offset:57864\n"
    "ds_read_b64_tr_b16 a[206:207], v131 offset:58888\n"
    "v_mfma_f32_16x16x32_bf16 v[56:59], a[220:223], a[180:183], v[56:59]\n"
    "buffer_load_dwordx4 v128, s[8:11], s52 offen offset:0  lds\n"
    "v_mfma_f32_16x16x32_bf16 v[60:63], a[216:219], a[184:187], v[60:63]\n"
    "v_mfma_f32_16x16x32_bf16 v[60:63], a[220:223], a[188:191], v[60:63]\n"
    "s_waitcnt lgkmcnt(6)\n"
    "v_mfma_f32_16x16x32_bf16 v[64:67], a[192:195], a[160:163], v[64:67]\n"
    "s_waitcnt lgkmcnt(4)\n"
    "ds_read_b64_tr_b16 a[208:209], v131 offset:49920\n"
    "ds_read_b64_tr_b16 a[210:211], v131 offset:50944\n"
    "v_mfma_f32_16x16x32_bf16 v[64:67], a[196:199], a[164:167], v[64:67]\n"
    "buffer_load_dwordx4 v128, s[8:11], s52 offen offset:1024  lds\n"
    "v_mfma_f32_16x16x32_bf16 v[68:71], a[192:195], a[168:171], v[68:71]\n"
    "v_mfma_f32_16x16x32_bf16 v[68:71], a[196:199], a[172:175], v[68:71]\n"
    "v_mfma_f32_16x16x32_bf16 v[72:75], a[192:195], a[176:179], v[72:75]\n"
    "ds_read_b64_tr_b16 a[212:213], v131 offset:58112\n"
    "ds_read_b64_tr_b16 a[214:215], v131 offset:59136\n"
    "v_mfma_f32_16x16x32_bf16 v[72:75], a[196:199], a[180:183], v[72:75]\n"
    "s_mov_b64 exec, 0\n"
    "s_cmp_eq_u32 s44, 0\n"
    "s_cbranch_scc1 Lpg13%=\n"
    "s_cmp_eq_u32 s44, 1\n"
    "s_cbranch_scc1 Lpg14%=\n"
    "s_cmp_eq_u32 s44, 2\n"
    "s_cbranch_scc1 Lpg15%=\n"
    "s_branch Lwb2%=\n"
    "Lwb2%=:\n"
    "s_mov_b64 exec, -1\n"
    "s_waitcnt vmcnt(12)\n"
    "v_mfma_f32_16x16x32_bf16 v[76:79], a[192:195], a[184:187], v[76:79]\n"
    "ds_read_b128 a[128:131], v129 offset:32768\n"
    "v_mfma_f32_16x16x32_bf16 v[76:79], a[196:199], a[188:191], v[76:79]\n"
    "s_waitcnt lgkmcnt(7)\n"
    "ds_read_b128 a[132:135], v129 offset:33280\n"
    "v_mfma_f32_16x16x32_bf16 v[80:83], a[200:203], a[160:163], v[80:83]\n"
    "s_waitcnt lgkmcnt(6)\n"
    "ds_read_b64_tr_b16 a[216:217], v131 offset:49928\n"
    "ds_read_b64_tr_b16 a[218:219], v131 offset:50952\n"
    "v_mfma_f32_16x16x32_bf16 v[80:83], a[204:207], a[164:167], v[80:83]\n"
    "ds_read_b128 a[136:139], v129 offset:34816\n"
    "v_mfma_f32_16x16x32_bf16 v[84:87], a[200:203], a[168:171], v[84:87]\n"
    "ds_read_b128 a[140:143], v129 offset:35328\n"
    "v_mfma_f32_16x16x32_bf16 v[84:87], a[204:207], a[172:175], v[84:87]\n"
    "ds_read_b128 a[144:147], v129 offset:49152\n"
    "v_mfma_f32_16x16x32_bf16 v[88:91], a[200:203], a[176:179], v[88:91]\n"
    "ds_read_b64_tr_b16 a[220:221], v131 offset:58120\n"
    "ds_read_b64_tr_b16 a[222:223], v131 offset:59144\n"
    "v_mfma_f32_16x16x32_bf16 v[88:91], a[204:207], a[180:183], v[88:91]\n"
    "ds_read_b128 a[148:151], v129 offset:49664\n"
    "v_mfma_f32_16x16x32_bf16 v[92:95], a[200:203], a[184:187], v[92:95]\n"
    "Lpg1%=:\n"
    "s_cbranch_execz Lpg5%=\n"
    "s_add_u32 m0, s51, 98304\n"
    "ds_read_b128 a[152:155], v129 offset:51200\n"
    "v_mfma_f32_16x16x32_bf16 v[92:95], a[204:207], a[188:191], v[92:95]\n"
    "s_waitcnt lgkmcnt(13)\n"
    "ds_read_b128 a[156:159], v129 offset:51712\n"
    "v_mfma_f32_16x16x32_bf16 v[96:99], a[208:211], a[160:163], v[96:99]\n"
    "s_waitcnt lgkmcnt(12)\n"
    "buffer_load_dwordx4 v132, s[12:15], s79 offen offset:0  lds\n"
    "v_mfma_f32_16x16x32_bf16 v[96:99], a[212:215], a[164:167], v[96:99]\n"
    "v_mfma_f32_16x16x32_bf16 v[100:103], a[208:211], a[168:171], v[100:103]\n"
    "v_mfma_f32_16x16x32_bf16 v[100:103], a[212:215], a[172:175], v[100:103]\n"
    "buffer_load_dwordx4 v133, s[12:15], s79 offen offset:1024  lds\n"
    "v_mfma_f32_16x16x32_bf16 v[104:107], a[208:211], a[176:179], v[104:107]\n"
    "v_mfma_f32_16x16x32_bf16 v[104:107], a[212:215], a[180:183], v[104:107]\n"
    "s_waitcnt vmcnt(10) lgkmcnt(0)\n"
    "v_mfma_f32_16x16x32_bf16 v[108:111], a[208:211], a[184:187], v[108:111]\n"
    "s_barrier\n"
    "ds_read_b64_tr_b16 a[192:193], v131 offset:16384\n"
    "ds_read_b64_tr_b16 a[194:195], v131 offset:17408\n"
    "v_mfma_f32_16x16x32_bf16 v[108:111], a[212:215], a[188:191], v[108:111]\n"
    "buffer_load_dwordx4 v134, s[12:15], s79 offen offset:2048  lds\n"
    "v_mfma_f32_16x16x32_bf16 v[112:115], a[216:219], a[160:163], v[112:115]\n"
    "ds_read_b64_tr_b16 a[196:197], v131 offset:24576\n"
    "ds_read_b64_tr_b16 a[198:199], v131 offset:25600\n"
    "v_mfma_f32_16x16x32_bf16 v[112:115], a[220:223], a[164:167], v[112:115]\n"
    "v_mfma_f32_16x16x32_bf16 v[116:119], a[216:219], a[168:171], v[116:119]\n"
    "v_mfma_f32_16x16x32_bf16 v[116:119], a[220:223], a[172:175], v[116:119]\n"
    "ds_read_b64_tr_b16 a[200:201], v131 offset:16392\n"
    "ds_read_b64_tr_b16 a[202:203], v131 offset:17416\n"
    "v_mfma_f32_16x16x32_bf16 v[120:123], a[216:219], a[176:179], v[120:123]\n"
    "buffer_load_dwordx4 v135, s[12:15], s79 offen offset:3072  lds\n"
    "v_mfma_f32_16x16x32_bf16 v[120:123], a[220:223], a[180:183], v[120:123]\n"
    "v_mfma_f32_16x16x32_bf16 v[124:127], a[216:219], a[184:187], v[124:127]\n"
    "ds_read_b64_tr_b16 a[204:205], v131 offset:24584\n"
    "ds_read_b64_tr_b16 a[206:207], v131 offset:25608\n"
    "v_mfma_f32_16x16x32_bf16 v[124:127], a[220:223], a[188:191], v[124:127]\n"
    "s_waitcnt lgkmcnt(6)\n"
    "v_mfma_f32_16x16x32_bf16 v[0:3], a[192:195], a[128:131], v[0:3]\n"
    "s_waitcnt lgkmcnt(4)\n"
    "ds_read_b64_tr_b16 a[208:209], v131 offset:16640\n"
    "ds_read_b64_tr_b16 a[210:211], v131 offset:17664\n"
    "v_mfma_f32_16x16x32_bf16 v[0:3], a[196:199], a[132:135], v[0:3]\n"
    "s_add_u32 m0, s51, 32768\n"
    "v_mfma_f32_16x16x32_bf16 v[4:7], a[192:195], a[136:139], v[4:7]\n"
    "s_add_u32 s52, s72, s84\n"
    "v_mfma_f32_16x16x32_bf16 v[4:7], a[196:199], a[140:143], v[4:7]\n"
    "buffer_load_dwordx4 v128, s[8:11], s52 offen offset:0  lds\n"
    "v_mfma_f32_16x16x32_bf16 v[8:11], a[192:195], a[144:147], v[8:11]\n"
    "ds_read_b64_tr_b16 a[212:213], v131 offset:24832\n"
    "ds_read_b64_tr_b16 a[214:215], v131 offset:25856\n"
    "v_mfma_f32_16x16x32_bf16 v[8:11], a[196:199], a[148:151], v[8:11]\n"
    "v_mfma_f32_16x16x32_bf16 v[12:15], a[192:195], a[152:155], v[12:15]\n"
    "v_mfma_f32_16x16x32_bf16 v[12:15], a[196:199], a[156:159], v[12:15]\n"
    "s_waitcnt lgkmcnt(6)\n"
    "buffer_load_dwordx4 v128, s[8:11], s52 offen offset:1024  lds\n"
    "v_mfma_f32_16x16x32_bf16 v[16:19], a[200:203], a[128:131], v[16:19]\n"
    "s_waitcnt lgkmcnt(4)\n"
    "ds_read_b64_tr_b16 a[216:217], v131 offset:16648\n"
    "ds_read_b64_tr_b16 a[218:219], v131 offset:17672\n"
    "v_mfma_f32_16x16x32_bf16 v[16:19], a[204:207], a[132:135], v[16:19]\n"
    "s_add_u32 m0, s51, 34816\n"
    "v_mfma_f32_16x16x32_bf16 v[20:23], a[200:203], a[136:139], v[20:23]\n"
    "s_add_u32 s52, s72, s85\n"
    "v_mfma_f32_16x16x32_bf16 v[20:23], a[204:207], a[140:143], v[20:23]\n"
    "buffer_load_dwordx4 v128, s[8:11], s52 offen offset:0  lds\n"
    "v_mfma_f32_16x16x32_bf16 v[24:27], a[200:203], a[144:147], v[24:27]\n"
    "ds_read_b64_tr_b16 a[220:221], v131 offset:24840\n"
    "ds_read_b64_tr_b16 a[222:223], v131 offset:25864\n"
    "v_mfma_f32_16x16x32_bf16 v[24:27], a[204:207], a[148:151], v[24:27]\n"
    "v_mfma_f32_16x16x32_bf16 v[28:31], a[200:203], a[152:155], v[28:31]\n"
    "v_mfma_f32_16x16x32_bf16 v[28:31], a[204:207], a[156:159], v[28:31]\n"
    "s_waitcnt lgkmcnt(6)\n"
    "buffer_load_dwordx4 v128, s[8:11], s52 offen offset:1024  lds\n"
    "v_mfma_f32_16x16x32_bf16 v[32:35], a[208:211], a[128:131], v[32:35]\n"
    "s_waitcnt lgkmcnt(4)\n"
    "ds_read_b64_tr_b16 a[192:193], v131 offset:16896\n"
    "ds_read_b64_tr_b16 a[194:195], v131 offset:17920\n"
    "v_mfma_f32_16x16x32_bf16 v[32:35], a[212:215], a[132:135], v[32:35]\n"
    "s_add_u32 m0, s51, 49152\n"
    "s_add_u32 s52, s72, s86\n"
    "v_mfma_f32_16x16x32_bf16 v[36:39], a[208:211], a[136:139], v[36:39]\n"
    "buffer_load_dwordx4 v128, s[8:11], s52 offen offset:0  lds\n"
    "v_mfma_f32_16x16x32_bf16 v[36:39], a[212:215], a[140:143], v[36:39]\n"
    "v_mfma_f32_16x16x32_bf16 v[40:43], a[208:211], a[144:147], v[40:43]\n"
    "ds_read_b64_tr_b16 a[196:197], v131 offset:25088\n"
    "ds_read_b64_tr_b16 a[198:199], v131 offset:26112\n"
    "v_mfma_f32_16x16x32_bf16 v[40:43], a[212:215], a[148:151], v[40:43]\n"
    "v_mfma_f32_16x16x32_bf16 v[44:47], a[208:211], a[152:155], v[44:47]\n"
    "buffer_load_dwordx4 v128, s[8:11], s52 offen offset:1024  lds\n"
    "v_mfma_f32_16x16x32_bf16 v[44:47], a[212:215], a[156:159], v[44:47]\n"
    "s_waitcnt lgkmcnt(6)\n"
    "v_mfma_f32_16x16x32_bf16 v[48:51], a[216:219], a[128:131], v[48:51]\n"
    "s_waitcnt lgkmcnt(4)\n"
    "ds_read_b64_tr_b16 a[200:201], v131 offset:16904\n"
    "ds_read_b64_tr_b16 a[202:203], v131 offset:17928\n"
    "v_mfma_f32_16x16x32_bf16 v[48:51], a[220:223], a[132:135], v[48:51]\n"
    "s_add_u32 m0, s51, 51200\n"
    "s_add_u32 s52, s72, s87\n"
    "v_mfma_f32_16x16x32_bf16 v[52:55], a[216:219], a[136:139], v[52:55]\n"
    "buffer_load_dwordx4 v128, s[8:11], s52 offen offset:0  lds\n"
    "v_mfma_f32_16x16x32_bf16 v[52:55], a[220:223], a[140:143], v[52:55]\n"
    "v_mfma_f32_16x16x32_bf16 v[56:59], a[216:219], a[144:147], v[56:59]\n"
    "ds_read_b64_tr_b16 a[204:205], v131 offset:25096\n"
    "ds_read_b64_tr_b16 a[206:207], v131 offset:26120\n"
    "v_mfma_f32_16x16x32_bf16 v[56:59], a[220:223], a[148:151], v[56:59]\n"
    "buffer_load_dwordx4 v128, s[8:11], s52 offen offset:1024  lds\n"
    "v_mfma_f32_16x16x32_bf16 v[60:63], a[216:219], a[152:155], v[60:63]\n"
    "s_add_u32 m0, s51, 114688\n"
    "v_mfma_f32_16x16x32_bf16 v[60:63], a[220:223], a[156:159], v[60:63]\n"
    "s_waitcnt lgkmcnt(6)\n"
    "v_mfma_f32_16x16x32_bf16 v[64:67], a[192:195], a[128:131], v[64:67]\n"
    "s_waitcnt lgkmcnt(4)\n"
    "ds_read_b64_tr_b16 a[208:209], v131 offset:17152\n"
    "ds_read_b64_tr_b16 a[210:211], v131 offset:18176\n"
    "v_mfma_f32_16x16x32_bf16 v[64:67], a[196:199], a[132:135], v[64:67]\n"
    "buffer_load_dwordx4 v132, s[12:15], s80 offen offset:0  lds\n"
    "v_mfma_f32_16x16x32_bf16 v[68:71], a[192:195], a[136:139], v[68:71]\n"
    "v_mfma_f32_16x16x32_bf16 v[68:71], a[196:199], a[140:143], v[68:71]\n"
    "v_mfma_f32_16x16x32_bf16 v[72:75], a[192:195], a[144:147], v[72:75]\n"
    "ds_read_b64_tr_b16 a[212:213], v131 offset:25344\n"
    "ds_read_b64_tr_b16 a[214:215], v131 offset:26368\n"
    "v_mfma_f32_16x16x32_bf16 v[72:75], a[196:199], a[148:151], v[72:75]\n"
    "s_waitcnt vmcnt(13)\n"
    "buffer_load_dwordx4 v133, s[12:15], s80 offen offset:1024  lds\n"
    "v_mfma_f32_16x16x32_bf16 v[76:79], a[192:195], a[152:155], v[76:79]\n"
    "ds_read_b128 a[160:163], v130 offset:0\n"
    "v_mfma_f32_16x16x32_bf16 v[76:79], a[196:199], a[156:159], v[76:79]\n"
    "s_waitcnt lgkmcnt(7)\n"
    "ds_read_b128 a[164:167], v130 offset:512\n"
    "v_mfma_f32_16x16x32_bf16 v[80:83], a[200:203], a[128:131], v[80:83]\n"
    "s_waitcnt lgkmcnt(6)\n"
    "ds_read_b64_tr_b16 a[216:217], v131 offset:17160\n"
    "ds_read_b64_tr_b16 a[218:219], v131 offset:18184\n"
    "v_mfma_f32_16x16x32_bf16 v[80:83], a[204:207], a[132:135], v[80:83]\n"
    "ds_read_b128 a[168:171], v130 offset:2048\n"
    "v_mfma_f32_16x16x32_bf16 v[84:87], a[200:203], a[136:139], v[84:87]\n"
    "ds_read_b128 a[172:175], v130 offset:2560\n"
    "v_mfma_f32_16x16x32_bf16 v[84:87], a[204:207], a[140:143], v[84:87]\n"
    "ds_read_b128 a[176:179], v130 offset:16384\n"
    "v_mfma_f32_16x16x32_bf16 v[88:91], a[200:203], a[144:147], v[88:91]\n"
    "ds_read_b64_tr_b16 a[220:221], v131 offset:25352\n"
    "ds_read_b64_tr_b16 a[222:223], v131 offset:26376\n"
    "v_mfma_f32_16x16x32_bf16 v[88:91], a[204:207], a[148:151], v[88:91]\n"
    "ds_read_b128 a[180:183], v130 offset:16896\n"
    "v_mfma_f32_16x16x32_bf16 v[92:95], a[200:203], a[152:155], v[92:95]\n"
    "ds_read_b128 a[184:187], v130 offset:18432\n"
    "v_mfma_f32_16x16x32_bf16 v[92:95], a[204:207], a[156:159], v[92:95]\n"
    "s_waitcnt lgkmcnt(13)\n"
    "ds_read_b128 a[188:191], v130 offset:18944\n"
    "v_mfma_f32_16x16x32_bf16 v[96:99], a[208:211], a[128:131], v[96:99]\n"
    "s_waitcnt lgkmcnt(12)\n"
    "v_mfma_f32_16x16x32_bf16 v[96:99], a[212:215], a[132:135], v[96:99]\n"
    "v_mfma_f32_16x16x32_bf16 v[100:103], a[208:211], a[136:139], v[100:103]\n"
    "buffer_load_dwordx4 v134, s[12:15], s80 offen offset:2048  lds\n"
    "v_mfma_f32_16x16x32_bf16 v[100:103], a[212:215], a[140:143], v[100:103]\n"
    "v_mfma_f32_16x16x32_bf16 v[104:107], a[208:211], a[144:147], v[104:107]\n"
    "buffer_load_dwordx4 v135, s[12:15], s80 offen offset:3072  lds\n"
    "v_mfma_f32_16x16x32_bf16 v[104:107], a[212:215], a[148:151], v[104:107]\n"
    "s_waitcnt vmcnt(12) lgkmcnt(0)\n"
    "v_mfma_f32_16x16x32_bf16 v[108:111], a[208:211], a[152:155], v[108:111]\n"
    "s_barrier\n"
    "ds_read_b64_tr_b16 a[192:193], v131 offset:32768\n"
    "ds_read_b64_tr_b16 a[194:195], v131 offset:33792\n"
    "v_mfma_f32_16x16x32_bf16 v[108:111], a[212:215], a[156:159], v[108:111]\n"
    "v_mfma_f32_16x16x32_bf16 v[112:115], a[216:219], a[128:131], v[112:115]\n"
    "ds_read_b64_tr_b16 a[196:197], v131 offset:40960\n"
    "ds_read_b64_tr_b16 a[198:199], v131 offset:41984\n"
    "v_mfma_f32_16x16x32_bf16 v[112:115], a[220:223], a[132:135], v[112:115]\n"
    "s_add_u32 m0, s51, 131072\n"
    "s_add_u32 s52, s73, s84\n"
    "v_mfma_f32_16x16x32_bf16 v[116:119], a[216:219], a[136:139], v[116:119]\n"
    "buffer_load_dwordx4 v128, s[8:11], s52 offen offset:0  lds\n"
    "v_mfma_f32_16x16x32_bf16 v[116:119], a[220:223], a[140:143], v[116:119]\n"
    "ds_read_b64_tr_b16 a[200:201], v131 offset:32776\n"
    "ds_read_b64_tr_b16 a[202:203], v131 offset:33800\n"
    "v_mfma_f32_16x16x32_bf16 v[120:123], a[216:219], a[144:147], v[120:123]\n"
    "v_mfma_f32_16x16x32_bf16 v[120:123], a[220:223], a[148:151], v[120:123]\n"
    "buffer_load_dwordx4 v128, s[8:11], s52 offen offset:1024  lds\n"
    "v_mfma_f32_16x16x32_bf16 v[124:127], a[216:219], a[152:155], v[124:127]\n"
    "ds_read_b64_tr_b16 a[204:205], v131 offset:40968\n"
    "ds_read_b64_tr_b16 a[206:207], v131 offset:41992\n"
    "v_mfma_f32_16x16x32_bf16 v[124:127], a[220:223], a[156:159], v[124:127]\n"
    "s_waitcnt lgkmcnt(6)\n"
    "v_mfma_f32_16x16x32_bf16 v[0:3], a[192:195], a[160:163], v[0:3]\n"
    "s_waitcnt lgkmcnt(4)\n"
    "ds_read_b64_tr_b16 a[208:209], v131 offset:33024\n"
    "ds_read_b64_tr_b16 a[210:211], v131 offset:34048\n"
    "v_mfma_f32_16x16x32_bf16 v[0:3], a[196:199], a[164:167], v[0:3]\n"
    "s_add_u32 m0, s51, 133120\n"
    "v_mfma_f32_16x16x32_bf16 v[4:7], a[192:195], a[168:171], v[4:7]\n"
    "s_add_u32 s52, s73, s85\n"
    "v_mfma_f32_16x16x32_bf16 v[4:7], a[196:199], a[172:175], v[4:7]\n"
    "buffer_load_dwordx4 v128, s[8:11], s52 offen offset:0  lds\n"
    "v_mfma_f32_16x16x32_bf16 v[8:11], a[192:195], a[176:179], v[8:11]\n"
    "ds_read_b64_tr_b16 a[212:213], v131 offset:41216\n"
    "ds_read_b64_tr_b16 a[214:215], v131 offset:42240\n"
    "v_mfma_f32_16x16x32_bf16 v[8:11], a[196:199], a[180:183], v[8:11]\n"
    "v_mfma_f32_16x16x32_bf16 v[12:15], a[192:195], a[184:187], v[12:15]\n"
    "v_mfma_f32_16x16x32_bf16 v[12:15], a[196:199], a[188:191], v[12:15]\n"
    "s_waitcnt lgkmcnt(6)\n"
    "buffer_load_dwordx4 v128, s[8:11], s52 offen offset:1024  lds\n"
    "v_mfma_f32_16x16x32_bf16 v[16:19], a[200:203], a[160:163], v[16:19]\n"
    "s_waitcnt lgkmcnt(4)\n"
    "ds_read_b64_tr_b16 a[216:217], v131 offset:33032\n"
    "ds_read_b64_tr_b16 a[218:219], v131 offset:34056\n"
    "v_mfma_f32_16x16x32_bf16 v[16:19], a[204:207], a[164:167], v[16:19]\n"
    "s_add_u32 m0, s51, 147456\n"
    "s_add_u32 s52, s73, s86\n"
    "v_mfma_f32_16x16x32_bf16 v[20:23], a[200:203], a[168:171], v[20:23]\n"
    "buffer_load_dwordx4 v128, s[8:11], s52 offen offset:0  lds\n"
    "v_mfma_f32_16x16x32_bf16 v[20:23], a[204:207], a[172:175], v[20:23]\n"
    "v_mfma_f32_16x16x32_bf16 v[24:27], a[200:203], a[176:179], v[24:27]\n"
    "ds_read_b64_tr_b16 a[220:221], v131 offset:41224\n"
    "ds_read_b64_tr_b16 a[222:223], v131 offset:42248\n"
    "v_mfma_f32_16x16x32_bf16 v[24:27], a[204:207], a[180:183], v[24:27]\n"
    "v_mfma_f32_16x16x32_bf16 v[28:31], a[200:203], a[184:187], v[28:31]\n"
    "buffer_load_dwordx4 v128, s[8:11], s52 offen offset:1024  lds\n"
    "v_mfma_f32_16x16x32_bf16 v[28:31], a[204:207], a[188:191], v[28:31]\n"
    "s_waitcnt lgkmcnt(6)\n"
    "v_mfma_f32_16x16x32_bf16 v[32:35], a[208:211], a[160:163], v[32:35]\n"
    "s_add_u32 m0, s51, 149504\n"
    "s_add_u32 s52, s73, s87\n"
    "ds_read_b64_tr_b16 a[192:193], v131 offset:33280\n"
    "ds_read_b64_tr_b16 a[194:195], v131 offset:34304\n"
    "s_waitcnt lgkmcnt(6)\n"
    "v_mfma_f32_16x16x32_bf16 v[32:35], a[212:215], a[164:167], v[32:35]\n"
    "buffer_load_dwordx4 v128, s[8:11], s52 offen offset:0  lds\n"
    "v_mfma_f32_16x16x32_bf16 v[36:39], a[208:211], a[168:171], v[36:39]\n"
    "v_mfma_f32_16x16x32_bf16 v[36:39], a[212:215], a[172:175], v[36:39]\n"
    "buffer_load_dwordx4 v128, s[8:11], s52 offen offset:1024  lds\n"
    "v_mfma_f32_16x16x32_bf16 v[40:43], a[208:211], a[176:179], v[40:43]\n"
    "ds_read_b64_tr_b16 a[196:197], v131 offset:41472\n"
    "ds_read_b64_tr_b16 a[198:199], v131 offset:42496\n"
    "v_mfma_f32_16x16x32_bf16 v[40:43], a[212:215], a[180:183], v[40:43]\n"
    "v_mfma_f32_16x16x32_bf16 v[44:47], a[208:211], a[184:187], v[44:47]\n"
    "s_add_u32 m0, s51, 81920\n"
    "v_mfma_f32_16x16x32_bf16 v[44:47], a[212:215], a[188:191], v[44:47]\n"
    "s_waitcnt lgkmcnt(6)\n"
    "buffer_load_dwordx4 v132, s[12:15], s81 offen offset:0  lds\n"
    "v_mfma_f32_16x16x32_bf16 v[48:51], a[216:219], a[160:163], v[48:51]\n"
    "s_waitcnt lgkmcnt(4)\n"
    "ds_read_b64_tr_b16 a[200:201], v131 offset:33288\n"
    "ds_read_b64_tr_b16 a[202:203], v131 offset:34312\n"
    "v_mfma_f32_16x16x32_bf16 v[48:51], a[220:223], a[164:167], v[48:51]\n"
    "v_mfma_f32_16x16x32_bf16 v[52:55], a[216:219], a[168:171], v[52:55]\n"
    "buffer_load_dwordx4 v133, s[12:15], s81 offen offset:1024  lds\n"
    "v_mfma_f32_16x16x32_bf16 v[52:55], a[220:223], a[172:175], v[52:55]\n"
    "v_mfma_f32_16x16x32_bf16 v[56:59], a[216:219], a[176:179], v[56:59]\n"
    "ds_read_b64_tr_b16 a[204:205], v131 offset:41480\n"
    "ds_read_b64_tr_b16 a[206:207], v131 offset:42504\n"
    "v_mfma_f32_16x16x32_bf16 v[56:59], a[220:223], a[180:183], v[56:59]\n"
    "buffer_load_dwordx4 v134, s[12:15], s81 offen offset:2048  lds\n"
    "v_mfma_f32_16x16x32_bf16 v[60:63], a[216:219], a[184:187], v[60:63]\n"
    "v_mfma_f32_16x16x32_bf16 v[60:63], a[220:223], a[188:191], v[60:63]\n"
    "s_waitcnt lgkmcnt(6)\n"
    "v_mfma_f32_16x16x32_bf16 v[64:67], a[192:195], a[160:163], v[64:67]\n"
    "s_waitcnt lgkmcnt(4)\n"
    "ds_read_b64_tr_b16 a[208:209], v131 offset:33536\n"
    "ds_read_b64_tr_b16 a[210:211], v131 offset:34560\n"
    "v_mfma_f32_16x16x32_bf16 v[64:67], a[196:199], a[164:167], v[64:67]\n"
    "buffer_load_dwordx4 v135, s[12:15], s81 offen offset:3072  lds\n"
    "v_mfma_f32_16x16x32_bf16 v[68:71], a[192:195], a[168:171], v[68:71]\n"
    "s_add_u32 m0, s51, 0\n"
    "s_add_u32 s52, s61, 0\n"
    "s_and_b32 s52, s52, 0x3ffff\n"
    "v_mfma_f32_16x16x32_bf16 v[68:71], a[196:199], a[172:175], v[68:71]\n"
    "buffer_load_dwordx4 v231, s[16:19], s52 offen offset:0 nt lds\n"
    "v_mfma_f32_16x16x32_bf16 v[72:75], a[192:195], a[176:179], v[72:75]\n"
    "ds_read_b64_tr_b16 a[212:213], v131 offset:41728\n"
    "ds_read_b64_tr_b16 a[214:215], v131 offset:42752\n"
    "v_mfma_f32_16x16x32_bf16 v[72:75], a[196:199], a[180:183], v[72:75]\n"
    "s_waitcnt vmcnt(17)\n"
    "v_mfma_f32_16x16x32_bf16 v[76:79], a[192:195], a[184:187], v[76:79]\n"
    "ds_read_b128 a[128:131], v129 offset:32768\n"
    "v_mfma_f32_16x16x32_bf16 v[76:79], a[196:199], a[188:191], v[76:79]\n"
    "s_waitcnt lgkmcnt(7)\n"
    "ds_read_b128 a[132:135], v129 offset:33280\n"
    "v_mfma_f32_16x16x32_bf16 v[80:83], a[200:203], a[160:163], v[80:83]\n"
    "s_waitcnt lgkmcnt(6)\n"
    "ds_read_b64_tr_b16 a[216:217], v131 offset:33544\n"
    "ds_read_b64_tr_b16 a[218:219], v131 offset:34568\n"
    "v_mfma_f32_16x16x32_bf16 v[80:83], a[204:207], a[164:167], v[80:83]\n"
    "ds_read_b128 a[136:139], v129 offset:34816\n"
    "v_mfma_f32_16x16x32_bf16 v[84:87], a[200:203], a[168:171], v[84:87]\n"
    "ds_read_b128 a[140:143], v129 offset:35328\n"
    "v_mfma_f32_16x16x32_bf16 v[84:87], a[204:207], a[172:175], v[84:87]\n"
    "ds_read_b128 a[144:147], v129 offset:49152\n"
    "v_mfma_f32_16x16x32_bf16 v[88:91], a[200:203], a[176:179], v[88:91]\n"
    "ds_read_b64_tr_b16 a[220:221], v131 offset:41736\n"
    "ds_read_b64_tr_b16 a[222:223], v131 offset:42760\n"
    "v_mfma_f32_16x16x32_bf16 v[88:91], a[204:207], a[180:183], v[88:91]\n"
    "ds_read_b128 a[148:151], v129 offset:49664\n"
    "v_mfma_f32_16x16x32_bf16 v[92:95], a[200:203], a[184:187], v[92:95]\n"
    "ds_read_b128 a[152:155], v129 offset:51200\n"
    "v_mfma_f32_16x16x32_bf16 v[92:95], a[204:207], a[188:191], v[92:95]\n"
    "s_waitcnt lgkmcnt(13)\n"
    "ds_read_b128 a[156:159], v129 offset:51712\n"
    "v_mfma_f32_16x16x32_bf16 v[96:99], a[208:211], a[160:163], v[96:99]\n"
    "s_waitcnt lgkmcnt(12)\n"
    "v_mfma_f32_16x16x32_bf16 v[96:99], a[212:215], a[164:167], v[96:99]\n"
    "buffer_load_dwordx4 v232, s[16:19], s52 offen offset:1024 nt lds\n"
    "v_mfma_f32_16x16x32_bf16 v[100:103], a[208:211], a[168:171], v[100:103]\n"
    "v_mfma_f32_16x16x32_bf16 v[100:103], a[212:215], a[172:175], v[100:103]\n"
    "buffer_load_dwordx4 v231, s[16:19], s52 offen offset:2048 nt lds\n"
    "v_mfma_f32_16x16x32_bf16 v[104:107], a[208:211], a[176:179], v[104:107]\n"
    "v_mfma_f32_16x16x32_bf16 v[104:107], a[212:215], a[180:183], v[104:107]\n"
    "s_waitcnt vmcnt(15) lgkmcnt(0)\n"
    "buffer_load_dwordx4 v232, s[16:19], s52 offen offset:3072 nt lds\n"
    "v_mfma_f32_16x16x32_bf16 v[108:111], a[208:211], a[184:187], v[108:111]\n"
    "s_barrier\n"
    "ds_read_b64_tr_b16 a[192:193], v131 offset:49152\n"
    "ds_read_b64_tr_b16 a[194:195], v131 offset:50176\n"
    "v_mfma_f32_16x16x32_bf16 v[108:111], a[212:215], a[188:191], v[108:111]\n"
    "v_mfma_f32_16x16x32_bf16 v[112:115], a[216:219], a[160:163], v[112:115]\n"
    "ds_read_b64_tr_b16 a[196:197], v131 offset:57344\n"
    "ds_read_b64_tr_b16 a[198:199], v131 offset:58368\n"
    "v_mfma_f32_16x16x32_bf16 v[112:115], a[220:223], a[164:167], v[112:115]\n"
    "s_add_u32 m0, s51, 32768\n"
    "s_add_u32 s52, s74, s84\n"
    "v_mfma_f32_16x16x32_bf16 v[116:119], a[216:219], a[168:171], v[116:119]\n"
    "buffer_load_dwordx4 v128, s[8:11], s52 offen offset:0  lds\n"
    "v_mfma_f32_16x16x32_bf16 v[116:119], a[220:223], a[172:175], v[116:119]\n"
    "ds_read_b64_tr_b16 a[200:201], v131 offset:49160\n"
    "ds_read_b64_tr_b16 a[202:203], v131 offset:50184\n"
    "v_mfma_f32_16x16x32_bf16 v[120:123], a[216:219], a[176:179], v[120:123]\n"
    "v_mfma_f32_16x16x32_bf16 v[120:123], a[220:223], a[180:183], v[120:123]\n"
    "buffer_load_dwordx4 v128, s[8:11], s52 offen offset:1024  lds\n"
    "v_mfma_f32_16x16x32_bf16 v[124:127], a[216:219], a[184:187], v[124:127]\n"
    "ds_read_b64_tr_b16 a[204:205], v131 offset:57352\n"
    "ds_read_b64_tr_b16 a[206:207], v131 offset:58376\n"
    "v_mfma_f32_16x16x32_bf16 v[124:127], a[220:223], a[188:191], v[124:127]\n"
    "s_waitcnt lgkmcnt(6)\n"
    "v_mfma_f32_16x16x32_bf16 v[0:3], a[192:195], a[128:131], v[0:3]\n"
    "s_waitcnt lgkmcnt(4)\n"
    "ds_read_b64_tr_b16 a[208:209], v131 offset:49408\n"
    "ds_read_b64_tr_b16 a[210:211], v131 offset:50432\n"
    "v_mfma_f32_16x16x32_bf16 v[0:3], a[196:199], a[132:135], v[0:3]\n"
    "s_add_u32 m0, s51, 34816\n"
    "v_mfma_f32_16x16x32_bf16 v[4:7], a[192:195], a[136:139], v[4:7]\n"
    "s_add_u32 s52, s74, s85\n"
    "v_mfma_f32_16x16x32_bf16 v[4:7], a[196:199], a[140:143], v[4:7]\n"
    "buffer_load_dwordx4 v128, s[8:11], s52 offen offset:0  lds\n"
    "v_mfma_f32_16x16x32_bf16 v[8:11], a[192:195], a[144:147], v[8:11]\n"
    "ds_read_b64_tr_b16 a[212:213], v131 offset:57600\n"
    "ds_read_b64_tr_b16 a[214:215], v131 offset:58624\n"
    "v_mfma_f32_16x16x32_bf16 v[8:11], a[196:199], a[148:151], v[8:11]\n"
    "v_mfma_f32_16x16x32_bf16 v[12:15], a[192:195], a[152:155], v[12:15]\n"
    "v_mfma_f32_16x16x32_bf16 v[12:15], a[196:199], a[156:159], v[12:15]\n"
    "s_waitcnt lgkmcnt(6)\n"
    "buffer_load_dwordx4 v128, s[8:11], s52 offen offset:1024  lds\n"
    "v_mfma_f32_16x16x32_bf16 v[16:19], a[200:203], a[128:131], v[16:19]\n"
    "s_waitcnt lgkmcnt(4)\n"
    "ds_read_b64_tr_b16 a[216:217], v131 offset:49416\n"
    "ds_read_b64_tr_b16 a[218:219], v131 offset:50440\n"
    "v_mfma_f32_16x16x32_bf16 v[16:19], a[204:207], a[132:135], v[16:19]\n"
    "s_add_u32 m0, s51, 49152\n"
    "s_add_u32 s52, s74, s86\n"
    "v_mfma_f32_16x16x32_bf16 v[20:23], a[200:203], a[136:139], v[20:23]\n"
    "buffer_load_dwordx4 v128, s[8:11], s52 offen offset:0  lds\n"
    "v_mfma_f32_16x16x32_bf16 v[20:23], a[204:207], a[140:143], v[20:23]\n"
    "v_mfma_f32_16x16x32_bf16 v[24:27], a[200:203], a[144:147], v[24:27]\n"
    "ds_read_b64_tr_b16 a[220:221], v131 offset:57608\n"
    "ds_read_b64_tr_b16 a[222:223], v131 offset:58632\n"
    "v_mfma_f32_16x16x32_bf16 v[24:27], a[204:207], a[148:151], v[24:27]\n"
    "v_mfma_f32_16x16x32_bf16 v[28:31], a[200:203], a[152:155], v[28:31]\n"
    "buffer_load_dwordx4 v128, s[8:11], s52 offen offset:1024  lds\n"
    "v_mfma_f32_16x16x32_bf16 v[28:31], a[204:207], a[156:159], v[28:31]\n"
    "s_waitcnt lgkmcnt(6)\n"
    "v_mfma_f32_16x16x32_bf16 v[32:35], a[208:211], a[128:131], v[32:35]\n"
    "s_add_u32 m0, s51, 51200\n"
    "s_add_u32 s52, s74, s87\n"
    "ds_read_b64_tr_b16 a[192:193], v131 offset:49664\n"
    "ds_read_b64_tr_b16 a[194:195], v131 offset:50688\n"
    "s_waitcnt lgkmcnt(6)\n"
    "v_mfma_f32_16x16x32_bf16 v[32:35], a[212:215], a[132:135], v[32:35]\n"
    "buffer_load_dwordx4 v128, s[8:11], s52 offen offset:0  lds\n"
    "v_mfma_f32_16x16x32_bf16 v[36:39], a[208:211], a[136:139], v[36:39]\n"
    "v_mfma_f32_16x16x32_bf16 v[36:39], a[212:215], a[140:143], v[36:39]\n"
    "buffer_load_dwordx4 v128, s[8:11], s52 offen offset:1024  lds\n"
    "v_mfma_f32_16x16x32_bf16 v[40:43], a[208:211], a[144:147], v[40:43]\n"
    "ds_read_b64_tr_b16 a[196:197], v131 offset:57856\n"
    "ds_read_b64_tr_b16 a[198:199], v131 offset:58880\n"
    "v_mfma_f32_16x16x32_bf16 v[40:43], a[212:215], a[148:151], v[40:43]\n"
    "v_mfma_f32_16x16x32_bf16 v[44:47], a[208:211], a[152:155], v[44:47]\n"
    "s_add_u32 m0, s51, 98304\n"
    "v_mfma_f32_16x16x32_bf16 v[44:47], a[212:215], a[156:159], v[44:47]\n"
    "s_waitcnt lgkmcnt(6)\n"
    "buffer_load_dwordx4 v132, s[12:15], s82 offen offset:0  lds\n"
    "v_mfma_f32_16x16x32_bf16 v[48:51], a[216:219], a[128:131], v[48:51]\n"
    "s_waitcnt lgkmcnt(4)\n"
    "ds_read_b64_tr_b16 a[200:201], v131 offset:49672\n"
    "ds_read_b64_tr_b16 a[202:203], v131 offset:50696\n"
    "v_mfma_f32_16x16x32_bf16 v[48:51], a[220:223], a[132:135], v[48:51]\n"
    "v_mfma_f32_16x16x32_bf16 v[52:55], a[216:219], a[136:139], v[52:55]\n"
    "buffer_load_dwordx4 v133, s[12:15], s82 offen offset:1024  lds\n"
    "v_mfma_f32_16x16x32_bf16 v[52:55], a[220:223], a[140:143], v[52:55]\n"
    "v_mfma_f32_16x16x32_bf16 v[56:59], a[216:219], a[144:147], v[56:59]\n"
    "ds_read_b64_tr_b16 a[204:205], v131 offset:57864\n"
    "ds_read_b64_tr_b16 a[206:207], v131 offset:58888\n"
    "v_mfma_f32_16x16x32_bf16 v[56:59], a[220:223], a[148:151], v[56:59]\n"
    "buffer_load_dwordx4 v134, s[12:15], s82 offen offset:2048  lds\n"
    "v_mfma_f32_16x16x32_bf16 v[60:63], a[216:219], a[152:155], v[60:63]\n"
    "v_mfma_f32_16x16x32_bf16 v[60:63], a[220:223], a[156:159], v[60:63]\n"
    "s_waitcnt lgkmcnt(6)\n"
    "v_mfma_f32_16x16x32_bf16 v[64:67], a[192:195], a[128:131], v[64:67]\n"
    "s_waitcnt lgkmcnt(4)\n"
    "ds_read_b64_tr_b16 a[208:209], v131 offset:49920\n"
    "ds_read_b64_tr_b16 a[210:211], v131 offset:50944\n"
    "v_mfma_f32_16x16x32_bf16 v[64:67], a[196:199], a[132:135], v[64:67]\n"
    "buffer_load_dwordx4 v135, s[12:15], s82 offen offset:3072  lds\n"
    "v_mfma_f32_16x16x32_bf16 v[68:71], a[192:195], a[136:139], v[68:71]\n"
    "v_mfma_f32_16x16x32_bf16 v[68:71], a[196:199], a[140:143], v[68:71]\n"
    "v_mfma_f32_16x16x32_bf16 v[72:75], a[192:195], a[144:147], v[72:75]\n"
    "ds_read_b64_tr_b16 a[212:213], v131 offset:58112\n"
    "ds_read_b64_tr_b16 a[214:215], v131 offset:59136\n"
    "v_mfma_f32_16x16x32_bf16 v[72:75], a[196:199], a[148:151], v[72:75]\n"
    "s_waitcnt vmcnt(20)\n"
    "v_mfma_f32_16x16x32_bf16 v[76:79], a[192:195], a[152:155], v[76:79]\n"
    "ds_read_b128 a[160:163], v130 offset:0\n"
    "v_mfma_f32_16x16x32_bf16 v[76:79], a[196:199], a[156:159], v[76:79]\n"
    "s_waitcnt lgkmcnt(7)\n"
    "ds_read_b128 a[164:167], v130 offset:512\n"
    "v_mfma_f32_16x16x32_bf16 v[80:83], a[200:203], a[128:131], v[80:83]\n"
    "s_waitcnt lgkmcnt(6)\n"
    "ds_read_b64_tr_b16 a[216:217], v131 offset:49928\n"
    "ds_read_b64_tr_b16 a[218:219], v131 offset:50952\n"
    "v_mfma_f32_16x16x32_bf16 v[80:83], a[204:207], a[132:135], v[80:83]\n"
    "ds_read_b128 a[168:171], v130 offset:2048\n"
    "v_mfma_f32_16x16x32_bf16 v[84:87], a[200:203], a[136:139], v[84:87]\n"
    "ds_read_b128 a[172:175], v130 offset:2560\n"
    "v_mfma_f32_16x16x32_bf16 v[84:87], a[204:207], a[140:143], v[84:87]\n"
    "ds_read_b128 a[176:179], v130 offset:16384\n"
    "v_mfma_f32_16x16x32_bf16 v[88:91], a[200:203], a[144:147], v[88:91]\n"
    "ds_read_b64_tr_b16 a[220:221], v131 offset:58120\n"
    "ds_read_b64_tr_b16 a[222:223], v131 offset:59144\n"
    "v_mfma_f32_16x16x32_bf16 v[88:91], a[204:207], a[148:151], v[88:91]\n"
    "ds_read_b128 a[180:183], v130 offset:16896\n"
    "v_mfma_f32_16x16x32_bf16 v[92:95], a[200:203], a[152:155], v[92:95]\n"
    "ds_read_b128 a[184:187], v130 offset:18432\n"
    "v_mfma_f32_16x16x32_bf16 v[92:95], a[204:207], a[156:159], v[92:95]\n"
    "s_waitcnt lgkmcnt(13)\n"
    "ds_read_b128 a[188:191], v130 offset:18944\n"
    "v_mfma_f32_16x16x32_bf16 v[96:99], a[208:211], a[128:131], v[96:99]\n"
    "s_waitcnt lgkmcnt(12)\n"
    "v_mfma_f32_16x16x32_bf16 v[96:99], a[212:215], a[132:135], v[96:99]\n"
    "v_mfma_f32_16x16x32_bf16 v[100:103], a[208:211], a[136:139], v[100:103]\n"
    "v_mfma_f32_16x16x32_bf16 v[100:103], a[212:215], a[140:143], v[100:103]\n"
    "v_mfma_f32_16x16x32_bf16 v[104:107], a[208:211], a[144:147], v[104:107]\n"
    "s_add_u32 m0, s51, 131072\n"
    "v_mfma_f32_16x16x32_bf16 v[104:107], a[212:215], a[148:151], v[104:107]\n"
    "s_waitcnt vmcnt(16) lgkmcnt(0)\n"
    "v_mfma_f32_16x16x32_bf16 v[108:111], a[208:211], a[152:155], v[108:111]\n"
    "s_barrier\n"
    "ds_read_b64_tr_b16 a[192:193], v131 offset:16384\n"
    "ds_read_b64_tr_b16 a[194:195], v131 offset:17408\n"
    "v_mfma_f32_16x16x32_bf16 v[108:111], a[212:215], a[156:159], v[108:111]\n"
    "v_mfma_f32_16x16x32_bf16 v[112:115], a[216:219], a[128:131], v[112:115]\n"
    "ds_read_b64_tr_b16 a[196:197], v131 offset:24576\n"
    "ds_read_b64_tr_b16 a[198:199], v131 offset:25600\n"
    "v_mfma_f32_16x16x32_bf16 v[112:115], a[220:223], a[132:135], v[112:115]\n"
    "s_add_u32 s52, s75, s84\n"
    "v_mfma_f32_16x16x32_bf16 v[116:119], a[216:219], a[136:139], v[116:119]\n"
    "buffer_load_dwordx4 v128, s[8:11], s52 offen offset:0  lds\n"
    "v_mfma_f32_16x16x32_bf16 v[116:119], a[220:223], a[140:143], v[116:119]\n"
    "ds_read_b64_tr_b16 a[200:201], v131 offset:16392\n"
    "ds_read_b64_tr_b16 a[202:203], v131 offset:17416\n"
    "v_mfma_f32_16x16x32_bf16 v[120:123], a[216:219], a[144:147], v[120:123]\n"
    "buffer_load_dwordx4 v128, s[8:11], s52 offen offset:1024  lds\n"
    "v_mfma_f32_16x16x32_bf16 v[120:123], a[220:223], a[148:151], v[120:123]\n"
    "s_add_u32 m0, s51, 133120\n"
    "s_add_u32 s52, s75, s85\n"
    "buffer_load_dwordx4 v128, s[8:11], s52 offen offset:0  lds\n"
    "v_mfma_f32_16x16x32_bf16 v[124:127], a[216:219], a[152:155], v[124:127]\n"
    "ds_read_b64_tr_b16 a[204:205], v131 offset:24584\n"
    "ds_read_b64_tr_b16 a[206:207], v131 offset:25608\n"
    "v_mfma_f32_16x16x32_bf16 v[124:127], a[220:223], a[156:159], v[124:127]\n"
    "s_waitcnt lgkmcnt(6)\n"
    "buffer_load_dwordx4 v128, s[8:11], s52 offen offset:1024  lds\n"
    "v_mfma_f32_16x16x32_bf16 v[0:3], a[192:195], a[160:163], v[0:3]\n"
    "s_waitcnt lgkmcnt(4)\n"
    "ds_read_b64_tr_b16 a[208:209], v131 offset:16640\n"
    "ds_read_b64_tr_b16 a[210:211], v131 offset:17664\n"
    "v_mfma_f32_16x16x32_bf16 v[0:3], a[196:199], a[164:167], v[0:3]\n"
    "s_add_u32 m0, s51, 147456\n"
    "v_mfma_f32_16x16x32_bf16 v[4:7], a[192:195], a[168:171], v[4:7]\n"
    "s_add_u32 s52, s75, s86\n"
    "v_mfma_f32_16x16x32_bf16 v[4:7], a[196:199], a[172:175], v[4:7]\n"
    "buffer_load_dwordx4 v128, s[8:11], s52 offen offset:0  lds\n"
    "v_mfma_f32_16x16x32_bf16 v[8:11], a[192:195], a[176:179], v[8:11]\n"
    "ds_read_b64_tr_b16 a[212:213], v131 offset:24832\n"
    "ds_read_b64_tr_b16 a[214:215], v131 offset:25856\n"
    "v_mfma_f32_16x16x32_bf16 v[8:11], a[196:199], a[180:183], v[8:11]\n"
    "v_mfma_f32_16x16x32_bf16 v[12:15], a[192:195], a[184:187], v[12:15]\n"
    "v_mfma_f32_16x16x32_bf16 v[12:15], a[196:199], a[188:191], v[12:15]\n"
    "s_waitcnt lgkmcnt(6)\n"
    "v_mfma_f32_16x16x32_bf16 v[16:19], a[200:203], a[160:163], v[16:19]\n"
    "s_waitcnt lgkmcnt(4)\n"
    "ds_read_b64_tr_b16 a[216:217], v131 offset:16648\n"
    "ds_read_b64_tr_b16 a[218:219], v131 offset:17672\n"
    "v_mfma_f32_16x16x32_bf16 v[16:19], a[204:207], a[164:167], v[16:19]\n"
    "buffer_load_dwordx4 v128, s[8:11], s52 offen offset:1024  lds\n"
    "v_mfma_f32_16x16x32_bf16 v[20:23], a[200:203], a[168:171], v[20:23]\n"
    "v_mfma_f32_16x16x32_bf16 v[20:23], a[204:207], a[172:175], v[20:23]\n"
    "s_add_u32 m0, s51, 149504\n"
    "v_mfma_f32_16x16x32_bf16 v[24:27], a[200:203], a[176:179], v[24:27]\n"
    "s_add_u32 s52, s75, s87\n"
    "ds_read_b64_tr_b16 a[220:221], v131 offset:24840\n"
    "ds_read_b64_tr_b16 a[222:223], v131 offset:25864\n"
    "v_mfma_f32_16x16x32_bf16 v[24:27], a[204:207], a[180:183], v[24:27]\n"
    "buffer_load_dwordx4 v128, s[8:11], s52 offen offset:0  lds\n"
    "v_mfma_f32_16x16x32_bf16 v[28:31], a[200:203], a[184:187], v[28:31]\n"
    "v_mfma_f32_16x16x32_bf16 v[28:31], a[204:207], a[188:191], v[28:31]\n"
    "s_waitcnt lgkmcnt(6)\n"
    "v_mfma_f32_16x16x32_bf16 v[32:35], a[208:211], a[160:163], v[32:35]\n"
    "s_waitcnt lgkmcnt(4)\n"
    "ds_read_b64_tr_b16 a[192:193], v131 offset:16896\n"
    "ds_read_b64_tr_b16 a[194:195], v131 offset:17920\n"
    "v_mfma_f32_16x16x32_bf16 v[32:35], a[212:215], a[164:167], v[32:35]\n"
    "v_mfma_f32_16x16x32_bf16 v[36:39], a[208:211], a[168:171], v[36:39]\n"
    "buffer_load_dwordx4 v128, s[8:11], s52 offen offset:1024  lds\n"
    "v_mfma_f32_16x16x32_bf16 v[36:39], a[212:215], a[172:175], v[36:39]\n"
    "v_mfma_f32_16x16x32_bf16 v[40:43], a[208:211], a[176:179], v[40:43]\n"
    "ds_read_b64_tr_b16 a[196:197], v131 offset:25088\n"
    "ds_read_b64_tr_b16 a[198:199], v131 offset:26112\n"
    "v_mfma_f32_16x16x32_bf16 v[40:43], a[212:215], a[180:183], v[40:43]\n"
    "s_add_u32 m0, s51, 114688\n"
    "v_mfma_f32_16x16x32_bf16 v[44:47], a[208:211], a[184:187], v[44:47]\n"
    "buffer_load_dwordx4 v132, s[12:15], s83 offen offset:0  lds\n"
    "v_mfma_f32_16x16x32_bf16 v[44:47], a[212:215], a[188:191], v[44:47]\n"
    "s_waitcnt lgkmcnt(6)\n"
    "v_mfma_f32_16x16x32_bf16 v[48:51], a[216:219], a[160:163], v[48:51]\n"
    "s_waitcnt lgkmcnt(4)\n"
    "ds_read_b64_tr_b16 a[200:201], v131 offset:16904\n"
    "ds_read_b64_tr_b16 a[202:203], v131 offset:17928\n"
    "v_mfma_f32_16x16x32_bf16 v[48:51], a[220:223], a[164:167], v[48:51]\n"
    "v_mfma_f32_16x16x32_bf16 v[52:55], a[216:219], a[168:171], v[52:55]\n"
    "v_mfma_f32_16x16x32_bf16 v[52:55], a[220:223], a[172:175], v[52:55]\n"
    "buffer_load_dwordx4 v133, s[12:15], s83 offen offset:1024  lds\n"
    "v_mfma_f32_16x16x32_bf16 v[56:59], a[216:219], a[176:179], v[56:59]\n"
    "ds_read_b64_tr_b16 a[204:205], v131 offset:25096\n"
    "ds_read_b64_tr_b16 a[206:207], v131 offset:26120\n"
    "v_mfma_f32_16x16x32_bf16 v[56:59], a[220:223], a[180:183], v[56:59]\n"
    "v_mfma_f32_16x16x32_bf16 v[60:63], a[216:219], a[184:187], v[60:63]\n"
    "v_mfma_f32_16x16x32_bf16 v[60:63], a[220:223], a[188:191], v[60:63]\n"
    "s_waitcnt lgkmcnt(6)\n"
    "buffer_load_dwordx4 v134, s[12:15], s83 offen offset:2048  lds\n"
    "v_mfma_f32_16x16x32_bf16 v[64:67], a[192:195], a[160:163], v[64:67]\n"
    "s_waitcnt lgkmcnt(4)\n"
    "ds_read_b64_tr_b16 a[208:209], v131 offset:17152\n"
    "ds_read_b64_tr_b16 a[210:211], v131 offset:18176\n"
    "v_mfma_f32_16x16x32_bf16 v[64:67], a[196:199], a[164:167], v[64:67]\n"
    "v_mfma_f32_16x16x32_bf16 v[68:71], a[192:195], a[168:171], v[68:71]\n"
    "v_mfma_f32_16x16x32_bf16 v[68:71], a[196:199], a[172:175], v[68:71]\n"
    "Lpg2%=:\n"
    "s_cbranch_execz Lpg6%=\n"
    "v_mfma_f32_16x16x32_bf16 v[72:75], a[192:195], a[176:179], v[72:75]\n"
    "ds_read_b64_tr_b16 a[212:213], v131 offset:25344\n"
    "ds_read_b64_tr_b16 a[214:215], v131 offset:26368\n"
    "v_mfma_f32_16x16x32_bf16 v[72:75], a[196:199], a[180:183], v[72:75]\n"
    "s_waitcnt vmcnt(15)\n"
    "buffer_load_dwordx4 v135, s[12:15], s83 offen offset:3072  lds\n"
    "v_mfma_f32_16x16x32_bf16 v[76:79], a[192:195], a[184:187], v[76:79]\n"
    "ds_read_b128 a[128:131], v129 offset:32768\n"
    "v_mfma_f32_16x16x32_bf16 v[76:79], a[196:199], a[188:191], v[76:79]\n"
    "s_waitcnt lgkmcnt(7)\n"
    "ds_read_b128 a[132:135], v129 offset:33280\n"
    "v_mfma_f32_16x16x32_bf16 v[80:83], a[200:203], a[160:163], v[80:83]\n"
    "s_waitcnt lgkmcnt(6)\n"
    "ds_read_b64_tr_b16 a[216:217], v131 offset:17160\n"
    "ds_read_b64_tr_b16 a[218:219], v131 offset:18184\n"
    "v_mfma_f32_16x16x32_bf16 v[80:83], a[204:207], a[164:167], v[80:83]\n"
    "ds_read_b128 a[136:139], v129 offset:34816\n"
    "v_mfma_f32_16x16x32_bf16 v[84:87], a[200:203], a[168:171], v[84:87]\n"
    "ds_read_b128 a[140:143], v129 offset:35328\n"
    "v_mfma_f32_16x16x32_bf16 v[84:87], a[204:207], a[172:175], v[84:87]\n"
    "ds_read_b128 a[144:147], v129 offset:49152\n"
    "v_mfma_f32_16x16x32_bf16 v[88:91], a[200:203], a[176:179], v[88:91]\n"
    "ds_read_b64_tr_b16 a[220:221], v131 offset:25352\n"
    "ds_read_b64_tr_b16 a[222:223], v131 offset:26376\n"
    "v_mfma_f32_16x16x32_bf16 v[88:91], a[204:207], a[180:183], v[88:91]\n"
    "s_add_u32 m0, s51, 16384\n"
    "ds_read_b128 a[148:151], v129 offset:49664\n"
    "v_mfma_f32_16x16x32_bf16 v[92:95], a[200:203], a[184:187], v[92:95]\n"
    "s_add_u32 s52, s61, 16384\n"
    "ds_read_b128 a[152:155], v129 offset:51200\n"
    "v_mfma_f32_16x16x32_bf16 v[92:95], a[204:207], a[188:191], v[92:95]\n"
    "s_waitcnt lgkmcnt(13)\n"
    "ds_read_b128 a[156:159], v129 offset:51712\n"
    "v_mfma_f32_16x16x32_bf16 v[96:99], a[208:211], a[160:163], v[96:99]\n"
    "s_waitcnt lgkmcnt(12)\n"
    "v_mfma_f32_16x16x32_bf16 v[96:99], a[212:215], a[164:167], v[96:99]\n"
    "s_and_b32 s52, s52, 0x3ffff\n"
    "v_mfma_f32_16x16x32_bf16 v[100:103], a[208:211], a[168:171], v[100:103]\n"
    "buffer_load_dwordx4 v231, s[16:19], s52 offen offset:0 nt lds\n"
    "v_mfma_f32_16x16x32_bf16 v[100:103], a[212:215], a[172:175], v[100:103]\n"
    "v_mfma_f32_16x16x32_bf16 v[104:107], a[208:211], a[176:179], v[104:107]\n"
    "v_mfma_f32_16x16x32_bf16 v[104:107], a[212:215], a[180:183], v[104:107]\n"
    "s_waitcnt vmcnt(13) lgkmcnt(0)\n"
    "v_mfma_f32_16x16x32_bf16 v[108:111], a[208:211], a[184:187], v[108:111]\n"
    "s_barrier\n"
    "ds_read_b64_tr_b16 a[192:193], v131 offset:32768\n"
    "ds_read_b64_tr_b16 a[194:195], v131 offset:33792\n"
    "v_mfma_f32_16x16x32_bf16 v[108:111], a[212:215], a[188:191], v[108:111]\n"
    "buffer_load_dwordx4 v232, s[16:19], s52 offen offset:1024 nt lds\n"
    "v_mfma_f32_16x16x32_bf16 v[112:115], a[216:219], a[160:163], v[112:115]\n"
    "ds_read_b64_tr_b16 a[196:197], v131 offset:40960\n"
    "ds_read_b64_tr_b16 a[198:199], v131 offset:41984\n"
    "v_mfma_f32_16x16x32_bf16 v[112:115], a[220:223], a[164:167], v[112:115]\n"
    "v_mfma_f32_16x16x32_bf16 v[116:119], a[216:219], a[168:171], v[116:119]\n"
    "v_mfma_f32_16x16x32_bf16 v[116:119], a[220:223], a[172:175], v[116:119]\n"
    "ds_read_b64_tr_b16 a[200:201], v131 offset:32776\n"
    "ds_read_b64_tr_b16 a[202:203], v131 offset:33800\n"
    "v_mfma_f32_16x16x32_bf16 v[120:123], a[216:219], a[176:179], v[120:123]\n"
    "buffer_load_dwordx4 v231, s[16:19], s52 offen offset:2048 nt lds\n"
    "v_mfma_f32_16x16x32_bf16 v[120:123], a[220:223], a[180:183], v[120:123]\n"
    "v_mfma_f32_16x16x32_bf16 v[124:127], a[216:219], a[184:187], v[124:127]\n"
    "ds_read_b64_tr_b16 a[204:205], v131 offset:40968\n"
    "ds_read_b64_tr_b16 a[206:207], v131 offset:41992\n"
    "v_mfma_f32_16x16x32_bf16 v[124:127], a[220:223], a[188:191], v[124:127]\n"
    "s_waitcnt lgkmcnt(6)\n"
    "v_mfma_f32_16x16x32_bf16 v[0:3], a[192:195], a[128:131], v[0:3]\n"
    "s_waitcnt lgkmcnt(4)\n"
    "ds_read_b64_tr_b16 a[208:209], v131 offset:33024\n"
    "ds_read_b64_tr_b16 a[210:211], v131 offset:34048\n"
    "v_mfma_f32_16x16x32_bf16 v[0:3], a[196:199], a[132:135], v[0:3]\n"
    "v_mfma_f32_16x16x32_bf16 v[4:7], a[192:195], a[136:139], v[4:7]\n"
    "v_mfma_f32_16x16x32_bf16 v[4:7], a[196:199], a[140:143], v[4:7]\n"
    "buffer_load_dwordx4 v232, s[16:19], s52 offen offset:3072 nt lds\n"
    "v_mfma_f32_16x16x32_bf16 v[8:11], a[192:195], a[144:147], v[8:11]\n"
    "ds_read_b64_tr_b16 a[212:213], v131 offset:41216\n"
    "ds_read_b64_tr_b16 a[214:215], v131 offset:42240\n"
    "v_mfma_f32_16x16x32_bf16 v[8:11], a[196:199], a[148:151], v[8:11]\n"
    "v_mfma_f32_16x16x32_bf16 v[12:15], a[192:195], a[152:155], v[12:15]\n"
    "v_mfma_f32_16x16x32_bf16 v[12:15], a[196:199], a[156:159], v[12:15]\n"
    "s_waitcnt lgkmcnt(6)\n"
    "v_mfma_f32_16x16x32_bf16 v[16:19], a[200:203], a[128:131], v[16:19]\n"
    "s_waitcnt lgkmcnt(4)\n"
    "ds_read_b64_tr_b16 a[216:217], v131 offset:33032\n"
    "ds_read_b64_tr_b16 a[218:219], v131 offset:34056\n"
    "v_mfma_f32_16x16x32_bf16 v[16:19], a[204:207], a[132:135], v[16:19]\n"
    "v_mfma_f32_16x16x32_bf16 v[20:23], a[200:203], a[136:139], v[20:23]\n"
    "s_add_u32 m0, s51, 65536\n"
    "v_mfma_f32_16x16x32_bf16 v[20:23], a[204:207], a[140:143], v[20:23]\n"
    "s_add_u32 s52, s61, 0\n"
    "v_mfma_f32_16x16x32_bf16 v[24:27], a[200:203], a[144:147], v[24:27]\n"
    "s_and_b32 s52, s52, 0x3ffff\n"
    "ds_read_b64_tr_b16 a[220:221], v131 offset:41224\n"
    "ds_read_b64_tr_b16 a[222:223], v131 offset:42248\n"
    "v_mfma_f32_16x16x32_bf16 v[24:27], a[204:207], a[148:151], v[24:27]\n"
    "buffer_load_dwordx4 v231, s[20:23], s52 offen offset:0 nt lds\n"
    "v_mfma_f32_16x16x32_bf16 v[28:31], a[200:203], a[152:155], v[28:31]\n"
    "v_mfma_f32_16x16x32_bf16 v[28:31], a[204:207], a[156:159], v[28:31]\n"
    "s_waitcnt lgkmcnt(6)\n"
    "v_mfma_f32_16x16x32_bf16 v[32:35], a[208:211], a[128:131], v[32:35]\n"
    "s_waitcnt lgkmcnt(4)\n"
    "ds_read_b64_tr_b16 a[192:193], v131 offset:33280\n"
    "ds_read_b64_tr_b16 a[194:195], v131 offset:34304\n"
    "v_mfma_f32_16x16x32_bf16 v[32:35], a[212:215], a[132:135], v[32:35]\n"
    "v_mfma_f32_16x16x32_bf16 v[36:39], a[208:211], a[136:139], v[36:39]\n"
    "v_mfma_f32_16x16x32_bf16 v[36:39], a[212:215], a[140:143], v[36:39]\n"
    "v_mfma_f32_16x16x32_bf16 v[40:43], a[208:211], a[144:147], v[40:43]\n"
    "ds_read_b64_tr_b16 a[196:197], v131 offset:41472\n"
    "ds_read_b64_tr_b16 a[198:199], v131 offset:42496\n"
    "v_mfma_f32_16x16x32_bf16 v[40:43], a[212:215], a[148:151], v[40:43]\n"
    "buffer_load_dwordx4 v232, s[20:23], s52 offen offset:1024 nt lds\n"
    "v_mfma_f32_16x16x32_bf16 v[44:47], a[208:211], a[152:155], v[44:47]\n"
    "v_mfma_f32_16x16x32_bf16 v[44:47], a[212:215], a[156:159], v[44:47]\n"
    "s_waitcnt lgkmcnt(6)\n"
    "v_mfma_f32_16x16x32_bf16 v[48:51], a[216:219], a[128:131], v[48:51]\n"
    "s_waitcnt lgkmcnt(4)\n"
    "ds_read_b64_tr_b16 a[200:201], v131 offset:33288\n"
    "ds_read_b64_tr_b16 a[202:203], v131 offset:34312\n"
    "v_mfma_f32_16x16x32_bf16 v[48:51], a[220:223], a[132:135], v[48:51]\n"
    "v_mfma_f32_16x16x32_bf16 v[52:55], a[216:219], a[136:139], v[52:55]\n"
    "v_mfma_f32_16x16x32_bf16 v[52:55], a[220:223], a[140:143], v[52:55]\n"
    "v_mfma_f32_16x16x32_bf16 v[56:59], a[216:219], a[144:147], v[56:59]\n"
    "ds_read_b64_tr_b16 a[204:205], v131 offset:41480\n"
    "ds_read_b64_tr_b16 a[206:207], v131 offset:42504\n"
    "v_mfma_f32_16x16x32_bf16 v[56:59], a[220:223], a[148:151], v[56:59]\n"
    "buffer_load_dwordx4 v231, s[20:23], s52 offen offset:2048 nt lds\n"
    "v_mfma_f32_16x16x32_bf16 v[60:63], a[216:219], a[152:155], v[60:63]\n"
    "v_mfma_f32_16x16x32_bf16 v[60:63], a[220:223], a[156:159], v[60:63]\n"
    "s_waitcnt lgkmcnt(6)\n"
    "v_mfma_f32_16x16x32_bf16 v[64:67], a[192:195], a[128:131], v[64:67]\n"
    "s_waitcnt lgkmcnt(4)\n"
    "ds_read_b64_tr_b16 a[208:209], v131 offset:33536\n"
    "ds_read_b64_tr_b16 a[210:211], v131 offset:34560\n"
    "v_mfma_f32_16x16x32_bf16 v[64:67], a[196:199], a[132:135], v[64:67]\n"
    "v_mfma_f32_16x16x32_bf16 v[68:71], a[192:195], a[136:139], v[68:71]\n"
    "v_mfma_f32_16x16x32_bf16 v[68:71], a[196:199], a[140:143], v[68:71]\n"
    "v_mfma_f32_16x16x32_bf16 v[72:75], a[192:195], a[144:147], v[72:75]\n"
    "ds_read_b64_tr_b16 a[212:213], v131 offset:41728\n"
    "ds_read_b64_tr_b16 a[214:215], v131 offset:42752\n"
    "v_mfma_f32_16x16x32_bf16 v[72:75], a[196:199], a[148:151], v[72:75]\n"
    "s_waitcnt vmcnt(11)\n"
    "v_mfma_f32_16x16x32_bf16 v[76:79], a[192:195], a[152:155], v[76:79]\n"
    "ds_read_b128 a[160:163], v130 offset:0\n"
    "v_mfma_f32_16x16x32_bf16 v[76:79], a[196:199], a[156:159], v[76:79]\n"
    "s_waitcnt lgkmcnt(7)\n"
    "ds_read_b128 a[164:167], v130 offset:512\n"
    "v_mfma_f32_16x16x32_bf16 v[80:83], a[200:203], a[128:131], v[80:83]\n"
    "s_waitcnt lgkmcnt(6)\n"
    "ds_read_b64_tr_b16 a[216:217], v131 offset:33544\n"
    "ds_read_b64_tr_b16 a[218:219], v131 offset:34568\n"
    "v_mfma_f32_16x16x32_bf16 v[80:83], a[204:207], a[132:135], v[80:83]\n"
    "ds_read_b128 a[168:171], v130 offset:2048\n"
    "v_mfma_f32_16x16x32_bf16 v[84:87], a[200:203], a[136:139], v[84:87]\n"
    "ds_read_b128 a[172:175], v130 offset:2560\n"
    "v_mfma_f32_16x16x32_bf16 v[84:87], a[204:207], a[140:143], v[84:87]\n"
    "ds_read_b128 a[176:179], v130 offset:16384\n"
    "v_mfma_f32_16x16x32_bf16 v[88:91], a[200:203], a[144:147], v[88:91]\n"
    "ds_read_b64_tr_b16 a[220:221], v131 offset:41736\n"
    "ds_read_b64_tr_b16 a[222:223], v131 offset:42760\n"
    "v_mfma_f32_16x16x32_bf16 v[88:91], a[204:207], a[148:151], v[88:91]\n"
    "ds_read_b128 a[180:183], v130 offset:16896\n"
    "v_mfma_f32_16x16x32_bf16 v[92:95], a[200:203], a[152:155], v[92:95]\n"
    "ds_read_b128 a[184:187], v130 offset:18432\n"
    "v_mfma_f32_16x16x32_bf16 v[92:95], a[204:207], a[156:159], v[92:95]\n"
    "s_waitcnt lgkmcnt(13)\n"
    "ds_read_b128 a[188:191], v130 offset:18944\n"
    "v_mfma_f32_16x16x32_bf16 v[96:99], a[208:211], a[128:131], v[96:99]\n"
    "s_waitcnt lgkmcnt(12)\n"
    "buffer_load_dwordx4 v232, s[20:23], s52 offen offset:3072 nt lds\n"
    "v_mfma_f32_16x16x32_bf16 v[96:99], a[212:215], a[132:135], v[96:99]\n"
    "v_mfma_f32_16x16x32_bf16 v[100:103], a[208:211], a[136:139], v[100:103]\n"
    "s_add_u32 m0, s51, 32768\n"
    "v_mfma_f32_16x16x32_bf16 v[100:103], a[212:215], a[140:143], v[100:103]\n"
    "s_add_u32 s52, s61, 32768\n"
    "v_mfma_f32_16x16x32_bf16 v[104:107], a[208:211], a[144:147], v[104:107]\n"
    "s_and_b32 s52, s52, 0x3ffff\n"
    "v_mfma_f32_16x16x32_bf16 v[104:107], a[212:215], a[148:151], v[104:107]\n"
    "s_waitcnt vmcnt(8) lgkmcnt(0)\n"
    "v_mfma_f32_16x16x32_bf16 v[108:111], a[208:211], a[152:155], v[108:111]\n"
    "s_barrier\n"
    "ds_read_b64_tr_b16 a[192:193], v131 offset:49152\n"
    "ds_read_b64_tr_b16 a[194:195], v131 offset:50176\n"
    "v_mfma_f32_16x16x32_bf16 v[108:111], a[212:215], a[156:159], v[108:111]\n"
    "buffer_load_dwordx4 v231, s[16:19], s52 offen offset:0 nt lds\n"
    "v_mfma_f32_16x16x32_bf16 v[112:115], a[216:219], a[128:131], v[112:115]\n"
    "ds_read_b64_tr_b16 a[196:197], v131 offset:57344\n"
    "ds_read_b64_tr_b16 a[198:199], v131 offset:58368\n"
    "v_mfma_f32_16x16x32_bf16 v[112:115], a[220:223], a[132:135], v[112:115]\n"
    "v_mfma_f32_16x16x32_bf16 v[116:119], a[216:219], a[136:139], v[116:119]\n"
    "v_mfma_f32_16x16x32_bf16 v[116:119], a[220:223], a[140:143], v[116:119]\n"
    "ds_read_b64_tr_b16 a[200:201], v131 offset:49160\n"
    "ds_read_b64_tr_b16 a[202:203], v131 offset:50184\n"
    "v_mfma_f32_16x16x32_bf16 v[120:123], a[216:219], a[144:147], v[120:123]\n"
    "v_mfma_f32_16x16x32_bf16 v[120:123], a[220:223], a[148:151], v[120:123]\n"
    "v_mfma_f32_16x16x32_bf16 v[124:127], a[216:219], a[152:155], v[124:127]\n"
    "ds_read_b64_tr_b16 a[204:205], v131 offset:57352\n"
    "ds_read_b64_tr_b16 a[206:207], v131 offset:58376\n"
    "v_mfma_f32_16x16x32_bf16 v[124:127], a[220:223], a[156:159], v[124:127]\n"
    "s_waitcnt lgkmcnt(6)\n"
    "v_mfma_f32_16x16x32_bf16 v[0:3], a[192:195], a[160:163], v[0:3]\n"
    "s_waitcnt lgkmcnt(4)\n"
    "ds_read_b64_tr_b16 a[208:209], v131 offset:49408\n"
    "ds_read_b64_tr_b16 a[210:211], v131 offset:50432\n"
    "v_mfma_f32_16x16x32_bf16 v[0:3], a[196:199], a[164:167], v[0:3]\n"
    "ds_read_b64_tr_b16 a[212:213], v131 offset:57600\n"
    "ds_read_b64_tr_b16 a[214:215], v131 offset:58624\n"
    "v_mfma_f32_16x16x32_bf16 v[4:7], a[192:195], a[168:171], v[4:7]\n"
    "ds_read_b64_tr_b16 a[216:217], v131 offset:49416\n"
    "ds_read_b64_tr_b16 a[218:219], v131 offset:50440\n"
    "v_mfma_f32_16x16x32_bf16 v[4:7], a[196:199], a[172:175], v[4:7]\n"
    "buffer_load_dwordx4 v232, s[16:19], s52 offen offset:1024 nt lds\n"
    "v_mfma_f32_16x16x32_bf16 v[8:11], a[192:195], a[176:179], v[8:11]\n"
    "ds_read_b64_tr_b16 a[220:221], v131 offset:57608\n"
    "ds_read_b64_tr_b16 a[222:223], v131 offset:58632\n"
    "v_mfma_f32_16x16x32_bf16 v[8:11], a[196:199], a[180:183], v[8:11]\n"
    "v_mul_f32 v0, s62, v0\n"
    "v_mul_f32 v1, s62, v1\n"
    "v_mfma_f32_16x16x32_bf16 v[12:15], a[192:195], a[184:187], v[12:15]\n"
    "ds_read_b64_tr_b16 a[192:193], v131 offset:49664\n"
    "ds_read_b64_tr_b16 a[194:195], v131 offset:50688\n"
    "v_mfma_f32_16x16x32_bf16 v[12:15], a[196:199], a[188:191], v[12:15]\n"
    "s_waitcnt lgkmcnt(12)\n"
    "ds_read_b64_tr_b16 a[196:197], v131 offset:57856\n"
    "ds_read_b64_tr_b16 a[198:199], v131 offset:58880\n"
    "v_mfma_f32_16x16x32_bf16 v[16:19], a[200:203], a[160:163], v[16:19]\n"
    "s_waitcnt lgkmcnt(12)\n"
    "v_mul_f32 v2, s62, v2\n"
    "v_mul_f32 v3, s62, v3\n"
    "v_mfma_f32_16x16x32_bf16 v[16:19], a[204:207], a[164:167], v[16:19]\n"
    "v_cvt_pk_bf16_f32 v0, v0, v1\n"
    "v_cvt_pk_bf16_f32 v2, v2, v3\n"
    "v_mfma_f32_16x16x32_bf16 v[20:23], a[200:203], a[168:171], v[20:23]\n"
    "v_accvgpr_write_b32 a0, v0\n"
    "v_accvgpr_write_b32 a1, v2\n"
    "v_mfma_f32_16x16x32_bf16 v[20:23], a[204:207], a[172:175], v[20:23]\n"
    "v_mfma_f32_16x16x32_bf16 v[24:27], a[200:203], a[176:179], v[24:27]\n"
    "v_mfma_f32_16x16x32_bf16 v[24:27], a[204:207], a[180:183], v[24:27]\n"
    "v_mul_f32 v16, s62, v16\n"
    "v_mul_f32 v17, s62, v17\n"
    "v_mfma_f32_16x16x32_bf16 v[28:31], a[200:203], a[184:187], v[28:31]\n"
    "ds_read_b64_tr_b16 a[200:201], v131 offset:49672\n"
    "ds_read_b64_tr_b16 a[202:203], v131 offset:50696\n"
    "v_mfma_f32_16x16x32_bf16 v[28:31], a[204:207], a[188:191], v[28:31]\n"
    "s_waitcnt lgkmcnt(12)\n"
    "buffer_load_dwordx4 v231, s[16:19], s52 offen offset:2048 nt lds\n"
    "v_mfma_f32_16x16x32_bf16 v[32:35], a[208:211], a[160:163], v[32:35]\n"
    "s_waitcnt lgkmcnt(10)\n"
    "ds_read_b64_tr_b16 a[204:205], v131 offset:57864\n"
    "ds_read_b64_tr_b16 a[206:207], v131 offset:58888\n"
    "v_mfma_f32_16x16x32_bf16 v[32:35], a[212:215], a[164:167], v[32:35]\n"
    "v_mul_f32 v18, s62, v18\n"
    "v_mul_f32 v19, s62, v19\n"
    "v_mfma_f32_16x16x32_bf16 v[36:39], a[208:211], a[168:171], v[36:39]\n"
    "v_cvt_pk_bf16_f32 v16, v16, v17\n"
    "v_cvt_pk_bf16_f32 v18, v18, v19\n"
    "v_mfma_f32_16x16x32_bf16 v[36:39], a[212:215], a[172:175], v[36:39]\n"
    "v_accvgpr_write_b32 a2, v16\n"
    "v_accvgpr_write_b32 a3, v18\n"
    "v_mfma_f32_16x16x32_bf16 v[40:43], a[208:211], a[176:179], v[40:43]\n"
    "v_mul_f32 v4, s62, v4\n"
    "v_mul_f32 v5, s62, v5\n"
    "v_mfma_f32_16x16x32_bf16 v[40:43], a[212:215], a[180:183], v[40:43]\n"
    "v_mul_f32 v6, s62, v6\n"
    "v_mul_f32 v7, s62, v7\n"
    "v_mfma_f32_16x16x32_bf16 v[44:47], a[208:211], a[184:187], v[44:47]\n"
    "ds_read_b64_tr_b16 a[208:209], v131 offset:49920\n"
    "ds_read_b64_tr_b16 a[210:211], v131 offset:50944\n"
    "v_mfma_f32_16x16x32_bf16 v[44:47], a[212:215], a[188:191], v[44:47]\n"
    "s_waitcnt lgkmcnt(12)\n"
    "ds_read_b64_tr_b16 a[212:213], v131 offset:58112\n"
    "ds_read_b64_tr_b16 a[214:215], v131 offset:59136\n"
    "v_mfma_f32_16x16x32_bf16 v[48:51], a[216:219], a[160:163], v[48:51]\n"
    "s_waitcnt lgkmcnt(12)\n"
    "v_cvt_pk_bf16_f32 v4, v4, v5\n"
    "v_cvt_pk_bf16_f32 v6, v6, v7\n"
    "v_mfma_f32_16x16x32_bf16 v[48:51], a[220:223], a[164:167], v[48:51]\n"
    "v_accvgpr_write_b32 a16, v4\n"
    "v_accvgpr_write_b32 a17, v6\n"
    "v_mfma_f32_16x16x32_bf16 v[52:55], a[216:219], a[168:171], v[52:55]\n"
    "buffer_load_dwordx4 v232, s[16:19], s52 offen offset:3072 nt lds\n"
    "v_mfma_f32_16x16x32_bf16 v[52:55], a[220:223], a[172:175], v[52:55]\n"
    "s_add_u32 m0, s51, 81920\n"
    "v_mul_f32 v20, s62, v20\n"
    "v_mul_f32 v21, s62, v21\n"
    "v_mfma_f32_16x16x32_bf16 v[56:59], a[216:219], a[176:179], v[56:59]\n"
    "s_add_u32 s52, s61, 16384\n"
    "v_mul_f32 v22, s62, v22\n"
    "v_mul_f32 v23, s62, v23\n"
    "v_mfma_f32_16x16x32_bf16 v[56:59], a[220:223], a[180:183], v[56:59]\n"
    "s_and_b32 s52, s52, 0x3ffff\n"
    "v_cvt_pk_bf16_f32 v20, v20, v21\n"
    "v_cvt_pk_bf16_f32 v22, v22, v23\n"
    "v_mfma_f32_16x16x32_bf16 v[60:63], a[216:219], a[184:187], v[60:63]\n"
    "ds_read_b64_tr_b16 a[216:217], v131 offset:49928\n"
    "ds_read_b64_tr_b16 a[218:219], v131 offset:50952\n"
    "v_mfma_f32_16x16x32_bf16 v[60:63], a[220:223], a[188:191], v[60:63]\n"
    "s_waitcnt lgkmcnt(12)\n"
    "ds_read_b64_tr_b16 a[220:221], v131 offset:58120\n"
    "ds_read_b64_tr_b16 a[222:223], v131 offset:59144\n"
    "v_mfma_f32_16x16x32_bf16 v[64:67], a[192:195], a[160:163], v[64:67]\n"
    "s_waitcnt lgkmcnt(12)\n"
    "v_accvgpr_write_b32 a18, v20\n"
    "v_accvgpr_write_b32 a19, v22\n"
    "v_mfma_f32_16x16x32_bf16 v[64:67], a[196:199], a[164:167], v[64:67]\n"
    "v_mul_f32 v8, s62, v8\n"
    "v_mul_f32 v9, s62, v9\n"
    "v_mfma_f32_16x16x32_bf16 v[68:71], a[192:195], a[168:171], v[68:71]\n"
    "v_mul_f32 v10, s62, v10\n"
    "v_mul_f32 v11, s62, v11\n"
    "v_mfma_f32_16x16x32_bf16 v[68:71], a[196:199], a[172:175], v[68:71]\n"
    "v_cvt_pk_bf16_f32 v8, v8, v9\n"
    "v_cvt_pk_bf16_f32 v10, v10, v11\n"
    "v_mfma_f32_16x16x32_bf16 v[72:75], a[192:195], a[176:179], v[72:75]\n"
    "v_accvgpr_write_b32 a32, v8\n"
    "v_accvgpr_write_b32 a33, v10\n"
    "v_mfma_f32_16x16x32_bf16 v[72:75], a[196:199], a[180:183], v[72:75]\n"
    "buffer_load_dwordx4 v231, s[20:23], s52 offen offset:0 nt lds\n"
    "v_mfma_f32_16x16x32_bf16 v[76:79], a[192:195], a[184:187], v[76:79]\n"
    "v_mul_f32 v24, s62, v24\n"
    "v_mul_f32 v25, s62, v25\n"
    "v_mfma_f32_16x16x32_bf16 v[76:79], a[196:199], a[188:191], v[76:79]\n"
    "s_waitcnt lgkmcnt(10)\n"
    "v_mul_f32 v26, s62, v26\n"
    "v_mul_f32 v27, s62, v27\n"
    "v_mfma_f32_16x16x32_bf16 v[80:83], a[200:203], a[160:163], v[80:83]\n"
    "s_waitcnt lgkmcnt(8)\n"
    "v_cvt_pk_bf16_f32 v24, v24, v25\n"
    "v_cvt_pk_bf16_f32 v26, v26, v27\n"
    "v_mfma_f32_16x16x32_bf16 v[80:83], a[204:207], a[164:167], v[80:83]\n"
    "v_accvgpr_write_b32 a34, v24\n"
    "v_accvgpr_write_b32 a35, v26\n"
    "v_mfma_f32_16x16x32_bf16 v[84:87], a[200:203], a[168:171], v[84:87]\n"
    "v_mul_f32 v12, s62, v12\n"
    "v_mul_f32 v13, s62, v13\n"
    "v_mfma_f32_16x16x32_bf16 v[84:87], a[204:207], a[172:175], v[84:87]\n"
    "v_mul_f32 v14, s62, v14\n"
    "v_mul_f32 v15, s62, v15\n"
    "v_mfma_f32_16x16x32_bf16 v[88:91], a[200:203], a[176:179], v[88:91]\n"
    "v_cvt_pk_bf16_f32 v12, v12, v13\n"
    "v_cvt_pk_bf16_f32 v14, v14, v15\n"
    "v_mfma_f32_16x16x32_bf16 v[88:91], a[204:207], a[180:183], v[88:91]\n"
    "v_accvgpr_write_b32 a48, v12\n"
    "v_accvgpr_write_b32 a49, v14\n"
    "v_mfma_f32_16x16x32_bf16 v[92:95], a[200:203], a[184:187], v[92:95]\n"
    "v_mul_f32 v28, s62, v28\n"
    "v_mul_f32 v29, s62, v29\n"
    "v_mfma_f32_16x16x32_bf16 v[92:95], a[204:207], a[188:191], v[92:95]\n"
    "s_waitcnt lgkmcnt(6)\n"
    "v_mul_f32 v30, s62, v30\n"
    "v_mul_f32 v31, s62, v31\n"
    "v_mfma_f32_16x16x32_bf16 v[96:99], a[208:211], a[160:163], v[96:99]\n"
    "s_waitcnt lgkmcnt(4)\n"
    "buffer_load_dwordx4 v232, s[20:23], s52 offen offset:1024 nt lds\n"
    "v_mfma_f32_16x16x32_bf16 v[96:99], a[212:215], a[164:167], v[96:99]\n"
    "v_cvt_pk_bf16_f32 v28, v28, v29\n"
    "v_cvt_pk_bf16_f32 v30, v30, v31\n"
    "v_mfma_f32_16x16x32_bf16 v[100:103], a[208:211], a[168:171], v[100:103]\n"
    "v_accvgpr_write_b32 a50, v28\n"
    "v_accvgpr_write_b32 a51, v30\n"
    "v_mfma_f32_16x16x32_bf16 v[100:103], a[212:215], a[172:175], v[100:103]\n"
    "v_mul_f32 v32, s62, v32\n"
    "v_mul_f32 v33, s62, v33\n"
    "v_mfma_f32_16x16x32_bf16 v[104:107], a[208:211], a[176:179], v[104:107]\n"
    "v_mul_f32 v34, s62, v34\n"
    "v_mul_f32 v35, s62, v35\n"
    "v_mfma_f32_16x16x32_bf16 v[104:107], a[212:215], a[180:183], v[104:107]\n"
    "v_cvt_pk_bf16_f32 v32, v32, v33\n"
    "v_cvt_pk_bf16_f32 v34, v34, v35\n"
    "v_mfma_f32_16x16x32_bf16 v[108:111], a[208:211], a[184:187], v[108:111]\n"
    "v_accvgpr_write_b32 a4, v32\n"
    "v_accvgpr_write_b32 a5, v34\n"
    "v_mfma_f32_16x16x32_bf16 v[108:111], a[212:215], a[188:191], v[108:111]\n"
    "s_waitcnt lgkmcnt(2)\n"
    "v_mul_f32 v48, s62, v48\n"
    "v_mul_f32 v49, s62, v49\n"
    "v_mfma_f32_16x16x32_bf16 v[112:115], a[216:219], a[160:163], v[112:115]\n"
    "s_waitcnt lgkmcnt(0)\n"
    "buffer_load_dwordx4 v231, s[20:23], s52 offen offset:2048 nt lds\n"
    "v_mfma_f32_16x16x32_bf16 v[112:115], a[220:223], a[164:167], v[112:115]\n"
    "v_mul_f32 v50, s62, v50\n"
    "v_mul_f32 v51, s62, v51\n"
    "v_mfma_f32_16x16x32_bf16 v[116:119], a[216:219], a[168:171], v[116:119]\n"
    "s_waitcnt vmcnt(39)\n"
    "v_cvt_pk_bf16_f32 v48, v48, v49\n"
    "v_cvt_pk_bf16_f32 v50, v50, v51\n"
    "v_mfma_f32_16x16x32_bf16 v[116:119], a[220:223], a[172:175], v[116:119]\n"
    "s_barrier\n"
    "ds_read_b128 a[64:67], v228 offset:0\n"
    "ds_read_b128 a[68:71], v228 offset:256\n"
    "v_mfma_f32_16x16x32_bf16 v[120:123], a[216:219], a[176:179], v[120:123]\n"
    "ds_read_b128 a[72:75], v228 offset:512\n"
    "ds_read_b128 a[76:79], v228 offset:768\n"
    "v_mfma_f32_16x16x32_bf16 v[120:123], a[220:223], a[180:183], v[120:123]\n"
    "ds_read_b128 a[80:83], v228 offset:4096\n"
    "ds_read_b128 a[84:87], v228 offset:4352\n"
    "v_mfma_f32_16x16x32_bf16 v[124:127], a[216:219], a[184:187], v[124:127]\n"
    "ds_read_b128 a[88:91], v228 offset:4608\n"
    "ds_read_b128 a[92:95], v228 offset:4864\n"
    "v_mfma_f32_16x16x32_bf16 v[124:127], a[220:223], a[188:191], v[124:127]\n"
    "v_accvgpr_write_b32 a6, v48\n"
    "v_accvgpr_write_b32 a7, v50\n"
    "v_mul_f32 v36, s62, v36\n"
    "v_mul_f32 v37, s62, v37\n"
    "v_mul_f32 v38, s62, v38\n"
    "v_mul_f32 v39, s62, v39\n"
    "v_cvt_pk_bf16_f32 v36, v36, v37\n"
    "v_cvt_pk_bf16_f32 v38, v38, v39\n"
    "v_accvgpr_write_b32 a20, v36\n"
    "v_accvgpr_write_b32 a21, v38\n"
    "v_mul_f32 v52, s62, v52\n"
    "v_mul_f32 v53, s62, v53\n"
    "s_waitcnt lgkmcnt(7)\n"
    "v_mfma_f32_16x16x32_bf16 v[160:163], a[64:67], a[0:3], 0\n"
    "s_waitcnt lgkmcnt(6)\n"
    "buffer_load_dwordx4 v232, s[20:23], s52 offen offset:3072 nt lds\n"
    "v_mfma_f32_16x16x32_bf16 v[160:163], a[68:71], a[4:7], v[160:163]\n"
    "s_add_u32 m0, s51, 49152\n"
    "s_add_u32 s52, s61, 49152\n"
    "s_and_b32 s52, s52, 0x3ffff\n"
    "v_mul_f32 v54, s62, v54\n"
    "v_mul_f32 v55, s62, v55\n"
    "v_cvt_pk_bf16_f32 v52, v52, v53\n"
    "v_cvt_pk_bf16_f32 v54, v54, v55\n"
    "v_accvgpr_write_b32 a22, v52\n"
    "v_accvgpr_write_b32 a23, v54\n"
    "v_mul_f32 v64, s62, v64\n"
    "v_mul_f32 v65, s62, v65\n"
    "v_mul_f32 v66, s62, v66\n"
    "v_mul_f32 v67, s62, v67\n"
    "v_cvt_pk_bf16_f32 v64, v64, v65\n"
    "v_cvt_pk_bf16_f32 v66, v66, v67\n"
    "v_accvgpr_write_b32 a8, v64\n"
    "v_accvgpr_write_b32 a9, v66\n"
    "v_mul_f32 v80, s62, v80\n"
    "v_mul_f32 v81, s62, v81\n"
    "v_mul_f32 v82, s62, v82\n"
    "v_mul_f32 v83, s62, v83\n"
    "v_cvt_pk_bf16_f32 v80, v80, v81\n"
    "v_cvt_pk_bf16_f32 v82, v82, v83\n"
    "v_accvgpr_write_b32 a10, v80\n"
    "v_accvgpr_write_b32 a11, v82\n"
    "v_mul_f32 v96, s62, v96\n"
    "v_mul_f32 v97, s62, v97\n"
    "s_waitcnt lgkmcnt(5)\n"
    "v_mfma_f32_16x16x32_bf16 v[160:163], a[72:75], a[8:11], v[160:163]\n"
    "s_waitcnt lgkmcnt(4)\n"
    "v_mul_f32 v98, s62, v98\n"
    "v_mul_f32 v99, s62, v99\n"
    "v_cvt_pk_bf16_f32 v96, v96, v97\n"
    "v_cvt_pk_bf16_f32 v98, v98, v99\n"
    "v_accvgpr_write_b32 a12, v96\n"
    "v_accvgpr_write_b32 a13, v98\n"
    "v_mul_f32 v112, s62, v112\n"
    "v_mul_f32 v113, s62, v113\n"
    "v_mul_f32 v114, s62, v114\n"
    "v_mul_f32 v115, s62, v115\n"
    "v_cvt_pk_bf16_f32 v112, v112, v113\n"
    "v_cvt_pk_bf16_f32 v114, v114, v115\n"
    "v_accvgpr_write_b32 a14, v112\n"
    "v_accvgpr_write_b32 a15, v114\n"
    "v_mul_f32 v68, s62, v68\n"
    "v_mul_f32 v69, s62, v69\n"
    "v_mfma_f32_16x16x32_bf16 v[160:163], a[76:79], a[12:15], v[160:163]\n"
    "v_mul_f32 v70, s62, v70\n"
    "v_mul_f32 v71, s62, v71\n"
    "v_mfma_f32_16x16x32_bf16 v[164:167], a[64:67], a[16:19], 0\n"
    "v_cvt_pk_bf16_f32 v68, v68, v69\n"
    "v_cvt_pk_bf16_f32 v70, v70, v71\n"
    "v_mfma_f32_16x16x32_bf16 v[164:167], a[68:71], a[20:23], v[164:167]\n"
    "v_accvgpr_write_b32 a24, v68\n"
    "v_accvgpr_write_b32 a25, v70\n"
    "v_mul_f32 v84, s62, v84\n"
    "v_mul_f32 v85, s62, v85\n"
    "v_mul_f32 v86, s62, v86\n"
    "v_mul_f32 v87, s62, v87\n"
    "v_cvt_pk_bf16_f32 v84, v84, v85\n"
    "v_cvt_pk_bf16_f32 v86, v86, v87\n"
    "v_accvgpr_write_b32 a26, v84\n"
    "v_accvgpr_write_b32 a27, v86\n"
    "v_mul_f32 v100, s62, v100\n"
    "v_mul_f32 v101, s62, v101\n"
    "v_mfma_f32_16x16x32_bf16 v[164:167], a[72:75], a[24:27], v[164:167]\n"
    "v_mul_f32 v102, s62, v102\n"
    "v_mul_f32 v103, s62, v103\n"
    "v_cvt_pk_bf16_f32 v100, v100, v101\n"
    "v_cvt_pk_bf16_f32 v102, v102, v103\n"
    "v_accvgpr_write_b32 a28, v100\n"
    "v_accvgpr_write_b32 a29, v102\n"
    "v_mul_f32 v116, s62, v116\n"
    "v_mul_f32 v117, s62, v117\n"
    "v_mul_f32 v118, s62, v118\n"
    "v_mul_f32 v119, s62, v119\n"
    "v_cvt_pk_bf16_f32 v116, v116, v117\n"
    "v_cvt_pk_bf16_f32 v118, v118, v119\n"
    "v_accvgpr_write_b32 a30, v116\n"
    "v_accvgpr_write_b32 a31, v118\n"
    "v_mul_f32 v40, s62, v40\n"
    "v_mul_f32 v41, s62, v41\n"
    "v_mfma_f32_16x16x32_bf16 v[164:167], a[76:79], a[28:31], v[164:167]\n"
    "v_mul_f32 v42, s62, v42\n"
    "v_mul_f32 v43, s62, v43\n"
    "v_mfma_f32_16x16x32_bf16 v[168:171], a[64:67], a[32:35], 0\n"
    "v_cvt_pk_bf16_f32 v40, v40, v41\n"
    "v_cvt_pk_bf16_f32 v42, v42, v43\n"
    "v_accvgpr_write_b32 a36, v40\n"
    "v_accvgpr_write_b32 a37, v42\n"
    "v_mul_f32 v56, s62, v56\n"
    "v_mul_f32 v57, s62, v57\n"
    "v_mul_f32 v58, s62, v58\n"
    "v_mul_f32 v59, s62, v59\n"
    "v_cvt_pk_bf16_f32 v56, v56, v57\n"
    "v_cvt_pk_bf16_f32 v58, v58, v59\n"
    "v_accvgpr_write_b32 a38, v56\n"
    "v_accvgpr_write_b32 a39, v58\n"
    "v_mul_f32 v72, s62, v72\n"
    "v_mul_f32 v73, s62, v73\n"
    "v_mfma_f32_16x16x32_bf16 v[168:171], a[68:71], a[36:39], v[168:171]\n"
    "buffer_load_dwordx4 v231, s[16:19], s52 offen offset:0 nt lds\n"
    "v_mul_f32 v74, s62, v74\n"
    "v_mul_f32 v75, s62, v75\n"
    "v_cvt_pk_bf16_f32 v72, v72, v73\n"
    "v_cvt_pk_bf16_f32 v74, v74, v75\n"
    "v_accvgpr_write_b32 a40, v72\n"
    "v_accvgpr_write_b32 a41, v74\n"
    "v_mul_f32 v88, s62, v88\n"
    "v_mul_f32 v89, s62, v89\n"
    "v_mul_f32 v90, s62, v90\n"
    "v_mul_f32 v91, s62, v91\n"
    "v_cvt_pk_bf16_f32 v88, v88, v89\n"
    "v_cvt_pk_bf16_f32 v90, v90, v91\n"
    "v_accvgpr_write_b32 a42, v88\n"
    "v_accvgpr_write_b32 a43, v90\n"
    "v_mul_f32 v104, s62, v104\n"
    "v_mul_f32 v105, s62, v105\n"
    "v_mfma_f32_16x16x32_bf16 v[168:171], a[72:75], a[40:43], v[168:171]\n"
    "v_mul_f32 v106, s62, v106\n"
    "v_mul_f32 v107, s62, v107\n"
    "v_cvt_pk_bf16_f32 v104, v104, v105\n"
    "v_cvt_pk_bf16_f32 v106, v106, v107\n"
    "v_accvgpr_write_b32 a44, v104\n"
    "v_accvgpr_write_b32 a45, v106\n"
    "v_mul_f32 v120, s62, v120\n"
    "v_mul_f32 v121, s62, v121\n"
    "v_mul_f32 v122, s62, v122\n"
    "v_mul_f32 v123, s62, v123\n"
    "v_cvt_pk_bf16_f32 v120, v120, v121\n"
    "v_cvt_pk_bf16_f32 v122, v122, v123\n"
    "v_accvgpr_write_b32 a46, v120\n"
    "v_accvgpr_write_b32 a47, v122\n"
    "v_mul_f32 v44, s62, v44\n"
    "v_mul_f32 v45, s62, v45\n"
    "v_mfma_f32_16x16x32_bf16 v[168:171], a[76:79], a[44:47], v[168:171]\n"
    "v_mul_f32 v46, s62, v46\n"
    "v_mul_f32 v47, s62, v47\n"
    "v_mfma_f32_16x16x32_bf16 v[172:175], a[64:67], a[48:51], 0\n"
    "ds_read_b128 a[64:67], v228 offset:8192\n"
    "v_cvt_pk_bf16_f32 v44, v44, v45\n"
    "v_cvt_pk_bf16_f32 v46, v46, v47\n"
    "v_accvgpr_write_b32 a52, v44\n"
    "v_accvgpr_write_b32 a53, v46\n"
    "v_mul_f32 v60, s62, v60\n"
    "v_mul_f32 v61, s62, v61\n"
    "v_mul_f32 v62, s62, v62\n"
    "v_mul_f32 v63, s62, v63\n"
    "v_cvt_pk_bf16_f32 v60, v60, v61\n"
    "v_cvt_pk_bf16_f32 v62, v62, v63\n"
    "v_accvgpr_write_b32 a54, v60\n"
    "v_accvgpr_write_b32 a55, v62\n"
    "v_mul_f32 v76, s62, v76\n"
    "v_mul_f32 v77, s62, v77\n"
    "v_mfma_f32_16x16x32_bf16 v[172:175], a[68:71], a[52:55], v[172:175]\n"
    "ds_read_b128 a[68:71], v228 offset:8448\n"
    "v_mul_f32 v78, s62, v78\n"
    "v_mul_f32 v79, s62, v79\n"
    "v_cvt_pk_bf16_f32 v76, v76, v77\n"
    "v_cvt_pk_bf16_f32 v78, v78, v79\n"
    "v_accvgpr_write_b32 a56, v76\n"
    "v_accvgpr_write_b32 a57, v78\n"
    "v_mul_f32 v92, s62, v92\n"
    "v_mul_f32 v93, s62, v93\n"
    "v_mul_f32 v94, s62, v94\n"
    "v_mul_f32 v95, s62, v95\n"
    "v_cvt_pk_bf16_f32 v92, v92, v93\n"
    "v_cvt_pk_bf16_f32 v94, v94, v95\n"
    "v_accvgpr_write_b32 a58, v92\n"
    "v_accvgpr_write_b32 a59, v94\n"
    "v_mul_f32 v108, s62, v108\n"
    "v_mul_f32 v109, s62, v109\n"
    "v_mfma_f32_16x16x32_bf16 v[172:175], a[72:75], a[56:59], v[172:175]\n"
    "ds_read_b128 a[72:75], v228 offset:8704\n"
    "v_mul_f32 v110, s62, v110\n"
    "v_mul_f32 v111, s62, v111\n"
    "v_cvt_pk_bf16_f32 v108, v108, v109\n"
    "v_cvt_pk_bf16_f32 v110, v110, v111\n"
    "v_accvgpr_write_b32 a60, v108\n"
    "v_accvgpr_write_b32 a61, v110\n"
    "v_mul_f32 v124, s62, v124\n"
    "v_mul_f32 v125, s62, v125\n"
    "v_mul_f32 v126, s62, v126\n"
    "Lpg3%=:\n"
    "s_cbranch_execz Lpg7%=\n"
    "v_mul_f32 v127, s62, v127\n"
    "v_cvt_pk_bf16_f32 v124, v124, v125\n"
    "v_cvt_pk_bf16_f32 v126, v126, v127\n"
    "v_accvgpr_write_b32 a62, v124\n"
    "v_accvgpr_write_b32 a63, v126\n"
    "v_max3_f32 v208, v160, v161, v162\n"
    "v_max3_f32 v212, v164, v165, v166\n"
    "v_mfma_f32_16x16x32_bf16 v[172:175], a[76:79], a[60:63], v[172:175]\n"
    "s_waitcnt lgkmcnt(6)\n"
    "ds_read_b128 a[76:79], v228 offset:8960\n"
    "v_max3_f32 v216, v168, v169, v170\n"
    "v_max_f32 v208, v208, v163\n"
    "v_max_f32 v212, v212, v167\n"
    "v_max_f32 v216, v216, v171\n"
    "v_mov_b32 v209, v208\n"
    "v_mov_b32 v213, v212\n"
    "v_mov_b32 v217, v216\n"
    "v_permlane16_swap_b32 v208, v209\n"
    "v_permlane16_swap_b32 v212, v213\n"
    "v_permlane16_swap_b32 v216, v217\n"
    "v_max_f32 v208, v208, v209\n"
    "v_max_f32 v212, v212, v213\n"
    "v_max_f32 v216, v216, v217\n"
    "v_mov_b32 v209, v208\n"
    "v_mov_b32 v213, v212\n"
    "v_mov_b32 v217, v216\n"
    "v_permlane32_swap_b32 v208, v209\n"
    "v_permlane32_swap_b32 v212, v213\n"
    "v_permlane32_swap_b32 v216, v217\n"
    "v_min_f32_e64 v210, -v208, -v209\n"
    "v_min_f32_e64 v214, -v212, -v213\n"
    "v_min_f32_e64 v218, -v216, -v217\n"
    "v_mov_b32 v208, v210\n"
    "v_mov_b32 v212, v214\n"
    "v_mov_b32 v216, v218\n"
    "v_mov_b32 v209, v210\n"
    "v_mov_b32 v213, v214\n"
    "v_mov_b32 v217, v218\n"
    "v_mov_b32 v211, v210\n"
    "v_mov_b32 v215, v214\n"
    "v_mov_b32 v219, v218\n"
    "v_mfma_f32_16x16x32_bf16 v[176:179], a[80:83], a[0:3], v[208:211]\n"
    "s_waitcnt lgkmcnt(6)\n"
    "v_max3_f32 v220, v172, v173, v174\n"
    "v_add_f32 v160, v208, v160\n"
    "v_mfma_f32_16x16x32_bf16 v[176:179], a[84:87], a[4:7], v[176:179]\n"
    "s_waitcnt lgkmcnt(5)\n"
    "v_max_f32 v220, v220, v175\n"
    "v_add_f32 v161, v208, v161\n"
    "v_mfma_f32_16x16x32_bf16 v[176:179], a[88:91], a[8:11], v[176:179]\n"
    "s_waitcnt lgkmcnt(4)\n"
    "v_mov_b32 v221, v220\n"
    "v_add_f32 v162, v208, v162\n"
    "v_mfma_f32_16x16x32_bf16 v[176:179], a[92:95], a[12:15], v[176:179]\n"
    "v_permlane16_swap_b32 v220, v221\n"
    "v_add_f32 v163, v208, v163\n"
    "v_mfma_f32_16x16x32_bf16 v[180:183], a[80:83], a[16:19], v[212:215]\n"
    "v_max_f32 v220, v220, v221\n"
    "v_exp_f32 v160, v160\n"
    "v_mfma_f32_16x16x32_bf16 v[180:183], a[84:87], a[20:23], v[180:183]\n"
    "v_mov_b32 v221, v220\n"
    "v_exp_f32 v161, v161\n"
    "v_mfma_f32_16x16x32_bf16 v[180:183], a[88:91], a[24:27], v[180:183]\n"
    "v_permlane32_swap_b32 v220, v221\n"
    "v_exp_f32 v162, v162\n"
    "v_mfma_f32_16x16x32_bf16 v[180:183], a[92:95], a[28:31], v[180:183]\n"
    "v_min_f32_e64 v222, -v220, -v221\n"
    "v_exp_f32 v163, v163\n"
    "v_mfma_f32_16x16x32_bf16 v[184:187], a[80:83], a[32:35], v[216:219]\n"
    "v_mov_b32 v220, v222\n"
    "v_add_f32 v224, v224, v160\n"
    "v_mfma_f32_16x16x32_bf16 v[184:187], a[84:87], a[36:39], v[184:187]\n"
    "v_mov_b32 v221, v222\n"
    "v_add_f32 v224, v224, v161\n"
    "v_mfma_f32_16x16x32_bf16 v[184:187], a[88:91], a[40:43], v[184:187]\n"
    "v_mov_b32 v223, v222\n"
    "v_add_f32 v224, v224, v162\n"
    "v_mfma_f32_16x16x32_bf16 v[184:187], a[92:95], a[44:47], v[184:187]\n"
    "v_add_f32 v224, v224, v163\n"
    "v_cvt_pk_bf16_f32 v128, v160, v161\n"
    "v_mfma_f32_16x16x32_bf16 v[188:191], a[80:83], a[48:51], v[220:223]\n"
    "ds_read_b128 a[80:83], v228 offset:12288\n"
    "v_cvt_pk_bf16_f32 v129, v162, v163\n"
    "v_mfma_f32_16x16x32_bf16 v[188:191], a[84:87], a[52:55], v[188:191]\n"
    "ds_read_b128 a[84:87], v228 offset:12544\n"
    "v_add_f32 v164, v212, v164\n"
    "v_mfma_f32_16x16x32_bf16 v[188:191], a[88:91], a[56:59], v[188:191]\n"
    "buffer_load_dwordx4 v232, s[16:19], s52 offen offset:1024 nt lds\n"
    "v_mfma_f32_16x16x32_bf16 v[188:191], a[92:95], a[60:63], v[188:191]\n"
    "s_waitcnt lgkmcnt(5)\n"
    "ds_read_b128 a[88:91], v228 offset:12800\n"
    "ds_read_b128 a[92:95], v228 offset:13056\n"
    "v_mfma_f32_16x16x32_bf16 v[192:195], a[64:67], a[0:3], v[208:211]\n"
    "s_waitcnt lgkmcnt(6)\n"
    "v_add_f32 v165, v212, v165\n"
    "v_add_f32 v166, v212, v166\n"
    "v_mfma_f32_16x16x32_bf16 v[192:195], a[68:71], a[4:7], v[192:195]\n"
    "s_waitcnt lgkmcnt(5)\n"
    "v_add_f32 v167, v212, v167\n"
    "v_exp_f32 v164, v164\n"
    "v_mfma_f32_16x16x32_bf16 v[192:195], a[72:75], a[8:11], v[192:195]\n"
    "s_waitcnt lgkmcnt(4)\n"
    "v_exp_f32 v165, v165\n"
    "v_mfma_f32_16x16x32_bf16 v[192:195], a[76:79], a[12:15], v[192:195]\n"
    "v_exp_f32 v166, v166\n"
    "v_mfma_f32_16x16x32_bf16 v[196:199], a[64:67], a[16:19], v[212:215]\n"
    "v_exp_f32 v167, v167\n"
    "v_mfma_f32_16x16x32_bf16 v[196:199], a[68:71], a[20:23], v[196:199]\n"
    "v_cvt_pk_bf16_f32 v132, v164, v165\n"
    "v_cvt_pk_bf16_f32 v133, v166, v167\n"
    "v_mfma_f32_16x16x32_bf16 v[196:199], a[72:75], a[24:27], v[196:199]\n"
    "v_add_f32 v168, v216, v168\n"
    "v_add_f32 v169, v216, v169\n"
    "v_mfma_f32_16x16x32_bf16 v[196:199], a[76:79], a[28:31], v[196:199]\n"
    "v_add_f32 v170, v216, v170\n"
    "v_add_f32 v171, v216, v171\n"
    "v_mfma_f32_16x16x32_bf16 v[200:203], a[64:67], a[32:35], v[216:219]\n"
    "v_exp_f32 v168, v168\n"
    "v_mfma_f32_16x16x32_bf16 v[200:203], a[68:71], a[36:39], v[200:203]\n"
    "v_exp_f32 v169, v169\n"
    "v_mfma_f32_16x16x32_bf16 v[200:203], a[72:75], a[40:43], v[200:203]\n"
    "v_exp_f32 v170, v170\n"
    "v_mfma_f32_16x16x32_bf16 v[200:203], a[76:79], a[44:47], v[200:203]\n"
    "v_exp_f32 v171, v171\n"
    "v_mfma_f32_16x16x32_bf16 v[204:207], a[64:67], a[48:51], v[220:223]\n"
    "v_add_f32 v226, v226, v168\n"
    "v_add_f32 v226, v226, v169\n"
    "v_mfma_f32_16x16x32_bf16 v[204:207], a[68:71], a[52:55], v[204:207]\n"
    "v_add_f32 v226, v226, v170\n"
    "v_add_f32 v226, v226, v171\n"
    "v_mfma_f32_16x16x32_bf16 v[204:207], a[72:75], a[56:59], v[204:207]\n"
    "v_cvt_pk_bf16_f32 v136, v168, v169\n"
    "v_cvt_pk_bf16_f32 v137, v170, v171\n"
    "v_mfma_f32_16x16x32_bf16 v[204:207], a[76:79], a[60:63], v[204:207]\n"
    "s_waitcnt lgkmcnt(3)\n"
    "v_add_f32 v172, v220, v172\n"
    "v_add_f32 v173, v220, v173\n"
    "v_add_f32 v174, v220, v174\n"
    "v_add_f32 v175, v220, v175\n"
    "v_exp_f32 v172, v172\n"
    "v_exp_f32 v173, v173\n"
    "v_exp_f32 v174, v174\n"
    "v_exp_f32 v175, v175\n"
    "v_cvt_pk_bf16_f32 v140, v172, v173\n"
    "v_cvt_pk_bf16_f32 v141, v174, v175\n"
    "v_exp_f32 v176, v176\n"
    "v_exp_f32 v177, v177\n"
    "v_exp_f32 v178, v178\n"
    "v_exp_f32 v179, v179\n"
    "v_add_f32 v224, v224, v176\n"
    "v_add_f32 v224, v224, v177\n"
    "v_add_f32 v224, v224, v178\n"
    "v_add_f32 v224, v224, v179\n"
    "v_cvt_pk_bf16_f32 v130, v176, v177\n"
    "v_cvt_pk_bf16_f32 v131, v178, v179\n"
    "v_exp_f32 v180, v180\n"
    "v_exp_f32 v181, v181\n"
    "v_exp_f32 v182, v182\n"
    "v_exp_f32 v183, v183\n"
    "v_cvt_pk_bf16_f32 v134, v180, v181\n"
    "v_cvt_pk_bf16_f32 v135, v182, v183\n"
    "v_exp_f32 v184, v184\n"
    "v_exp_f32 v185, v185\n"
    "v_exp_f32 v186, v186\n"
    "v_exp_f32 v187, v187\n"
    "v_add_f32 v226, v226, v184\n"
    "v_add_f32 v226, v226, v185\n"
    "v_add_f32 v226, v226, v186\n"
    "v_add_f32 v226, v226, v187\n"
    "v_cvt_pk_bf16_f32 v138, v184, v185\n"
    "v_cvt_pk_bf16_f32 v139, v186, v187\n"
    "v_exp_f32 v188, v188\n"
    "v_exp_f32 v189, v189\n"
    "v_exp_f32 v190, v190\n"
    "v_exp_f32 v191, v191\n"
    "v_cvt_pk_bf16_f32 v142, v188, v189\n"
    "v_cvt_pk_bf16_f32 v143, v190, v191\n"
    "v_exp_f32 v192, v192\n"
    "v_exp_f32 v193, v193\n"
    "v_exp_f32 v194, v194\n"
    "v_exp_f32 v195, v195\n"
    "v_add_f32 v224, v224, v192\n"
    "v_add_f32 v224, v224, v193\n"
    "v_add_f32 v224, v224, v194\n"
    "v_add_f32 v224, v224, v195\n"
    "v_cvt_pk_bf16_f32 v144, v192, v193\n"
    "v_cvt_pk_bf16_f32 v145, v194, v195\n"
    "v_mfma_f32_16x16x32_bf16 v[192:195], a[80:83], a[0:3], v[208:211]\n"
    "s_waitcnt lgkmcnt(2)\n"
    "v_exp_f32 v196, v196\n"
    "v_mfma_f32_16x16x32_bf16 v[192:195], a[84:87], a[4:7], v[192:195]\n"
    "s_waitcnt lgkmcnt(1)\n"
    "v_exp_f32 v197, v197\n"
    "v_mfma_f32_16x16x32_bf16 v[192:195], a[88:91], a[8:11], v[192:195]\n"
    "s_waitcnt lgkmcnt(0)\n"
    "v_exp_f32 v198, v198\n"
    "v_mfma_f32_16x16x32_bf16 v[192:195], a[92:95], a[12:15], v[192:195]\n"
    "s_waitcnt vmcnt(10)\n"
    "s_barrier\n"
    "ds_read_b128 a[64:67], v228 offset:16384\n"
    "ds_read_b128 a[68:71], v228 offset:16640\n"
    "ds_read_b128 a[72:75], v228 offset:16896\n"
    "ds_read_b128 a[76:79], v228 offset:17152\n"
    "ds_read_b64_tr_b16 a[96:97], v229 offset:0\n"
    "ds_read_b64_tr_b16 a[98:99], v229 offset:4096\n"
    "ds_read_b64_tr_b16 a[100:101], v229 offset:8192\n"
    "ds_read_b64_tr_b16 a[102:103], v229 offset:12288\n"
    "v_exp_f32 v199, v199\n"
    "v_cvt_pk_bf16_f32 v148, v196, v197\n"
    "v_cvt_pk_bf16_f32 v149, v198, v199\n"
    "v_mfma_f32_16x16x32_bf16 v[196:199], a[80:83], a[16:19], v[212:215]\n"
    "v_exp_f32 v200, v200\n"
    "v_mfma_f32_16x16x32_bf16 v[196:199], a[84:87], a[20:23], v[196:199]\n"
    "v_exp_f32 v201, v201\n"
    "v_mfma_f32_16x16x32_bf16 v[196:199], a[88:91], a[24:27], v[196:199]\n"
    "v_exp_f32 v202, v202\n"
    "v_mfma_f32_16x16x32_bf16 v[196:199], a[92:95], a[28:31], v[196:199]\n"
    "v_exp_f32 v203, v203\n"
    "v_add_f32 v226, v226, v200\n"
    "v_add_f32 v226, v226, v201\n"
    "v_add_f32 v226, v226, v202\n"
    "v_add_f32 v226, v226, v203\n"
    "v_cvt_pk_bf16_f32 v152, v200, v201\n"
    "v_cvt_pk_bf16_f32 v153, v202, v203\n"
    "v_mfma_f32_16x16x32_bf16 v[200:203], a[80:83], a[32:35], v[216:219]\n"
    "v_exp_f32 v204, v204\n"
    "v_mfma_f32_16x16x32_bf16 v[200:203], a[84:87], a[36:39], v[200:203]\n"
    "v_exp_f32 v205, v205\n"
    "v_mfma_f32_16x16x32_bf16 v[200:203], a[88:91], a[40:43], v[200:203]\n"
    "v_exp_f32 v206, v206\n"
    "v_mfma_f32_16x16x32_bf16 v[200:203], a[92:95], a[44:47], v[200:203]\n"
    "v_exp_f32 v207, v207\n"
    "v_cvt_pk_bf16_f32 v156, v204, v205\n"
    "v_cvt_pk_bf16_f32 v157, v206, v207\n"
    "v_mfma_f32_16x16x32_bf16 v[204:207], a[80:83], a[48:51], v[220:223]\n"
    "ds_read_b128 a[80:83], v228 offset:20480\n"
    "v_exp_f32 v192, v192\n"
    "v_mfma_f32_16x16x32_bf16 v[204:207], a[84:87], a[52:55], v[204:207]\n"
    "v_exp_f32 v193, v193\n"
    "v_mfma_f32_16x16x32_bf16 v[204:207], a[88:91], a[56:59], v[204:207]\n"
    "v_exp_f32 v194, v194\n"
    "v_mfma_f32_16x16x32_bf16 v[204:207], a[92:95], a[60:63], v[204:207]\n"
    "v_exp_f32 v195, v195\n"
    "v_add_f32 v224, v224, v192\n"
    "v_add_f32 v224, v224, v193\n"
    "v_add_f32 v224, v224, v194\n"
    "v_add_f32 v224, v224, v195\n"
    "v_cvt_pk_bf16_f32 v146, v192, v193\n"
    "v_cvt_pk_bf16_f32 v147, v194, v195\n"
    "v_exp_f32 v196, v196\n"
    "v_exp_f32 v197, v197\n"
    "v_exp_f32 v198, v198\n"
    "v_exp_f32 v199, v199\n"
    "v_cvt_pk_bf16_f32 v150, v196, v197\n"
    "v_cvt_pk_bf16_f32 v151, v198, v199\n"
    "v_exp_f32 v200, v200\n"
    "v_exp_f32 v201, v201\n"
    "v_exp_f32 v202, v202\n"
    "v_exp_f32 v203, v203\n"
    "v_add_f32 v226, v226, v200\n"
    "v_add_f32 v226, v226, v201\n"
    "v_add_f32 v226, v226, v202\n"
    "v_add_f32 v226, v226, v203\n"
    "v_cvt_pk_bf16_f32 v154, v200, v201\n"
    "Lit0%=:\n"
    "s_waitcnt lgkmcnt(8)\n"
    "v_mfma_f32_16x16x32_bf16 v[192:195], a[64:67], a[0:3], v[208:211]\n"
    "s_mov_b32 m0, s56\n"
    "v_exp_f32 v204, v204\n"
    "s_waitcnt lgkmcnt(7)\n"
    "v_mfma_f32_16x16x32_bf16 v[192:195], a[68:71], a[4:7], v[192:195]\n"
    "s_waitcnt lgkmcnt(6)\n"
    "ds_read_b64_tr_b16 a[104:105], v230 offset:0\n"
    "v_cvt_pk_bf16_f32 v155, v202, v203\n"
    "v_mfma_f32_16x16x32_bf16 v[192:195], a[72:75], a[8:11], v[192:195]\n"
    "s_waitcnt lgkmcnt(6)\n"
    "v_exp_f32 v205, v205\n"
    "v_mfma_f32_16x16x32_bf16 v[192:195], a[76:79], a[12:15], v[192:195]\n"
    "buffer_load_dwordx4 v231, s[16:19], s58 offen offset:2048 nt lds\n"
    "v_mfma_f32_16x16x32_bf16 v[196:199], a[76:79], a[28:31], v[212:215]\n"
    "v_exp_f32 v206, v206\n"
    "v_mfma_f32_16x16x32_bf16 v[196:199], a[72:75], a[24:27], v[196:199]\n"
    "ds_read_b64_tr_b16 a[106:107], v230 offset:4096\n"
    "v_cvt_pk_bf16_f32 v158, v204, v205\n"
    "v_mfma_f32_16x16x32_bf16 v[196:199], a[68:71], a[20:23], v[196:199]\n"
    "v_exp_f32 v207, v207\n"
    "v_mfma_f32_16x16x32_bf16 v[196:199], a[64:67], a[16:19], v[196:199]\n"
    "s_waitcnt lgkmcnt(5)\n"
    "ds_read_b128 a[84:87], v228 offset:20736\n"
    "v_cvt_pk_bf16_f32 v159, v206, v207\n"
    "v_mfma_f32_16x16x32_bf16 v[0:3], a[96:99], v[128:131], 0\n"
    "s_waitcnt lgkmcnt(4)\n"
    "v_exp_f32 v192, v192\n"
    "v_mfma_f32_16x16x32_bf16 v[0:3], a[100:103], v[144:147], v[0:3]\n"
    "ds_read_b64_tr_b16 a[108:109], v230 offset:8192\n"
    "v_add_f32 v224, v224, v192\n"
    "v_mfma_f32_16x16x32_bf16 v[4:7], a[100:103], v[148:151], 0\n"
    "v_exp_f32 v193, v193\n"
    "v_mfma_f32_16x16x32_bf16 v[4:7], a[96:99], v[132:135], v[4:7]\n"
    "ds_read_b64_tr_b16 a[110:111], v230 offset:12288\n"
    "v_add_f32 v224, v224, v193\n"
    "v_mfma_f32_16x16x32_bf16 v[8:11], a[96:99], v[136:139], 0\n"
    "v_exp_f32 v194, v194\n"
    "v_mfma_f32_16x16x32_bf16 v[8:11], a[100:103], v[152:155], v[8:11]\n"
    "ds_read_b128 a[88:91], v228 offset:20992\n"
    "v_add_f32 v224, v224, v194\n"
    "v_mfma_f32_16x16x32_bf16 v[12:15], a[100:103], v[156:159], 0\n"
    "v_exp_f32 v195, v195\n"
    "v_mfma_f32_16x16x32_bf16 v[12:15], a[96:99], v[140:143], v[12:15]\n"
    "v_cvt_pk_bf16_f32 v160, v192, v193\n"
    "v_add_f32 v224, v224, v195\n"
    "v_mfma_f32_16x16x32_bf16 v[200:203], a[64:67], a[32:35], v[216:219]\n"
    "v_exp_f32 v196, v196\n"
    "v_mfma_f32_16x16x32_bf16 v[200:203], a[68:71], a[36:39], v[200:203]\n"
    "ds_read_b64_tr_b16 a[96:97], v229 offset:256\n"
    "v_cvt_pk_bf16_f32 v161, v194, v195\n"
    "v_mfma_f32_16x16x32_bf16 v[200:203], a[72:75], a[40:43], v[200:203]\n"
    "v_exp_f32 v197, v197\n"
    "v_mfma_f32_16x16x32_bf16 v[200:203], a[76:79], a[44:47], v[200:203]\n"
    "buffer_load_dwordx4 v232, s[16:19], s58 offen offset:3072 nt lds\n"
    "v_mfma_f32_16x16x32_bf16 v[204:207], a[76:79], a[60:63], v[220:223]\n"
    "v_exp_f32 v198, v198\n"
    "v_mfma_f32_16x16x32_bf16 v[204:207], a[72:75], a[56:59], v[204:207]\n"
    "s_add_u32 s58, s58, 16384\n"
    "ds_read_b64_tr_b16 a[98:99], v229 offset:4352\n"
    "v_cvt_pk_bf16_f32 v164, v196, v197\n"
    "v_mfma_f32_16x16x32_bf16 v[204:207], a[68:71], a[52:55], v[204:207]\n"
    "s_and_b32 s58, s58, 0x3ffff\n"
    "v_exp_f32 v199, v199\n"
    "v_mfma_f32_16x16x32_bf16 v[204:207], a[64:67], a[48:51], v[204:207]\n"
    "s_waitcnt lgkmcnt(6)\n"
    "ds_read_b128 a[92:95], v228 offset:21248\n"
    "v_cvt_pk_bf16_f32 v165, v198, v199\n"
    "v_mfma_f32_16x16x32_bf16 v[16:19], a[104:107], v[128:131], 0\n"
    "s_waitcnt lgkmcnt(4)\n"
    "v_exp_f32 v200, v200\n"
    "v_mfma_f32_16x16x32_bf16 v[16:19], a[108:111], v[144:147], v[16:19]\n"
    "ds_read_b64_tr_b16 a[100:101], v229 offset:8448\n"
    "v_add_f32 v226, v226, v200\n"
    "v_mfma_f32_16x16x32_bf16 v[20:23], a[108:111], v[148:151], 0\n"
    "v_exp_f32 v201, v201\n"
    "v_mfma_f32_16x16x32_bf16 v[20:23], a[104:107], v[132:135], v[20:23]\n"
    "s_add_u32 s56, s56, 16384\n"
    "ds_read_b64_tr_b16 a[102:103], v229 offset:12544\n"
    "v_add_f32 v226, v226, v201\n"
    "v_mfma_f32_16x16x32_bf16 v[24:27], a[104:107], v[136:139], 0\n"
    "s_and_b32 s56, s56, 0xffff\n"
    "v_exp_f32 v202, v202\n"
    "v_mfma_f32_16x16x32_bf16 v[24:27], a[108:111], v[152:155], v[24:27]\n"
    "ds_read_b128 a[64:67], v228 offset:24576\n"
    "v_add_f32 v226, v226, v202\n"
    "v_mfma_f32_16x16x32_bf16 v[28:31], a[108:111], v[156:159], 0\n"
    "v_exp_f32 v203, v203\n"
    "v_mfma_f32_16x16x32_bf16 v[28:31], a[104:107], v[140:143], v[28:31]\n"
    "v_cvt_pk_bf16_f32 v168, v200, v201\n"
    "v_add_f32 v226, v226, v203\n"
    "v_mfma_f32_16x16x32_bf16 v[192:195], a[80:83], a[0:3], v[208:211]\n"
    "s_mov_b32 m0, s57\n"
    "v_exp_f32 v204, v204\n"
    "v_mfma_f32_16x16x32_bf16 v[192:195], a[84:87], a[4:7], v[192:195]\n"
    "s_waitcnt lgkmcnt(6)\n"
    "ds_read_b64_tr_b16 a[104:105], v230 offset:256\n"
    "v_cvt_pk_bf16_f32 v169, v202, v203\n"
    "v_mfma_f32_16x16x32_bf16 v[192:195], a[88:91], a[8:11], v[192:195]\n"
    "s_waitcnt lgkmcnt(4)\n"
    "v_exp_f32 v205, v205\n"
    "v_mfma_f32_16x16x32_bf16 v[192:195], a[92:95], a[12:15], v[192:195]\n"
    "buffer_load_dwordx4 v231, s[20:23], s59 offen offset:0 nt lds\n"
    "v_mfma_f32_16x16x32_bf16 v[196:199], a[92:95], a[28:31], v[212:215]\n"
    "v_exp_f32 v206, v206\n"
    "v_mfma_f32_16x16x32_bf16 v[196:199], a[88:91], a[24:27], v[196:199]\n"
    "ds_read_b64_tr_b16 a[106:107], v230 offset:4352\n"
    "v_cvt_pk_bf16_f32 v172, v204, v205\n"
    "v_mfma_f32_16x16x32_bf16 v[196:199], a[84:87], a[20:23], v[196:199]\n"
    "s_add_u32 m0, s51, 131072\n"
    "v_exp_f32 v207, v207\n"
    "v_mfma_f32_16x16x32_bf16 v[196:199], a[80:83], a[16:19], v[196:199]\n"
    "s_xor_b32 s53, s88, 0\n"
    "ds_read_b128 a[68:71], v228 offset:24832\n"
    "v_cvt_pk_bf16_f32 v173, v206, v207\n"
    "v_mfma_f32_16x16x32_bf16 a[116:119], a[112:115], v[132:135], 0\n"
    "buffer_load_dwordx4 v234, s[24:27], s53 offen offset:0  lds\n"
    "v_mfma_f32_16x16x32_bf16 a[116:119], a[112:115], v[148:151], a[116:119]\n"
    "buffer_load_dwordx4 v235, s[24:27], s53 offen offset:1024  lds\n"
    "v_mfma_f32_16x16x32_bf16 v[32:35], a[96:99], v[128:131], 0\n"
    "s_waitcnt lgkmcnt(4)\n"
    "v_exp_f32 v192, v192\n"
    "v_mfma_f32_16x16x32_bf16 v[32:35], a[100:103], v[144:147], v[32:35]\n"
    "ds_read_b64_tr_b16 a[108:109], v230 offset:8448\n"
    "v_add_f32 v224, v224, v192\n"
    "v_mfma_f32_16x16x32_bf16 v[36:39], a[100:103], v[148:151], 0\n"
    "v_exp_f32 v193, v193\n"
    "v_mfma_f32_16x16x32_bf16 v[36:39], a[96:99], v[132:135], v[36:39]\n"
    "ds_read_b64_tr_b16 a[110:111], v230 offset:12544\n"
    "v_add_f32 v224, v224, v193\n"
    "v_mfma_f32_16x16x32_bf16 v[40:43], a[96:99], v[136:139], 0\n"
    "v_exp_f32 v194, v194\n"
    "v_mfma_f32_16x16x32_bf16 v[40:43], a[100:103], v[152:155], v[40:43]\n"
    "ds_read_b128 a[72:75], v228 offset:25088\n"
    "v_add_f32 v224, v224, v194\n"
    "v_mfma_f32_16x16x32_bf16 v[44:47], a[100:103], v[156:159], 0\n"
    "v_exp_f32 v195, v195\n"
    "v_mfma_f32_16x16x32_bf16 v[44:47], a[96:99], v[140:143], v[44:47]\n"
    "v_cvt_pk_bf16_f32 v162, v192, v193\n"
    "v_add_f32 v224, v224, v195\n"
    "v_mfma_f32_16x16x32_bf16 v[200:203], a[80:83], a[32:35], v[216:219]\n"
    "v_exp_f32 v196, v196\n"
    "v_mfma_f32_16x16x32_bf16 v[200:203], a[84:87], a[36:39], v[200:203]\n"
    "ds_read_b64_tr_b16 a[96:97], v229 offset:512\n"
    "v_cvt_pk_bf16_f32 v163, v194, v195\n"
    "v_mfma_f32_16x16x32_bf16 v[200:203], a[88:91], a[40:43], v[200:203]\n"
    "s_mov_b32 m0, s57\n"
    "v_exp_f32 v197, v197\n"
    "v_mfma_f32_16x16x32_bf16 v[200:203], a[92:95], a[44:47], v[200:203]\n"
    "buffer_load_dwordx4 v232, s[20:23], s59 offen offset:1024 nt lds\n"
    "v_mfma_f32_16x16x32_bf16 v[204:207], a[92:95], a[60:63], v[220:223]\n"
    "v_exp_f32 v198, v198\n"
    "v_mfma_f32_16x16x32_bf16 v[204:207], a[88:91], a[56:59], v[204:207]\n"
    "ds_read_b64_tr_b16 a[98:99], v229 offset:4608\n"
    "v_cvt_pk_bf16_f32 v166, v196, v197\n"
    "v_mfma_f32_16x16x32_bf16 v[204:207], a[84:87], a[52:55], v[204:207]\n"
    "v_exp_f32 v199, v199\n"
    "v_mfma_f32_16x16x32_bf16 v[204:207], a[80:83], a[48:51], v[204:207]\n"
    "s_waitcnt lgkmcnt(6)\n"
    "ds_read_b128 a[76:79], v228 offset:25344\n"
    "v_cvt_pk_bf16_f32 v167, v198, v199\n"
    "v_mfma_f32_16x16x32_bf16 v[48:51], a[104:107], v[128:131], 0\n"
    "s_waitcnt lgkmcnt(4)\n"
    "v_exp_f32 v200, v200\n"
    "v_mfma_f32_16x16x32_bf16 v[48:51], a[108:111], v[144:147], v[48:51]\n"
    "ds_read_b64_tr_b16 a[100:101], v229 offset:8704\n"
    "v_add_f32 v226, v226, v200\n"
    "v_mfma_f32_16x16x32_bf16 v[52:55], a[108:111], v[148:151], 0\n"
    "v_exp_f32 v201, v201\n"
    "v_mfma_f32_16x16x32_bf16 v[52:55], a[104:107], v[132:135], v[52:55]\n"
    "ds_read_b64_tr_b16 a[102:103], v229 offset:12800\n"
    "v_add_f32 v226, v226, v201\n"
    "v_mfma_f32_16x16x32_bf16 v[56:59], a[104:107], v[136:139], 0\n"
    "v_exp_f32 v202, v202\n"
    "v_mfma_f32_16x16x32_bf16 v[56:59], a[108:111], v[152:155], v[56:59]\n"
    "ds_read_b128 a[80:83], v228 offset:28672\n"
    "v_add_f32 v226, v226, v202\n"
    "v_mfma_f32_16x16x32_bf16 v[60:63], a[108:111], v[156:159], 0\n"
    "v_exp_f32 v203, v203\n"
    "v_mfma_f32_16x16x32_bf16 v[60:63], a[104:107], v[140:143], v[60:63]\n"
    "v_cvt_pk_bf16_f32 v170, v200, v201\n"
    "v_add_f32 v226, v226, v203\n"
    "v_mfma_f32_16x16x32_bf16 v[192:195], a[64:67], a[0:3], v[208:211]\n"
    "v_exp_f32 v204, v204\n"
    "v_mfma_f32_16x16x32_bf16 v[192:195], a[68:71], a[4:7], v[192:195]\n"
    "s_waitcnt lgkmcnt(6)\n"
    "ds_read_b64_tr_b16 a[104:105], v230 offset:512\n"
    "v_cvt_pk_bf16_f32 v171, v202, v203\n"
    "v_mfma_f32_16x16x32_bf16 v[192:195], a[72:75], a[8:11], v[192:195]\n"
    "s_waitcnt lgkmcnt(4)\n"
    "v_exp_f32 v205, v205\n"
    "v_mfma_f32_16x16x32_bf16 v[192:195], a[76:79], a[12:15], v[192:195]\n"
    "buffer_load_dwordx4 v231, s[20:23], s59 offen offset:2048 nt lds\n"
    "v_mfma_f32_16x16x32_bf16 v[196:199], a[76:79], a[28:31], v[212:215]\n"
    "v_exp_f32 v206, v206\n"
    "v_mfma_f32_16x16x32_bf16 v[196:199], a[72:75], a[24:27], v[196:199]\n"
    "ds_read_b64_tr_b16 a[106:107], v230 offset:4608\n"
    "v_cvt_pk_bf16_f32 v174, v204, v205\n"
    "v_mfma_f32_16x16x32_bf16 v[196:199], a[68:71], a[20:23], v[196:199]\n"
    "v_exp_f32 v207, v207\n"
    "v_mfma_f32_16x16x32_bf16 v[196:199], a[64:67], a[16:19], v[196:199]\n"
    "ds_read_b128 a[84:87], v228 offset:28928\n"
    "v_cvt_pk_bf16_f32 v175, v206, v207\n"
    "v_mfma_f32_16x16x32_bf16 v[64:67], a[96:99], v[128:131], 0\n"
    "s_waitcnt lgkmcnt(4)\n"
    "v_exp_f32 v192, v192\n"
    "v_mfma_f32_16x16x32_bf16 v[64:67], a[100:103], v[144:147], v[64:67]\n"
    "ds_read_b64_tr_b16 a[108:109], v230 offset:8704\n"
    "v_add_f32 v224, v224, v192\n"
    "v_mfma_f32_16x16x32_bf16 v[68:71], a[100:103], v[148:151], 0\n"
    "v_exp_f32 v193, v193\n"
    "v_mfma_f32_16x16x32_bf16 v[68:71], a[96:99], v[132:135], v[68:71]\n"
    "ds_read_b64_tr_b16 a[110:111], v230 offset:12800\n"
    "v_add_f32 v224, v224, v193\n"
    "v_mfma_f32_16x16x32_bf16 v[72:75], a[96:99], v[136:139], 0\n"
    "v_exp_f32 v194, v194\n"
    "v_mfma_f32_16x16x32_bf16 v[72:75], a[100:103], v[152:155], v[72:75]\n"
    "ds_read_b128 a[88:91], v228 offset:29184\n"
    "v_add_f32 v224, v224, v194\n"
    "v_mfma_f32_16x16x32_bf16 v[76:79], a[100:103], v[156:159], 0\n"
    "v_exp_f32 v195, v195\n"
    "v_mfma_f32_16x16x32_bf16 v[76:79], a[96:99], v[140:143], v[76:79]\n"
    "v_cvt_pk_bf16_f32 v176, v192, v193\n"
    "v_add_f32 v224, v224, v195\n"
    "v_mfma_f32_16x16x32_bf16 v[200:203], a[64:67], a[32:35], v[216:219]\n"
    "v_exp_f32 v196, v196\n"
    "v_mfma_f32_16x16x32_bf16 v[200:203], a[68:71], a[36:39], v[200:203]\n"
    "ds_read_b64_tr_b16 a[96:97], v229 offset:768\n"
    "v_cvt_pk_bf16_f32 v177, v194, v195\n"
    "v_mfma_f32_16x16x32_bf16 v[200:203], a[72:75], a[40:43], v[200:203]\n"
    "v_exp_f32 v197, v197\n"
    "v_mfma_f32_16x16x32_bf16 v[200:203], a[76:79], a[44:47], v[200:203]\n"
    "buffer_load_dwordx4 v232, s[20:23], s59 offen offset:3072 nt lds\n"
    "v_mfma_f32_16x16x32_bf16 v[204:207], a[76:79], a[60:63], v[220:223]\n"
    "v_exp_f32 v198, v198\n"
    "v_mfma_f32_16x16x32_bf16 v[204:207], a[72:75], a[56:59], v[204:207]\n"
    "s_add_u32 s59, s59, 16384\n"
    "ds_read_b64_tr_b16 a[98:99], v229 offset:4864\n"
    "v_cvt_pk_bf16_f32 v180, v196, v197\n"
    "v_mfma_f32_16x16x32_bf16 v[204:207], a[68:71], a[52:55], v[204:207]\n"
    "s_and_b32 s59, s59, 0x3ffff\n"
    "v_exp_f32 v199, v199\n"
    "v_mfma_f32_16x16x32_bf16 v[204:207], a[64:67], a[48:51], v[204:207]\n"
    "s_add_u32 m0, s51, 131072\n"
    "ds_read_b128 a[92:95], v228 offset:29440\n"
    "v_cvt_pk_bf16_f32 v181, v198, v199\n"
    "v_mfma_f32_16x16x32_bf16 a[120:123], a[112:115], v[140:143], 0\n"
    "buffer_load_dwordx4 v236, s[24:27], s53 offen offset:2048  lds\n"
    "v_mfma_f32_16x16x32_bf16 a[120:123], a[112:115], v[156:159], a[120:123]\n"
    "s_waitcnt lgkmcnt(7)\n"
    "buffer_load_dwordx4 v237, s[24:27], s53 offen offset:3072  lds\n"
    "v_mfma_f32_16x16x32_bf16 v[80:83], a[104:107], v[128:131], 0\n"
    "s_waitcnt lgkmcnt(4)\n"
    "v_exp_f32 v200, v200\n"
    "v_mfma_f32_16x16x32_bf16 v[80:83], a[108:111], v[144:147], v[80:83]\n"
    "s_add_u32 s57, s57, 16384\n"
    "ds_read_b64_tr_b16 a[100:101], v229 offset:8960\n"
    "v_add_f32 v226, v226, v200\n"
    "v_mfma_f32_16x16x32_bf16 v[84:87], a[108:111], v[148:151], 0\n"
    "s_bitset0_b32 s57, 17\n"
    "v_exp_f32 v201, v201\n"
    "v_mfma_f32_16x16x32_bf16 v[84:87], a[104:107], v[132:135], v[84:87]\n"
    "ds_read_b64_tr_b16 a[102:103], v229 offset:13056\n"
    "v_add_f32 v226, v226, v201\n"
    "v_mfma_f32_16x16x32_bf16 v[88:91], a[104:107], v[136:139], 0\n"
    "s_waitcnt vmcnt(12) lgkmcnt(2)\n"
    "v_exp_f32 v202, v202\n"
    "v_mfma_f32_16x16x32_bf16 v[88:91], a[108:111], v[152:155], v[88:91]\n"
    "s_barrier\n"
    "ds_read_b128 a[64:67], v228 offset:32768\n"
    "v_add_f32 v226, v226, v202\n"
    "v_mfma_f32_16x16x32_bf16 v[92:95], a[108:111], v[156:159], 0\n"
    "s_bitset1_b32 s57, 16\n"
    "v_exp_f32 v203, v203\n"
    "v_mfma_f32_16x16x32_bf16 v[92:95], a[104:107], v[140:143], v[92:95]\n"
    "v_cvt_pk_bf16_f32 v184, v200, v201\n"
    "v_add_f32 v226, v226, v203\n"
    "v_mfma_f32_16x16x32_bf16 v[192:195], a[80:83], a[0:3], v[208:211]\n"
    "s_mov_b32 m0, s56\n"
    "v_exp_f32 v204, v204\n"
    "v_mfma_f32_16x16x32_bf16 v[192:195], a[84:87], a[4:7], v[192:195]\n"
    "ds_read_b64_tr_b16 a[104:105], v230 offset:768\n"
    "v_cvt_pk_bf16_f32 v185, v202, v203\n"
    "v_mfma_f32_16x16x32_bf16 v[192:195], a[88:91], a[8:11], v[192:195]\n"
    "v_exp_f32 v205, v205\n"
    "v_mfma_f32_16x16x32_bf16 v[192:195], a[92:95], a[12:15], v[192:195]\n"
    "buffer_load_dwordx4 v231, s[16:19], s58 offen offset:0 nt lds\n"
    "v_mfma_f32_16x16x32_bf16 v[196:199], a[92:95], a[28:31], v[212:215]\n"
    "v_exp_f32 v206, v206\n"
    "v_mfma_f32_16x16x32_bf16 v[196:199], a[88:91], a[24:27], v[196:199]\n"
    "ds_read_b64_tr_b16 a[106:107], v230 offset:4864\n"
    "v_cvt_pk_bf16_f32 v188, v204, v205\n"
    "v_mfma_f32_16x16x32_bf16 v[196:199], a[84:87], a[20:23], v[196:199]\n"
    "v_exp_f32 v207, v207\n"
    "v_mfma_f32_16x16x32_bf16 v[196:199], a[80:83], a[16:19], v[196:199]\n"
    "ds_read_b128 a[68:71], v228 offset:33024\n"
    "v_cvt_pk_bf16_f32 v189, v206, v207\n"
    "v_mfma_f32_16x16x32_bf16 v[96:99], a[96:99], v[128:131], 0\n"
    "s_waitcnt lgkmcnt(4)\n"
    "v_exp_f32 v192, v192\n"
    "v_mfma_f32_16x16x32_bf16 v[96:99], a[100:103], v[144:147], v[96:99]\n"
    "ds_read_b64_tr_b16 a[108:109], v230 offset:8960\n"
    "v_add_f32 v224, v224, v192\n"
    "v_mfma_f32_16x16x32_bf16 v[100:103], a[100:103], v[148:151], 0\n"
    "v_exp_f32 v193, v193\n"
    "v_mfma_f32_16x16x32_bf16 v[100:103], a[96:99], v[132:135], v[100:103]\n"
    "ds_read_b64_tr_b16 a[110:111], v230 offset:13056\n"
    "v_add_f32 v224, v224, v193\n"
    "v_mfma_f32_16x16x32_bf16 v[104:107], a[96:99], v[136:139], 0\n"
    "v_exp_f32 v194, v194\n"
    "v_mfma_f32_16x16x32_bf16 v[104:107], a[100:103], v[152:155], v[104:107]\n"
    "ds_read_b128 a[72:75], v228 offset:33280\n"
    "v_add_f32 v224, v224, v194\n"
    "v_mfma_f32_16x16x32_bf16 v[108:111], a[100:103], v[156:159], 0\n"
    "v_exp_f32 v195, v195\n"
    "v_mfma_f32_16x16x32_bf16 v[108:111], a[96:99], v[140:143], v[108:111]\n"
    "v_cvt_pk_bf16_f32 v178, v192, v193\n"
    "v_add_f32 v224, v224, v195\n"
    "v_mfma_f32_16x16x32_bf16 v[200:203], a[80:83], a[32:35], v[216:219]\n"
    "v_exp_f32 v196, v196\n"
    "v_mfma_f32_16x16x32_bf16 v[200:203], a[84:87], a[36:39], v[200:203]\n"
    "ds_read_b64_tr_b16 a[96:97], v229 offset:16384\n"
    "v_cvt_pk_bf16_f32 v179, v194, v195\n"
    "v_mfma_f32_16x16x32_bf16 v[200:203], a[88:91], a[40:43], v[200:203]\n"
    "v_exp_f32 v197, v197\n"
    "v_mfma_f32_16x16x32_bf16 v[200:203], a[92:95], a[44:47], v[200:203]\n"
    "buffer_load_dwordx4 v232, s[16:19], s58 offen offset:1024 nt lds\n"
    "v_mfma_f32_16x16x32_bf16 v[204:207], a[92:95], a[60:63], v[220:223]\n"
    "v_exp_f32 v198, v198\n"
    "v_mfma_f32_16x16x32_bf16 v[204:207], a[88:91], a[56:59], v[204:207]\n"
    "ds_read_b64_tr_b16 a[98:99], v229 offset:20480\n"
    "v_cvt_pk_bf16_f32 v182, v196, v197\n"
    "v_mfma_f32_16x16x32_bf16 v[204:207], a[84:87], a[52:55], v[204:207]\n"
    "v_exp_f32 v199, v199\n"
    "v_mfma_f32_16x16x32_bf16 v[204:207], a[80:83], a[48:51], v[204:207]\n"
    "s_waitcnt lgkmcnt(6)\n"
    "ds_read_b128 a[76:79], v228 offset:33536\n"
    "v_cvt_pk_bf16_f32 v183, v198, v199\n"
    "v_mfma_f32_16x16x32_bf16 v[112:115], a[104:107], v[128:131], 0\n"
    "s_waitcnt lgkmcnt(4)\n"
    "v_exp_f32 v200, v200\n"
    "v_mfma_f32_16x16x32_bf16 v[112:115], a[108:111], v[144:147], v[112:115]\n"
    "ds_read_b64_tr_b16 a[100:101], v229 offset:24576\n"
    "v_add_f32 v226, v226, v200\n"
    "v_mfma_f32_16x16x32_bf16 v[116:119], a[108:111], v[148:151], 0\n"
    "v_exp_f32 v201, v201\n"
    "v_mfma_f32_16x16x32_bf16 v[116:119], a[104:107], v[132:135], v[116:119]\n"
    "ds_read_b64_tr_b16 a[102:103], v229 offset:28672\n"
    "v_add_f32 v226, v226, v201\n"
    "v_mfma_f32_16x16x32_bf16 v[120:123], a[104:107], v[136:139], 0\n"
    "v_exp_f32 v202, v202\n"
    "v_mfma_f32_16x16x32_bf16 v[120:123], a[108:111], v[152:155], v[120:123]\n"
    "ds_read_b128 a[80:83], v228 offset:36864\n"
    "v_add_f32 v226, v226, v202\n"
    "v_mfma_f32_16x16x32_bf16 v[124:127], a[108:111], v[156:159], 0\n"
    "v_exp_f32 v203, v203\n"
    "v_mfma_f32_16x16x32_bf16 v[124:127], a[104:107], v[140:143], v[124:127]\n"
    "v_cvt_pk_bf16_f32 v186, v200, v201\n"
    "v_add_f32 v226, v226, v203\n"
    "v_mfma_f32_16x16x32_bf16 v[192:195], a[64:67], a[0:3], v[208:211]\n"
    "s_mov_b32 m0, s56\n"
    "v_exp_f32 v204, v204\n"
    "v_mfma_f32_16x16x32_bf16 v[192:195], a[68:71], a[4:7], v[192:195]\n"
    "s_waitcnt lgkmcnt(6)\n"
    "ds_read_b64_tr_b16 a[104:105], v230 offset:16384\n"
    "v_cvt_pk_bf16_f32 v187, v202, v203\n"
    "v_mfma_f32_16x16x32_bf16 v[192:195], a[72:75], a[8:11], v[192:195]\n"
    "s_waitcnt lgkmcnt(4)\n"
    "v_exp_f32 v205, v205\n"
    "v_mfma_f32_16x16x32_bf16 v[192:195], a[76:79], a[12:15], v[192:195]\n"
    "buffer_load_dwordx4 v231, s[16:19], s58 offen offset:2048 nt lds\n"
    "v_mfma_f32_16x16x32_bf16 v[196:199], a[76:79], a[28:31], v[212:215]\n"
    "v_exp_f32 v206, v206\n"
    "v_mfma_f32_16x16x32_bf16 v[196:199], a[72:75], a[24:27], v[196:199]\n"
    "ds_read_b64_tr_b16 a[106:107], v230 offset:20480\n"
    "v_cvt_pk_bf16_f32 v190, v204, v205\n"
    "v_mfma_f32_16x16x32_bf16 v[196:199], a[68:71], a[20:23], v[196:199]\n"
    "s_add_u32 m0, s51, 147456\n"
    "v_exp_f32 v207, v207\n"
    "v_mfma_f32_16x16x32_bf16 v[196:199], a[64:67], a[16:19], v[196:199]\n"
    "ds_read_b128 a[84:87], v228 offset:37120\n"
    "v_cvt_pk_bf16_f32 v191, v206, v207\n"
    "v_mfma_f32_16x16x32_bf16 v[0:3], a[96:99], v[160:163], v[0:3]\n"
    "s_waitcnt lgkmcnt(4)\n"
    "v_exp_f32 v192, v192\n"
    "v_mfma_f32_16x16x32_bf16 v[0:3], a[100:103], v[176:179], v[0:3]\n"
    "s_xor_b32 s53, s88, 16384\n"
    "ds_read_b64_tr_b16 a[108:109], v230 offset:24576\n"
    "v_add_f32 v224, v224, v192\n"
    "v_mfma_f32_16x16x32_bf16 v[4:7], a[100:103], v[180:183], v[4:7]\n"
    "s_memrealtime s[8:9]\n"
    "buffer_load_dwordx4 v234, s[24:27], s53 offen offset:0  lds\n"
    "v_exp_f32 v193, v193\n"
    "v_mfma_f32_16x16x32_bf16 v[4:7], a[96:99], v[164:167], v[4:7]\n"
    "ds_read_b64_tr_b16 a[110:111], v230 offset:28672\n"
    "v_add_f32 v224, v224, v193\n"
    "v_mfma_f32_16x16x32_bf16 v[8:11], a[96:99], v[168:171], v[8:11]\n"
    "Lpg4%=:\n"
    "s_cbranch_execz Lpg8%=\n"
    "v_exp_f32 v194, v194\n"
    "v_mfma_f32_16x16x32_bf16 v[8:11], a[100:103], v[184:187], v[8:11]\n"
    "ds_read_b128 a[88:91], v228 offset:37376\n"
    "v_add_f32 v224, v224, v194\n"
    "v_mfma_f32_16x16x32_bf16 v[12:15], a[100:103], v[188:191], v[12:15]\n"
    "v_exp_f32 v195, v195\n"
    "v_mfma_f32_16x16x32_bf16 v[12:15], a[96:99], v[172:175], v[12:15]\n"
    "v_cvt_pk_bf16_f32 v128, v192, v193\n"
    "v_add_f32 v224, v224, v195\n"
    "v_mfma_f32_16x16x32_bf16 v[200:203], a[64:67], a[32:35], v[216:219]\n"
    "v_exp_f32 v196, v196\n"
    "v_mfma_f32_16x16x32_bf16 v[200:203], a[68:71], a[36:39], v[200:203]\n"
    "ds_read_b64_tr_b16 a[96:97], v229 offset:16640\n"
    "v_cvt_pk_bf16_f32 v129, v194, v195\n"
    "v_mfma_f32_16x16x32_bf16 v[200:203], a[72:75], a[40:43], v[200:203]\n"
    "s_mov_b32 m0, s56\n"
    "v_exp_f32 v197, v197\n"
    "v_mfma_f32_16x16x32_bf16 v[200:203], a[76:79], a[44:47], v[200:203]\n"
    "buffer_load_dwordx4 v232, s[16:19], s58 offen offset:3072 nt lds\n"
    "v_mfma_f32_16x16x32_bf16 v[204:207], a[76:79], a[60:63], v[220:223]\n"
    "v_exp_f32 v198, v198\n"
    "v_mfma_f32_16x16x32_bf16 v[204:207], a[72:75], a[56:59], v[204:207]\n"
    "s_add_u32 s58, s58, 16384\n"
    "ds_read_b64_tr_b16 a[98:99], v229 offset:20736\n"
    "v_cvt_pk_bf16_f32 v132, v196, v197\n"
    "v_mfma_f32_16x16x32_bf16 v[204:207], a[68:71], a[52:55], v[204:207]\n"
    "s_and_b32 s58, s58, 0x3ffff\n"
    "v_exp_f32 v199, v199\n"
    "v_mfma_f32_16x16x32_bf16 v[204:207], a[64:67], a[48:51], v[204:207]\n"
    "s_waitcnt lgkmcnt(6)\n"
    "ds_read_b128 a[92:95], v228 offset:37632\n"
    "v_cvt_pk_bf16_f32 v133, v198, v199\n"
    "v_mfma_f32_16x16x32_bf16 v[16:19], a[104:107], v[160:163], v[16:19]\n"
    "s_waitcnt lgkmcnt(4)\n"
    "v_exp_f32 v200, v200\n"
    "v_mfma_f32_16x16x32_bf16 v[16:19], a[108:111], v[176:179], v[16:19]\n"
    "s_add_u32 m0, s51, 147456\n"
    "ds_read_b64_tr_b16 a[100:101], v229 offset:24832\n"
    "v_add_f32 v226, v226, v200\n"
    "v_mfma_f32_16x16x32_bf16 v[20:23], a[108:111], v[180:183], v[20:23]\n"
    "buffer_load_dwordx4 v235, s[24:27], s53 offen offset:1024  lds\n"
    "v_exp_f32 v201, v201\n"
    "v_mfma_f32_16x16x32_bf16 v[20:23], a[104:107], v[164:167], v[20:23]\n"
    "s_add_u32 s56, s56, 16384\n"
    "ds_read_b64_tr_b16 a[102:103], v229 offset:28928\n"
    "v_add_f32 v226, v226, v201\n"
    "v_mfma_f32_16x16x32_bf16 v[24:27], a[104:107], v[168:171], v[24:27]\n"
    "s_and_b32 s56, s56, 0xffff\n"
    "v_exp_f32 v202, v202\n"
    "v_mfma_f32_16x16x32_bf16 v[24:27], a[108:111], v[184:187], v[24:27]\n"
    "ds_read_b128 a[64:67], v228 offset:40960\n"
    "v_add_f32 v226, v226, v202\n"
    "v_mfma_f32_16x16x32_bf16 v[28:31], a[108:111], v[188:191], v[28:31]\n"
    "v_exp_f32 v203, v203\n"
    "v_mfma_f32_16x16x32_bf16 v[28:31], a[104:107], v[172:175], v[28:31]\n"
    "v_cvt_pk_bf16_f32 v136, v200, v201\n"
    "v_add_f32 v226, v226, v203\n"
    "v_mfma_f32_16x16x32_bf16 v[192:195], a[80:83], a[0:3], v[208:211]\n"
    "s_mov_b32 m0, s57\n"
    "v_exp_f32 v204, v204\n"
    "v_mfma_f32_16x16x32_bf16 v[192:195], a[84:87], a[4:7], v[192:195]\n"
    "s_waitcnt lgkmcnt(6)\n"
    "ds_read_b64_tr_b16 a[104:105], v230 offset:16640\n"
    "v_cvt_pk_bf16_f32 v137, v202, v203\n"
    "v_mfma_f32_16x16x32_bf16 v[192:195], a[88:91], a[8:11], v[192:195]\n"
    "s_waitcnt lgkmcnt(4)\n"
    "v_exp_f32 v205, v205\n"
    "v_mfma_f32_16x16x32_bf16 v[192:195], a[92:95], a[12:15], v[192:195]\n"
    "buffer_load_dwordx4 v231, s[20:23], s59 offen offset:0 nt lds\n"
    "v_mfma_f32_16x16x32_bf16 v[196:199], a[92:95], a[28:31], v[212:215]\n"
    "v_exp_f32 v206, v206\n"
    "v_mfma_f32_16x16x32_bf16 v[196:199], a[88:91], a[24:27], v[196:199]\n"
    "ds_read_b64_tr_b16 a[106:107], v230 offset:20736\n"
    "v_cvt_pk_bf16_f32 v140, v204, v205\n"
    "v_mfma_f32_16x16x32_bf16 v[196:199], a[84:87], a[20:23], v[196:199]\n"
    "v_exp_f32 v207, v207\n"
    "v_mfma_f32_16x16x32_bf16 v[196:199], a[80:83], a[16:19], v[196:199]\n"
    "ds_read_b128 a[68:71], v228 offset:41216\n"
    "v_cvt_pk_bf16_f32 v141, v206, v207\n"
    "v_mfma_f32_16x16x32_bf16 a[116:119], a[112:115], v[164:167], a[116:119]\n"
    "s_waitcnt vmcnt(7)\n"
    "ds_read_b64_tr_b16 a[128:129], v242 offset:0\n"
    "ds_read_b64_tr_b16 a[132:133], v243 offset:0\n"
    "v_mfma_f32_16x16x32_bf16 a[116:119], a[112:115], v[180:183], a[116:119]\n"
    "ds_read_b64_tr_b16 a[136:137], v242 offset:256\n"
    "ds_read_b64_tr_b16 a[140:141], v243 offset:256\n"
    "v_mfma_f32_16x16x32_bf16 v[32:35], a[96:99], v[160:163], v[32:35]\n"
    "s_waitcnt lgkmcnt(8)\n"
    "v_exp_f32 v192, v192\n"
    "v_mfma_f32_16x16x32_bf16 v[32:35], a[100:103], v[176:179], v[32:35]\n"
    "s_add_u32 m0, s51, 147456\n"
    "ds_read_b64_tr_b16 a[108:109], v230 offset:24832\n"
    "v_add_f32 v224, v224, v192\n"
    "v_mfma_f32_16x16x32_bf16 v[36:39], a[100:103], v[180:183], v[36:39]\n"
    "buffer_load_dwordx4 v236, s[24:27], s53 offen offset:2048  lds\n"
    "v_exp_f32 v193, v193\n"
    "v_mfma_f32_16x16x32_bf16 v[36:39], a[96:99], v[164:167], v[36:39]\n"
    "ds_read_b64_tr_b16 a[110:111], v230 offset:28928\n"
    "v_add_f32 v224, v224, v193\n"
    "v_mfma_f32_16x16x32_bf16 v[40:43], a[96:99], v[168:171], v[40:43]\n"
    "v_exp_f32 v194, v194\n"
    "v_mfma_f32_16x16x32_bf16 v[40:43], a[100:103], v[184:187], v[40:43]\n"
    "ds_read_b128 a[72:75], v228 offset:41472\n"
    "v_add_f32 v224, v224, v194\n"
    "v_mfma_f32_16x16x32_bf16 v[44:47], a[100:103], v[188:191], v[44:47]\n"
    "v_exp_f32 v195, v195\n"
    "v_mfma_f32_16x16x32_bf16 v[44:47], a[96:99], v[172:175], v[44:47]\n"
    "v_cvt_pk_bf16_f32 v130, v192, v193\n"
    "v_add_f32 v224, v224, v195\n"
    "v_mfma_f32_16x16x32_bf16 v[200:203], a[80:83], a[32:35], v[216:219]\n"
    "v_exp_f32 v196, v196\n"
    "v_mfma_f32_16x16x32_bf16 v[200:203], a[84:87], a[36:39], v[200:203]\n"
    "ds_read_b64_tr_b16 a[96:97], v229 offset:16896\n"
    "v_cvt_pk_bf16_f32 v131, v194, v195\n"
    "v_mfma_f32_16x16x32_bf16 v[200:203], a[88:91], a[40:43], v[200:203]\n"
    "s_mov_b32 m0, s57\n"
    "v_exp_f32 v197, v197\n"
    "v_mfma_f32_16x16x32_bf16 v[200:203], a[92:95], a[44:47], v[200:203]\n"
    "buffer_load_dwordx4 v232, s[20:23], s59 offen offset:1024 nt lds\n"
    "v_mfma_f32_16x16x32_bf16 v[204:207], a[92:95], a[60:63], v[220:223]\n"
    "v_exp_f32 v198, v198\n"
    "v_mfma_f32_16x16x32_bf16 v[204:207], a[88:91], a[56:59], v[204:207]\n"
    "ds_read_b64_tr_b16 a[98:99], v229 offset:20992\n"
    "v_cvt_pk_bf16_f32 v134, v196, v197\n"
    "v_mfma_f32_16x16x32_bf16 v[204:207], a[84:87], a[52:55], v[204:207]\n"
    "v_exp_f32 v199, v199\n"
    "v_mfma_f32_16x16x32_bf16 v[204:207], a[80:83], a[48:51], v[204:207]\n"
    "s_waitcnt lgkmcnt(10)\n"
    "ds_read_b128 a[76:79], v228 offset:41728\n"
    "v_cvt_pk_bf16_f32 v135, v198, v199\n"
    "v_mfma_f32_16x16x32_bf16 v[48:51], a[104:107], v[160:163], v[48:51]\n"
    "s_waitcnt lgkmcnt(4)\n"
    "v_exp_f32 v200, v200\n"
    "v_mfma_f32_16x16x32_bf16 v[48:51], a[108:111], v[176:179], v[48:51]\n"
    "s_add_u32 m0, s51, 147456\n"
    "ds_read_b64_tr_b16 a[100:101], v229 offset:25088\n"
    "v_add_f32 v226, v226, v200\n"
    "v_mfma_f32_16x16x32_bf16 v[52:55], a[108:111], v[180:183], v[52:55]\n"
    "s_sub_u32 s11, s60, s8\n"
    "buffer_load_dwordx4 v237, s[24:27], s53 offen offset:3072  lds\n"
    "v_exp_f32 v201, v201\n"
    "v_mfma_f32_16x16x32_bf16 v[52:55], a[104:107], v[164:167], v[52:55]\n"
    "s_add_u32 s60, s60, 142\n"
    "ds_read_b64_tr_b16 a[102:103], v229 offset:29184\n"
    "v_add_f32 v226, v226, v201\n"
    "v_mfma_f32_16x16x32_bf16 v[56:59], a[104:107], v[168:171], v[56:59]\n"
    "s_cmp_gt_i32 s11, 2\n"
    "v_exp_f32 v202, v202\n"
    "v_mfma_f32_16x16x32_bf16 v[56:59], a[108:111], v[184:187], v[56:59]\n"
    "s_cbranch_scc1 Lpa1x%=\n"
    "Lpb1x%=:\n"
    "ds_read_b128 a[80:83], v228 offset:45056\n"
    "v_add_f32 v226, v226, v202\n"
    "v_mfma_f32_16x16x32_bf16 v[60:63], a[108:111], v[188:191], v[60:63]\n"
    "v_exp_f32 v203, v203\n"
    "v_mfma_f32_16x16x32_bf16 v[60:63], a[104:107], v[172:175], v[60:63]\n"
    "v_cvt_pk_bf16_f32 v138, v200, v201\n"
    "v_add_f32 v226, v226, v203\n"
    "v_mfma_f32_16x16x32_bf16 v[192:195], a[64:67], a[0:3], v[208:211]\n"
    "s_mov_b32 m0, s57\n"
    "v_exp_f32 v204, v204\n"
    "v_mfma_f32_16x16x32_bf16 v[192:195], a[68:71], a[4:7], v[192:195]\n"
    "s_waitcnt lgkmcnt(6)\n"
    "ds_read_b64_tr_b16 a[104:105], v230 offset:16896\n"
    "v_cvt_pk_bf16_f32 v139, v202, v203\n"
    "v_mfma_f32_16x16x32_bf16 v[192:195], a[72:75], a[8:11], v[192:195]\n"
    "s_waitcnt lgkmcnt(4)\n"
    "v_exp_f32 v205, v205\n"
    "v_mfma_f32_16x16x32_bf16 v[192:195], a[76:79], a[12:15], v[192:195]\n"
    "buffer_load_dwordx4 v231, s[20:23], s59 offen offset:2048 nt lds\n"
    "v_mfma_f32_16x16x32_bf16 v[196:199], a[76:79], a[28:31], v[212:215]\n"
    "v_exp_f32 v206, v206\n"
    "v_mfma_f32_16x16x32_bf16 v[196:199], a[72:75], a[24:27], v[196:199]\n"
    "ds_read_b64_tr_b16 a[106:107], v230 offset:20992\n"
    "v_cvt_pk_bf16_f32 v142, v204, v205\n"
    "v_mfma_f32_16x16x32_bf16 v[196:199], a[68:71], a[20:23], v[196:199]\n"
    "v_exp_f32 v207, v207\n"
    "v_mfma_f32_16x16x32_bf16 v[196:199], a[64:67], a[16:19], v[196:199]\n"
    "ds_read_b128 a[84:87], v228 offset:45312\n"
    "v_cvt_pk_bf16_f32 v143, v206, v207\n"
    "v_mfma_f32_16x16x32_bf16 v[64:67], a[96:99], v[160:163], v[64:67]\n"
    "s_waitcnt lgkmcnt(4)\n"
    "v_exp_f32 v192, v192\n"
    "v_mfma_f32_16x16x32_bf16 v[64:67], a[100:103], v[176:179], v[64:67]\n"
    "ds_read_b64_tr_b16 a[108:109], v230 offset:25088\n"
    "v_add_f32 v224, v224, v192\n"
    "v_mfma_f32_16x16x32_bf16 v[68:71], a[100:103], v[180:183], v[68:71]\n"
    "v_exp_f32 v193, v193\n"
    "v_mfma_f32_16x16x32_bf16 v[68:71], a[96:99], v[164:167], v[68:71]\n"
    "ds_read_b64_tr_b16 a[110:111], v230 offset:29184\n"
    "v_add_f32 v224, v224, v193\n"
    "v_mfma_f32_16x16x32_bf16 v[72:75], a[96:99], v[168:171], v[72:75]\n"
    "v_exp_f32 v194, v194\n"
    "v_mfma_f32_16x16x32_bf16 v[72:75], a[100:103], v[184:187], v[72:75]\n"
    "ds_read_b128 a[88:91], v228 offset:45568\n"
    "v_add_f32 v224, v224, v194\n"
    "v_mfma_f32_16x16x32_bf16 v[76:79], a[100:103], v[188:191], v[76:79]\n"
    "v_exp_f32 v195, v195\n"
    "v_mfma_f32_16x16x32_bf16 v[76:79], a[96:99], v[172:175], v[76:79]\n"
    "v_cvt_pk_bf16_f32 v144, v192, v193\n"
    "v_add_f32 v224, v224, v195\n"
    "v_mfma_f32_16x16x32_bf16 v[200:203], a[64:67], a[32:35], v[216:219]\n"
    "v_exp_f32 v196, v196\n"
    "v_mfma_f32_16x16x32_bf16 v[200:203], a[68:71], a[36:39], v[200:203]\n"
    "ds_read_b64_tr_b16 a[96:97], v229 offset:17152\n"
    "v_cvt_pk_bf16_f32 v145, v194, v195\n"
    "v_mfma_f32_16x16x32_bf16 v[200:203], a[72:75], a[40:43], v[200:203]\n"
    "v_exp_f32 v197, v197\n"
    "v_mfma_f32_16x16x32_bf16 v[200:203], a[76:79], a[44:47], v[200:203]\n"
    "buffer_load_dwordx4 v232, s[20:23], s59 offen offset:3072 nt lds\n"
    "v_mfma_f32_16x16x32_bf16 v[204:207], a[76:79], a[60:63], v[220:223]\n"
    "v_exp_f32 v198, v198\n"
    "v_mfma_f32_16x16x32_bf16 v[204:207], a[72:75], a[56:59], v[204:207]\n"
    "s_add_u32 s59, s59, 16384\n"
    "ds_read_b64_tr_b16 a[98:99], v229 offset:21248\n"
    "v_cvt_pk_bf16_f32 v148, v196, v197\n"
    "v_mfma_f32_16x16x32_bf16 v[204:207], a[68:71], a[52:55], v[204:207]\n"
    "s_and_b32 s59, s59, 0x3ffff\n"
    "v_exp_f32 v199, v199\n"
    "v_mfma_f32_16x16x32_bf16 v[204:207], a[64:67], a[48:51], v[204:207]\n"
    "ds_read_b128 a[92:95], v228 offset:45824\n"
    "v_cvt_pk_bf16_f32 v149, v198, v199\n"
    "v_mfma_f32_16x16x32_bf16 a[120:123], a[112:115], v[172:175], a[120:123]\n"
    "ds_read_b64_tr_b16 a[144:145], v242 offset:512\n"
    "ds_read_b64_tr_b16 a[148:149], v243 offset:512\n"
    "v_mfma_f32_16x16x32_bf16 a[120:123], a[112:115], v[188:191], a[120:123]\n"
    "s_waitcnt lgkmcnt(9)\n"
    "ds_read_b64_tr_b16 a[152:153], v242 offset:768\n"
    "ds_read_b64_tr_b16 a[156:157], v243 offset:768\n"
    "v_mfma_f32_16x16x32_bf16 v[80:83], a[104:107], v[160:163], v[80:83]\n"
    "s_waitcnt lgkmcnt(8)\n"
    "v_exp_f32 v200, v200\n"
    "v_mfma_f32_16x16x32_bf16 v[80:83], a[108:111], v[176:179], v[80:83]\n"
    "s_add_u32 s57, s57, 16384\n"
    "ds_read_b64_tr_b16 a[100:101], v229 offset:25344\n"
    "v_add_f32 v226, v226, v200\n"
    "v_mfma_f32_16x16x32_bf16 v[84:87], a[108:111], v[180:183], v[84:87]\n"
    "s_bitset0_b32 s57, 17\n"
    "v_exp_f32 v201, v201\n"
    "v_mfma_f32_16x16x32_bf16 v[84:87], a[104:107], v[164:167], v[84:87]\n"
    "ds_read_b64_tr_b16 a[102:103], v229 offset:29440\n"
    "v_add_f32 v226, v226, v201\n"
    "v_mfma_f32_16x16x32_bf16 v[88:91], a[104:107], v[168:171], v[88:91]\n"
    "s_waitcnt vmcnt(14) lgkmcnt(6)\n"
    "v_exp_f32 v202, v202\n"
    "v_mfma_f32_16x16x32_bf16 v[88:91], a[108:111], v[184:187], v[88:91]\n"
    "s_barrier\n"
    "ds_read_b128 a[64:67], v228 offset:49152\n"
    "v_add_f32 v226, v226, v202\n"
    "v_mfma_f32_16x16x32_bf16 v[92:95], a[108:111], v[188:191], v[92:95]\n"
    "s_bitset1_b32 s57, 16\n"
    "v_exp_f32 v203, v203\n"
    "v_mfma_f32_16x16x32_bf16 v[92:95], a[104:107], v[172:175], v[92:95]\n"
    "v_cvt_pk_bf16_f32 v152, v200, v201\n"
    "v_add_f32 v226, v226, v203\n"
    "v_mfma_f32_16x16x32_bf16 v[192:195], a[80:83], a[0:3], v[208:211]\n"
    "s_mov_b32 m0, s56\n"
    "v_exp_f32 v204, v204\n"
    "v_mfma_f32_16x16x32_bf16 v[192:195], a[84:87], a[4:7], v[192:195]\n"
    "ds_read_b64_tr_b16 a[104:105], v230 offset:17152\n"
    "v_cvt_pk_bf16_f32 v153, v202, v203\n"
    "v_mfma_f32_16x16x32_bf16 v[192:195], a[88:91], a[8:11], v[192:195]\n"
    "v_exp_f32 v205, v205\n"
    "v_mfma_f32_16x16x32_bf16 v[192:195], a[92:95], a[12:15], v[192:195]\n"
    "buffer_load_dwordx4 v231, s[16:19], s58 offen offset:0 nt lds\n"
    "v_mfma_f32_16x16x32_bf16 v[196:199], a[92:95], a[28:31], v[212:215]\n"
    "v_exp_f32 v206, v206\n"
    "v_mfma_f32_16x16x32_bf16 v[196:199], a[88:91], a[24:27], v[196:199]\n"
    "ds_read_b64_tr_b16 a[106:107], v230 offset:21248\n"
    "v_cvt_pk_bf16_f32 v156, v204, v205\n"
    "v_mfma_f32_16x16x32_bf16 v[196:199], a[84:87], a[20:23], v[196:199]\n"
    "v_exp_f32 v207, v207\n"
    "v_mfma_f32_16x16x32_bf16 v[196:199], a[80:83], a[16:19], v[196:199]\n"
    "ds_read_b128 a[68:71], v228 offset:49408\n"
    "v_cvt_pk_bf16_f32 v157, v206, v207\n"
    "v_mfma_f32_16x16x32_bf16 v[96:99], a[96:99], v[160:163], v[96:99]\n"
    "s_waitcnt lgkmcnt(4)\n"
    "v_exp_f32 v192, v192\n"
    "v_mfma_f32_16x16x32_bf16 v[96:99], a[100:103], v[176:179], v[96:99]\n"
    "ds_read_b64_tr_b16 a[108:109], v230 offset:25344\n"
    "v_add_f32 v224, v224, v192\n"
    "v_mfma_f32_16x16x32_bf16 v[100:103], a[100:103], v[180:183], v[100:103]\n"
    "v_exp_f32 v193, v193\n"
    "v_mfma_f32_16x16x32_bf16 v[100:103], a[96:99], v[164:167], v[100:103]\n"
    "ds_read_b64_tr_b16 a[110:111], v230 offset:29440\n"
    "v_add_f32 v224, v224, v193\n"
    "v_mfma_f32_16x16x32_bf16 v[104:107], a[96:99], v[168:171], v[104:107]\n"
    "v_exp_f32 v194, v194\n"
    "v_mfma_f32_16x16x32_bf16 v[104:107], a[100:103], v[184:187], v[104:107]\n"
    "ds_read_b128 a[72:75], v228 offset:49664\n"
    "v_add_f32 v224, v224, v194\n"
    "v_mfma_f32_16x16x32_bf16 v[108:111], a[100:103], v[188:191], v[108:111]\n"
    "v_exp_f32 v195, v195\n"
    "v_mfma_f32_16x16x32_bf16 v[108:111], a[96:99], v[172:175], v[108:111]\n"
    "v_cvt_pk_bf16_f32 v146, v192, v193\n"
    "v_add_f32 v224, v224, v195\n"
    "v_mfma_f32_16x16x32_bf16 v[200:203], a[80:83], a[32:35], v[216:219]\n"
    "v_exp_f32 v196, v196\n"
    "v_mfma_f32_16x16x32_bf16 v[200:203], a[84:87], a[36:39], v[200:203]\n"
    "ds_read_b64_tr_b16 a[96:97], v229 offset:32768\n"
    "v_cvt_pk_bf16_f32 v147, v194, v195\n"
    "v_mfma_f32_16x16x32_bf16 v[200:203], a[88:91], a[40:43], v[200:203]\n"
    "v_exp_f32 v197, v197\n"
    "v_mfma_f32_16x16x32_bf16 v[200:203], a[92:95], a[44:47], v[200:203]\n"
    "buffer_load_dwordx4 v232, s[16:19], s58 offen offset:1024 nt lds\n"
    "v_mfma_f32_16x16x32_bf16 v[204:207], a[92:95], a[60:63], v[220:223]\n"
    "v_exp_f32 v198, v198\n"
    "v_mfma_f32_16x16x32_bf16 v[204:207], a[88:91], a[56:59], v[204:207]\n"
    "ds_read_b64_tr_b16 a[98:99], v229 offset:36864\n"
    "v_cvt_pk_bf16_f32 v150, v196, v197\n"
    "v_mfma_f32_16x16x32_bf16 v[204:207], a[84:87], a[52:55], v[204:207]\n"
    "v_exp_f32 v199, v199\n"
    "v_mfma_f32_16x16x32_bf16 v[204:207], a[80:83], a[48:51], v[204:207]\n"
    "s_waitcnt lgkmcnt(6)\n"
    "ds_read_b128 a[76:79], v228 offset:49920\n"
    "v_cvt_pk_bf16_f32 v151, v198, v199\n"
    "v_mfma_f32_16x16x32_bf16 v[112:115], a[104:107], v[160:163], v[112:115]\n"
    "s_waitcnt lgkmcnt(4)\n"
    "v_exp_f32 v200, v200\n"
    "v_mfma_f32_16x16x32_bf16 v[112:115], a[108:111], v[176:179], v[112:115]\n"
    "ds_read_b64_tr_b16 a[100:101], v229 offset:40960\n"
    "v_add_f32 v226, v226, v200\n"
    "v_mfma_f32_16x16x32_bf16 v[116:119], a[108:111], v[180:183], v[116:119]\n"
    "v_exp_f32 v201, v201\n"
    "v_mfma_f32_16x16x32_bf16 v[116:119], a[104:107], v[164:167], v[116:119]\n"
    "ds_read_b64_tr_b16 a[102:103], v229 offset:45056\n"
    "v_add_f32 v226, v226, v201\n"
    "v_mfma_f32_16x16x32_bf16 v[120:123], a[104:107], v[168:171], v[120:123]\n"
    "v_exp_f32 v202, v202\n"
    "v_mfma_f32_16x16x32_bf16 v[120:123], a[108:111], v[184:187], v[120:123]\n"
    "ds_read_b128 a[80:83], v228 offset:53248\n"
    "v_add_f32 v226, v226, v202\n"
    "v_mfma_f32_16x16x32_bf16 v[124:127], a[108:111], v[188:191], v[124:127]\n"
    "v_exp_f32 v203, v203\n"
    "v_mfma_f32_16x16x32_bf16 v[124:127], a[104:107], v[172:175], v[124:127]\n"
    "v_cvt_pk_bf16_f32 v154, v200, v201\n"
    "v_add_f32 v226, v226, v203\n"
    "v_mfma_f32_16x16x32_bf16 v[192:195], a[64:67], a[0:3], v[208:211]\n"
    "s_mov_b32 m0, s56\n"
    "v_exp_f32 v204, v204\n"
    "v_mfma_f32_16x16x32_bf16 v[192:195], a[68:71], a[4:7], v[192:195]\n"
    "s_waitcnt lgkmcnt(6)\n"
    "ds_read_b64_tr_b16 a[104:105], v230 offset:32768\n"
    "v_cvt_pk_bf16_f32 v155, v202, v203\n"
    "v_mfma_f32_16x16x32_bf16 v[192:195], a[72:75], a[8:11], v[192:195]\n"
    "s_waitcnt lgkmcnt(4)\n"
    "v_exp_f32 v205, v205\n"
    "v_mfma_f32_16x16x32_bf16 v[192:195], a[76:79], a[12:15], v[192:195]\n"
    "s_nop 0\n"
    "buffer_load_dwordx4 v231, s[16:19], s58 offen offset:2048 nt lds\n"
    "v_mfma_f32_16x16x32_bf16 v[196:199], a[76:79], a[28:31], v[212:215]\n"
    "v_exp_f32 v206, v206\n"
    "v_mfma_f32_16x16x32_bf16 v[196:199], a[72:75], a[24:27], v[196:199]\n"
    "ds_read_b64_tr_b16 a[106:107], v230 offset:36864\n"
    "v_cvt_pk_bf16_f32 v158, v204, v205\n"
    "v_mfma_f32_16x16x32_bf16 v[196:199], a[68:71], a[20:23], v[196:199]\n"
    "s_add_u32 m0, s51, 131072\n"
    "v_exp_f32 v207, v207\n"
    "v_mfma_f32_16x16x32_bf16 v[196:199], a[64:67], a[16:19], v[196:199]\n"
    "ds_read_b128 a[84:87], v228 offset:53504\n"
    "v_cvt_pk_bf16_f32 v159, v206, v207\n"
    "v_mfma_f32_16x16x32_bf16 v[0:3], a[96:99], v[128:131], v[0:3]\n"
    "s_waitcnt lgkmcnt(4)\n"
    "v_exp_f32 v192, v192\n"
    "v_mfma_f32_16x16x32_bf16 v[0:3], a[100:103], v[144:147], v[0:3]\n"
    "s_xor_b32 s53, s88, 32768\n"
    "ds_read_b64_tr_b16 a[108:109], v230 offset:40960\n"
    "v_add_f32 v224, v224, v192\n"
    "v_mfma_f32_16x16x32_bf16 v[4:7], a[100:103], v[148:151], v[4:7]\n"
    "s_memrealtime s[8:9]\n"
    "buffer_load_dwordx4 v234, s[24:27], s53 offen offset:0  lds\n"
    "v_exp_f32 v193, v193\n"
    "v_mfma_f32_16x16x32_bf16 v[4:7], a[96:99], v[132:135], v[4:7]\n"
    "ds_read_b64_tr_b16 a[110:111], v230 offset:45056\n"
    "v_add_f32 v224, v224, v193\n"
    "v_mfma_f32_16x16x32_bf16 v[8:11], a[96:99], v[136:139], v[8:11]\n"
    "v_exp_f32 v194, v194\n"
    "v_mfma_f32_16x16x32_bf16 v[8:11], a[100:103], v[152:155], v[8:11]\n"
    "ds_read_b128 a[88:91], v228 offset:53760\n"
    "v_add_f32 v224, v224, v194\n"
    "v_mfma_f32_16x16x32_bf16 v[12:15], a[100:103], v[156:159], v[12:15]\n"
    "v_exp_f32 v195, v195\n"
    "v_mfma_f32_16x16x32_bf16 v[12:15], a[96:99], v[140:143], v[12:15]\n"
    "v_cvt_pk_bf16_f32 v160, v192, v193\n"
    "v_add_f32 v224, v224, v195\n"
    "v_mfma_f32_16x16x32_bf16 v[200:203], a[64:67], a[32:35], v[216:219]\n"
    "v_exp_f32 v196, v196\n"
    "v_mfma_f32_16x16x32_bf16 v[200:203], a[68:71], a[36:39], v[200:203]\n"
    "ds_read_b64_tr_b16 a[96:97], v229 offset:33024\n"
    "v_cvt_pk_bf16_f32 v161, v194, v195\n"
    "v_mfma_f32_16x16x32_bf16 v[200:203], a[72:75], a[40:43], v[200:203]\n"
    "s_mov_b32 m0, s56\n"
    "v_exp_f32 v197, v197\n"
    "v_mfma_f32_16x16x32_bf16 v[200:203], a[76:79], a[44:47], v[200:203]\n"
    "buffer_load_dwordx4 v232, s[16:19], s58 offen offset:3072 nt lds\n"
    "v_mfma_f32_16x16x32_bf16 v[204:207], a[76:79], a[60:63], v[220:223]\n"
    "v_exp_f32 v198, v198\n"
    "v_mfma_f32_16x16x32_bf16 v[204:207], a[72:75], a[56:59], v[204:207]\n"
    "s_add_u32 s58, s58, 16384\n"
    "ds_read_b64_tr_b16 a[98:99], v229 offset:37120\n"
    "v_cvt_pk_bf16_f32 v164, v196, v197\n"
    "v_mfma_f32_16x16x32_bf16 v[204:207], a[68:71], a[52:55], v[204:207]\n"
    "s_and_b32 s58, s58, 0x3ffff\n"
    "v_exp_f32 v199, v199\n"
    "v_mfma_f32_16x16x32_bf16 v[204:207], a[64:67], a[48:51], v[204:207]\n"
    "s_waitcnt lgkmcnt(6)\n"
    "ds_read_b128 a[92:95], v228 offset:54016\n"
    "v_cvt_pk_bf16_f32 v165, v198, v199\n"
    "v_mfma_f32_16x16x32_bf16 v[16:19], a[104:107], v[128:131], v[16:19]\n"
    "s_waitcnt lgkmcnt(4)\n"
    "v_exp_f32 v200, v200\n"
    "v_mfma_f32_16x16x32_bf16 v[16:19], a[108:111], v[144:147], v[16:19]\n"
    "s_add_u32 m0, s51, 131072\n"
    "ds_read_b64_tr_b16 a[100:101], v229 offset:41216\n"
    "v_add_f32 v226, v226, v200\n"
    "v_mfma_f32_16x16x32_bf16 v[20:23], a[108:111], v[148:151], v[20:23]\n"
    "buffer_load_dwordx4 v235, s[24:27], s53 offen offset:1024  lds\n"
    "v_exp_f32 v201, v201\n"
    "v_mfma_f32_16x16x32_bf16 v[20:23], a[104:107], v[132:135], v[20:23]\n"
    "s_add_u32 s56, s56, 16384\n"
    "ds_read_b64_tr_b16 a[102:103], v229 offset:45312\n"
    "v_add_f32 v226, v226, v201\n"
    "v_mfma_f32_16x16x32_bf16 v[24:27], a[104:107], v[136:139], v[24:27]\n"
    "s_and_b32 s56, s56, 0xffff\n"
    "v_exp_f32 v202, v202\n"
    "v_mfma_f32_16x16x32_bf16 v[24:27], a[108:111], v[152:155], v[24:27]\n"
    "ds_read_b128 a[64:67], v228 offset:57344\n"
    "v_add_f32 v226, v226, v202\n"
    "v_mfma_f32_16x16x32_bf16 v[28:31], a[108:111], v[156:159], v[28:31]\n"
    "v_exp_f32 v203, v203\n"
    "v_mfma_f32_16x16x32_bf16 v[28:31], a[104:107], v[140:143], v[28:31]\n"
    "v_cvt_pk_bf16_f32 v168, v200, v201\n"
    "v_add_f32 v226, v226, v203\n"
    "v_mfma_f32_16x16x32_bf16 v[192:195], a[80:83], a[0:3], v[208:211]\n"
    "s_mov_b32 m0, s57\n"
    "v_exp_f32 v204, v204\n"
    "v_mfma_f32_16x16x32_bf16 v[192:195], a[84:87], a[4:7], v[192:195]\n"
    "s_waitcnt lgkmcnt(6)\n"
    "ds_read_b64_tr_b16 a[104:105], v230 offset:33024\n"
    "v_cvt_pk_bf16_f32 v169, v202, v203\n"
    "v_mfma_f32_16x16x32_bf16 v[192:195], a[88:91], a[8:11], v[192:195]\n"
    "s_waitcnt lgkmcnt(4)\n"
    "v_exp_f32 v205, v205\n"
    "v_mfma_f32_16x16x32_bf16 v[192:195], a[92:95], a[12:15], v[192:195]\n"
    "buffer_load_dwordx4 v231, s[20:23], s59 offen offset:0 nt lds\n"
    "v_mfma_f32_16x16x32_bf16 v[196:199], a[92:95], a[28:31], v[212:215]\n"
    "v_exp_f32 v206, v206\n"
    "v_mfma_f32_16x16x32_bf16 v[196:199], a[88:91], a[24:27], v[196:199]\n"
    "ds_read_b64_tr_b16 a[106:107], v230 offset:37120\n"
    "v_cvt_pk_bf16_f32 v172, v204, v205\n"
    "v_mfma_f32_16x16x32_bf16 v[196:199], a[84:87], a[20:23], v[196:199]\n"
    "v_exp_f32 v207, v207\n"
    "v_mfma_f32_16x16x32_bf16 v[196:199], a[80:83], a[16:19], v[196:199]\n"
    "ds_read_b128 a[68:71], v228 offset:57600\n"
    "v_cvt_pk_bf16_f32 v173, v206, v207\n"
    "v_mfma_f32_16x16x32_bf16 a[116:119], a[112:115], v[132:135], a[116:119]\n"
    "s_waitcnt vmcnt(9)\n"
    "ds_read_b64_tr_b16 a[130:131], v242 offset:16384\n"
    "ds_read_b64_tr_b16 a[134:135], v243 offset:16384\n"
    "v_mfma_f32_16x16x32_bf16 a[116:119], a[112:115], v[148:151], a[116:119]\n"
    "ds_read_b64_tr_b16 a[138:139], v242 offset:16640\n"
    "ds_read_b64_tr_b16 a[142:143], v243 offset:16640\n"
    "v_mfma_f32_16x16x32_bf16 v[32:35], a[96:99], v[128:131], v[32:35]\n"
    "s_waitcnt lgkmcnt(8)\n"
    "v_exp_f32 v192, v192\n"
    "v_mfma_f32_16x16x32_bf16 v[32:35], a[100:103], v[144:147], v[32:35]\n"
    "s_add_u32 m0, s51, 131072\n"
    "ds_read_b64_tr_b16 a[108:109], v230 offset:41216\n"
    "v_add_f32 v224, v224, v192\n"
    "v_mfma_f32_16x16x32_bf16 v[36:39], a[100:103], v[148:151], v[36:39]\n"
    "buffer_load_dwordx4 v236, s[24:27], s53 offen offset:2048  lds\n"
    "v_exp_f32 v193, v193\n"
    "v_mfma_f32_16x16x32_bf16 v[36:39], a[96:99], v[132:135], v[36:39]\n"
    "ds_read_b64_tr_b16 a[110:111], v230 offset:45312\n"
    "v_add_f32 v224, v224, v193\n"
    "v_mfma_f32_16x16x32_bf16 v[40:43], a[96:99], v[136:139], v[40:43]\n"
    "v_exp_f32 v194, v194\n"
    "v_mfma_f32_16x16x32_bf16 v[40:43], a[100:103], v[152:155], v[40:43]\n"
    "ds_read_b128 a[72:75], v228 offset:57856\n"
    "v_add_f32 v224, v224, v194\n"
    "v_mfma_f32_16x16x32_bf16 v[44:47], a[100:103], v[156:159], v[44:47]\n"
    "v_exp_f32 v195, v195\n"
    "v_mfma_f32_16x16x32_bf16 v[44:47], a[96:99], v[140:143], v[44:47]\n"
    "v_cvt_pk_bf16_f32 v162, v192, v193\n"
    "v_add_f32 v224, v224, v195\n"
    "v_mfma_f32_16x16x32_bf16 v[200:203], a[80:83], a[32:35], v[216:219]\n"
    "v_exp_f32 v196, v196\n"
    "v_mfma_f32_16x16x32_bf16 v[200:203], a[84:87], a[36:39], v[200:203]\n"
    "ds_read_b64_tr_b16 a[96:97], v229 offset:33280\n"
    "v_cvt_pk_bf16_f32 v163, v194, v195\n"
    "v_mfma_f32_16x16x32_bf16 v[200:203], a[88:91], a[40:43], v[200:203]\n"
    "s_mov_b32 m0, s57\n"
    "v_exp_f32 v197, v197\n"
    "v_mfma_f32_16x16x32_bf16 v[200:203], a[92:95], a[44:47], v[200:203]\n"
    "buffer_load_dwordx4 v232, s[20:23], s59 offen offset:1024 nt lds\n"
    "v_mfma_f32_16x16x32_bf16 v[204:207], a[92:95], a[60:63], v[220:223]\n"
    "v_exp_f32 v198, v198\n"
    "v_mfma_f32_16x16x32_bf16 v[204:207], a[88:91], a[56:59], v[204:207]\n"
    "ds_read_b64_tr_b16 a[98:99], v229 offset:37376\n"
    "v_cvt_pk_bf16_f32 v166, v196, v197\n"
    "v_mfma_f32_16x16x32_bf16 v[204:207], a[84:87], a[52:55], v[204:207]\n"
    "v_exp_f32 v199, v199\n"
    "v_mfma_f32_16x16x32_bf16 v[204:207], a[80:83], a[48:51], v[204:207]\n"
    "s_waitcnt lgkmcnt(10)\n"
    "ds_read_b128 a[76:79], v228 offset:58112\n"
    "v_cvt_pk_bf16_f32 v167, v198, v199\n"
    "v_mfma_f32_16x16x32_bf16 v[48:51], a[104:107], v[128:131], v[48:51]\n"
    "s_waitcnt lgkmcnt(4)\n"
    "v_exp_f32 v200, v200\n"
    "v_mfma_f32_16x16x32_bf16 v[48:51], a[108:111], v[144:147], v[48:51]\n"
    "s_add_u32 m0, s51, 131072\n"
    "ds_read_b64_tr_b16 a[100:101], v229 offset:41472\n"
    "v_add_f32 v226, v226, v200\n"
    "v_mfma_f32_16x16x32_bf16 v[52:55], a[108:111], v[148:151], v[52:55]\n"
    "s_sub_u32 s11, s60, s8\n"
    "buffer_load_dwordx4 v237, s[24:27], s53 offen offset:3072  lds\n"
    "v_exp_f32 v201, v201\n"
    "v_mfma_f32_16x16x32_bf16 v[52:55], a[104:107], v[132:135], v[52:55]\n"
    "s_add_u32 s60, s60, 142\n"
    "ds_read_b64_tr_b16 a[102:103], v229 offset:45568\n"
    "v_add_f32 v226, v226, v201\n"
    "v_mfma_f32_16x16x32_bf16 v[56:59], a[104:107], v[136:139], v[56:59]\n"
    "s_cmp_gt_i32 s11, 2\n"
    "v_exp_f32 v202, v202\n"
    "v_mfma_f32_16x16x32_bf16 v[56:59], a[108:111], v[152:155], v[56:59]\n"
    "s_cbranch_scc1 Lpa2x%=\n"
    "Lpb2x%=:\n"
    "ds_read_b128 a[80:83], v228 offset:61440\n"
    "v_add_f32 v226, v226, v202\n"
    "v_mfma_f32_16x16x32_bf16 v[60:63], a[108:111], v[156:159], v[60:63]\n"
    "v_exp_f32 v203, v203\n"
    "v_mfma_f32_16x16x32_bf16 v[60:63], a[104:107], v[140:143], v[60:63]\n"
    "v_cvt_pk_bf16_f32 v170, v200, v201\n"
    "v_add_f32 v226, v226, v203\n"
    "v_mfma_f32_16x16x32_bf16 v[192:195], a[64:67], a[0:3], v[208:211]\n"
    "s_mov_b32 m0, s57\n"
    "v_exp_f32 v204, v204\n"
    "v_mfma_f32_16x16x32_bf16 v[192:195], a[68:71], a[4:7], v[192:195]\n"
    "s_waitcnt lgkmcnt(6)\n"
    "ds_read_b64_tr_b16 a[104:105], v230 offset:33280\n"
    "v_cvt_pk_bf16_f32 v171, v202, v203\n"
    "v_mfma_f32_16x16x32_bf16 v[192:195], a[72:75], a[8:11], v[192:195]\n"
    "s_waitcnt lgkmcnt(4)\n"
    "v_exp_f32 v205, v205\n"
    "v_mfma_f32_16x16x32_bf16 v[192:195], a[76:79], a[12:15], v[192:195]\n"
    "buffer_load_dwordx4 v231, s[20:23], s59 offen offset:2048 nt lds\n"
    "v_mfma_f32_16x16x32_bf16 v[196:199], a[76:79], a[28:31], v[212:215]\n"
    "v_exp_f32 v206, v206\n"
    "v_mfma_f32_16x16x32_bf16 v[196:199], a[72:75], a[24:27], v[196:199]\n"
    "ds_read_b64_tr_b16 a[106:107], v230 offset:37376\n"
    "v_cvt_pk_bf16_f32 v174, v204, v205\n"
    "v_mfma_f32_16x16x32_bf16 v[196:199], a[68:71], a[20:23], v[196:199]\n"
    "v_exp_f32 v207, v207\n"
    "v_mfma_f32_16x16x32_bf16 v[196:199], a[64:67], a[16:19], v[196:199]\n"
    "ds_read_b128 a[84:87], v228 offset:61696\n"
    "v_cvt_pk_bf16_f32 v175, v206, v207\n"
    "v_mfma_f32_16x16x32_bf16 v[64:67], a[96:99], v[128:131], v[64:67]\n"
    "s_waitcnt lgkmcnt(4)\n"
    "v_exp_f32 v192, v192\n"
    "v_mfma_f32_16x16x32_bf16 v[64:67], a[100:103], v[144:147], v[64:67]\n"
    "ds_read_b64_tr_b16 a[108:109], v230 offset:41472\n"
    "v_add_f32 v224, v224, v192\n"
    "v_mfma_f32_16x16x32_bf16 v[68:71], a[100:103], v[148:151], v[68:71]\n"
    "v_exp_f32 v193, v193\n"
    "v_mfma_f32_16x16x32_bf16 v[68:71], a[96:99], v[132:135], v[68:71]\n"
    "ds_read_b64_tr_b16 a[110:111], v230 offset:45568\n"
    "v_add_f32 v224, v224, v193\n"
    "v_mfma_f32_16x16x32_bf16 v[72:75], a[96:99], v[136:139], v[72:75]\n"
    "v_exp_f32 v194, v194\n"
    "v_mfma_f32_16x16x32_bf16 v[72:75], a[100:103], v[152:155], v[72:75]\n"
    "ds_read_b128 a[88:91], v228 offset:61952\n"
    "v_add_f32 v224, v224, v194\n"
    "v_mfma_f32_16x16x32_bf16 v[76:79], a[100:103], v[156:159], v[76:79]\n"
    "v_exp_f32 v195, v195\n"
    "v_mfma_f32_16x16x32_bf16 v[76:79], a[96:99], v[140:143], v[76:79]\n"
    "v_cvt_pk_bf16_f32 v176, v192, v193\n"
    "v_add_f32 v224, v224, v195\n"
    "v_mfma_f32_16x16x32_bf16 v[200:203], a[64:67], a[32:35], v[216:219]\n"
    "v_exp_f32 v196, v196\n"
    "v_mfma_f32_16x16x32_bf16 v[200:203], a[68:71], a[36:39], v[200:203]\n"
    "ds_read_b64_tr_b16 a[96:97], v229 offset:33536\n"
    "v_cvt_pk_bf16_f32 v177, v194, v195\n"
    "v_mfma_f32_16x16x32_bf16 v[200:203], a[72:75], a[40:43], v[200:203]\n"
    "v_exp_f32 v197, v197\n"
    "v_mfma_f32_16x16x32_bf16 v[200:203], a[76:79], a[44:47], v[200:203]\n"
    "buffer_load_dwordx4 v232, s[20:23], s59 offen offset:3072 nt lds\n"
    "v_mfma_f32_16x16x32_bf16 v[204:207], a[76:79], a[60:63], v[220:223]\n"
    "v_exp_f32 v198, v198\n"
    "v_mfma_f32_16x16x32_bf16 v[204:207], a[72:75], a[56:59], v[204:207]\n"
    "s_add_u32 s59, s59, 16384\n"
    "ds_read_b64_tr_b16 a[98:99], v229 offset:37632\n"
    "v_cvt_pk_bf16_f32 v180, v196, v197\n"
    "v_mfma_f32_16x16x32_bf16 v[204:207], a[68:71], a[52:55], v[204:207]\n"
    "s_and_b32 s59, s59, 0x3ffff\n"
    "v_exp_f32 v199, v199\n"
    "v_mfma_f32_16x16x32_bf16 v[204:207], a[64:67], a[48:51], v[204:207]\n"
    "ds_read_b128 a[92:95], v228 offset:62208\n"
    "v_cvt_pk_bf16_f32 v181, v198, v199\n"
    "v_mfma_f32_16x16x32_bf16 a[120:123], a[112:115], v[140:143], a[120:123]\n"
    "ds_read_b64_tr_b16 a[146:147], v242 offset:16896\n"
    "ds_read_b64_tr_b16 a[150:151], v243 offset:16896\n"
    "v_mfma_f32_16x16x32_bf16 a[120:123], a[112:115], v[156:159], a[120:123]\n"
    "s_waitcnt lgkmcnt(9)\n"
    "ds_read_b64_tr_b16 a[154:155], v242 offset:17152\n"
    "ds_read_b64_tr_b16 a[158:159], v243 offset:17152\n"
    "v_mfma_f32_16x16x32_bf16 v[80:83], a[104:107], v[128:131], v[80:83]\n"
    "s_waitcnt lgkmcnt(8)\n"
    "v_exp_f32 v200, v200\n"
    "v_mfma_f32_16x16x32_bf16 v[80:83], a[108:111], v[144:147], v[80:83]\n"
    "s_add_u32 s57, s57, 16384\n"
    "ds_read_b64_tr_b16 a[100:101], v229 offset:41728\n"
    "v_add_f32 v226, v226, v200\n"
    "v_mfma_f32_16x16x32_bf16 v[84:87], a[108:111], v[148:151], v[84:87]\n"
    "s_bitset0_b32 s57, 17\n"
    "v_exp_f32 v201, v201\n"
    "v_mfma_f32_16x16x32_bf16 v[84:87], a[104:107], v[132:135], v[84:87]\n"
    "ds_read_b64_tr_b16 a[102:103], v229 offset:45824\n"
    "Lpg5%=:\n"
    "s_cbranch_execz Lwb0%=\n"
    "v_add_f32 v226, v226, v201\n"
    "v_mfma_f32_16x16x32_bf16 v[88:91], a[104:107], v[136:139], v[88:91]\n"
    "s_waitcnt vmcnt(12) lgkmcnt(6)\n"
    "v_exp_f32 v202, v202\n"
    "v_mfma_f32_16x16x32_bf16 v[88:91], a[108:111], v[152:155], v[88:91]\n"
    "s_barrier\n"
    "ds_read_b128 a[64:67], v228 offset:0\n"
    "v_add_f32 v226, v226, v202\n"
    "v_mfma_f32_16x16x32_bf16 v[92:95], a[108:111], v[156:159], v[92:95]\n"
    "s_bitset1_b32 s57, 16\n"
    "v_exp_f32 v203, v203\n"
    "v_mfma_f32_16x16x32_bf16 v[92:95], a[104:107], v[140:143], v[92:95]\n"
    "v_cvt_pk_bf16_f32 v184, v200, v201\n"
    "v_add_f32 v226, v226, v203\n"
    "v_mfma_f32_16x16x32_bf16 v[192:195], a[80:83], a[0:3], v[208:211]\n"
    "s_mov_b32 m0, s56\n"
    "v_exp_f32 v204, v204\n"
    "v_mfma_f32_16x16x32_bf16 v[192:195], a[84:87], a[4:7], v[192:195]\n"
    "ds_read_b64_tr_b16 a[104:105], v230 offset:33536\n"
    "v_cvt_pk_bf16_f32 v185, v202, v203\n"
    "v_mfma_f32_16x16x32_bf16 v[192:195], a[88:91], a[8:11], v[192:195]\n"
    "v_exp_f32 v205, v205\n"
    "v_mfma_f32_16x16x32_bf16 v[192:195], a[92:95], a[12:15], v[192:195]\n"
    "buffer_load_dwordx4 v231, s[16:19], s58 offen offset:0 nt lds\n"
    "v_mfma_f32_16x16x32_bf16 v[196:199], a[92:95], a[28:31], v[212:215]\n"
    "v_exp_f32 v206, v206\n"
    "v_mfma_f32_16x16x32_bf16 v[196:199], a[88:91], a[24:27], v[196:199]\n"
    "ds_read_b64_tr_b16 a[106:107], v230 offset:37632\n"
    "v_cvt_pk_bf16_f32 v188, v204, v205\n"
    "v_mfma_f32_16x16x32_bf16 v[196:199], a[84:87], a[20:23], v[196:199]\n"
    "v_exp_f32 v207, v207\n"
    "v_mfma_f32_16x16x32_bf16 v[196:199], a[80:83], a[16:19], v[196:199]\n"
    "ds_read_b128 a[68:71], v228 offset:256\n"
    "v_cvt_pk_bf16_f32 v189, v206, v207\n"
    "v_mfma_f32_16x16x32_bf16 v[96:99], a[96:99], v[128:131], v[96:99]\n"
    "s_waitcnt lgkmcnt(4)\n"
    "v_exp_f32 v192, v192\n"
    "v_mfma_f32_16x16x32_bf16 v[96:99], a[100:103], v[144:147], v[96:99]\n"
    "ds_read_b64_tr_b16 a[108:109], v230 offset:41728\n"
    "v_add_f32 v224, v224, v192\n"
    "v_mfma_f32_16x16x32_bf16 v[100:103], a[100:103], v[148:151], v[100:103]\n"
    "v_exp_f32 v193, v193\n"
    "v_mfma_f32_16x16x32_bf16 v[100:103], a[96:99], v[132:135], v[100:103]\n"
    "ds_read_b64_tr_b16 a[110:111], v230 offset:45824\n"
    "v_add_f32 v224, v224, v193\n"
    "v_mfma_f32_16x16x32_bf16 v[104:107], a[96:99], v[136:139], v[104:107]\n"
    "v_exp_f32 v194, v194\n"
    "v_mfma_f32_16x16x32_bf16 v[104:107], a[100:103], v[152:155], v[104:107]\n"
    "ds_read_b128 a[72:75], v228 offset:512\n"
    "v_add_f32 v224, v224, v194\n"
    "v_mfma_f32_16x16x32_bf16 v[108:111], a[100:103], v[156:159], v[108:111]\n"
    "v_exp_f32 v195, v195\n"
    "v_mfma_f32_16x16x32_bf16 v[108:111], a[96:99], v[140:143], v[108:111]\n"
    "v_cvt_pk_bf16_f32 v178, v192, v193\n"
    "v_add_f32 v224, v224, v195\n"
    "v_mfma_f32_16x16x32_bf16 v[200:203], a[80:83], a[32:35], v[216:219]\n"
    "v_exp_f32 v196, v196\n"
    "v_mfma_f32_16x16x32_bf16 v[200:203], a[84:87], a[36:39], v[200:203]\n"
    "ds_read_b64_tr_b16 a[96:97], v229 offset:49152\n"
    "v_cvt_pk_bf16_f32 v179, v194, v195\n"
    "v_mfma_f32_16x16x32_bf16 v[200:203], a[88:91], a[40:43], v[200:203]\n"
    "v_exp_f32 v197, v197\n"
    "v_mfma_f32_16x16x32_bf16 v[200:203], a[92:95], a[44:47], v[200:203]\n"
    "buffer_load_dwordx4 v232, s[16:19], s58 offen offset:1024 nt lds\n"
    "v_mfma_f32_16x16x32_bf16 v[204:207], a[92:95], a[60:63], v[220:223]\n"
    "v_exp_f32 v198, v198\n"
    "v_mfma_f32_16x16x32_bf16 v[204:207], a[88:91], a[56:59], v[204:207]\n"
    "ds_read_b64_tr_b16 a[98:99], v229 offset:53248\n"
    "v_cvt_pk_bf16_f32 v182, v196, v197\n"
    "v_mfma_f32_16x16x32_bf16 v[204:207], a[84:87], a[52:55], v[204:207]\n"
    "v_exp_f32 v199, v199\n"
    "v_mfma_f32_16x16x32_bf16 v[204:207], a[80:83], a[48:51], v[204:207]\n"
    "s_waitcnt lgkmcnt(6)\n"
    "ds_read_b128 a[76:79], v228 offset:768\n"
    "v_cvt_pk_bf16_f32 v183, v198, v199\n"
    "v_mfma_f32_16x16x32_bf16 v[112:115], a[104:107], v[128:131], v[112:115]\n"
    "s_waitcnt lgkmcnt(4)\n"
    "v_exp_f32 v200, v200\n"
    "v_mfma_f32_16x16x32_bf16 v[112:115], a[108:111], v[144:147], v[112:115]\n"
    "ds_read_b64_tr_b16 a[100:101], v229 offset:57344\n"
    "v_add_f32 v226, v226, v200\n"
    "v_mfma_f32_16x16x32_bf16 v[116:119], a[108:111], v[148:151], v[116:119]\n"
    "v_exp_f32 v201, v201\n"
    "v_mfma_f32_16x16x32_bf16 v[116:119], a[104:107], v[132:135], v[116:119]\n"
    "ds_read_b64_tr_b16 a[102:103], v229 offset:61440\n"
    "v_add_f32 v226, v226, v201\n"
    "v_mfma_f32_16x16x32_bf16 v[120:123], a[104:107], v[136:139], v[120:123]\n"
    "v_exp_f32 v202, v202\n"
    "v_mfma_f32_16x16x32_bf16 v[120:123], a[108:111], v[152:155], v[120:123]\n"
    "ds_read_b128 a[80:83], v228 offset:4096\n"
    "v_add_f32 v226, v226, v202\n"
    "v_mfma_f32_16x16x32_bf16 v[124:127], a[108:111], v[156:159], v[124:127]\n"
    "v_exp_f32 v203, v203\n"
    "v_mfma_f32_16x16x32_bf16 v[124:127], a[104:107], v[140:143], v[124:127]\n"
    "v_cvt_pk_bf16_f32 v186, v200, v201\n"
    "v_add_f32 v226, v226, v203\n"
    "v_mfma_f32_16x16x32_bf16 v[192:195], a[64:67], a[0:3], v[208:211]\n"
    "s_mov_b32 m0, s56\n"
    "v_exp_f32 v204, v204\n"
    "v_mfma_f32_16x16x32_bf16 v[192:195], a[68:71], a[4:7], v[192:195]\n"
    "s_waitcnt lgkmcnt(6)\n"
    "ds_read_b64_tr_b16 a[104:105], v230 offset:49152\n"
    "v_cvt_pk_bf16_f32 v187, v202, v203\n"
    "v_mfma_f32_16x16x32_bf16 v[192:195], a[72:75], a[8:11], v[192:195]\n"
    "s_waitcnt lgkmcnt(4)\n"
    "v_exp_f32 v205, v205\n"
    "v_mfma_f32_16x16x32_bf16 v[192:195], a[76:79], a[12:15], v[192:195]\n"
    "s_nop 0\n"
    "buffer_load_dwordx4 v231, s[16:19], s58 offen offset:2048 nt lds\n"
    "v_mfma_f32_16x16x32_bf16 v[196:199], a[76:79], a[28:31], v[212:215]\n"
    "v_exp_f32 v206, v206\n"
    "v_mfma_f32_16x16x32_bf16 v[196:199], a[72:75], a[24:27], v[196:199]\n"
    "ds_read_b64_tr_b16 a[106:107], v230 offset:53248\n"
    "v_cvt_pk_bf16_f32 v190, v204, v205\n"
    "v_mfma_f32_16x16x32_bf16 v[196:199], a[68:71], a[20:23], v[196:199]\n"
    "s_add_u32 m0, s51, 147456\n"
    "v_exp_f32 v207, v207\n"
    "v_mfma_f32_16x16x32_bf16 v[196:199], a[64:67], a[16:19], v[196:199]\n"
    "ds_read_b128 a[84:87], v228 offset:4352\n"
    "v_cvt_pk_bf16_f32 v191, v206, v207\n"
    "v_mfma_f32_16x16x32_bf16 v[0:3], a[96:99], v[160:163], v[0:3]\n"
    "s_waitcnt lgkmcnt(4)\n"
    "v_exp_f32 v192, v192\n"
    "v_mfma_f32_16x16x32_bf16 v[0:3], a[100:103], v[176:179], v[0:3]\n"
    "s_xor_b32 s53, s88, 49152\n"
    "ds_read_b64_tr_b16 a[108:109], v230 offset:57344\n"
    "v_add_f32 v224, v224, v192\n"
    "v_mfma_f32_16x16x32_bf16 v[4:7], a[100:103], v[180:183], v[4:7]\n"
    "s_memrealtime s[8:9]\n"
    "buffer_load_dwordx4 v234, s[24:27], s53 offen offset:0  lds\n"
    "v_exp_f32 v193, v193\n"
    "v_mfma_f32_16x16x32_bf16 v[4:7], a[96:99], v[164:167], v[4:7]\n"
    "ds_read_b64_tr_b16 a[110:111], v230 offset:61440\n"
    "v_add_f32 v224, v224, v193\n"
    "v_mfma_f32_16x16x32_bf16 v[8:11], a[96:99], v[168:171], v[8:11]\n"
    "v_exp_f32 v194, v194\n"
    "v_mfma_f32_16x16x32_bf16 v[8:11], a[100:103], v[184:187], v[8:11]\n"
    "ds_read_b128 a[88:91], v228 offset:4608\n"
    "v_add_f32 v224, v224, v194\n"
    "v_mfma_f32_16x16x32_bf16 v[12:15], a[100:103], v[188:191], v[12:15]\n"
    "v_exp_f32 v195, v195\n"
    "v_mfma_f32_16x16x32_bf16 v[12:15], a[96:99], v[172:175], v[12:15]\n"
    "v_cvt_pk_bf16_f32 v128, v192, v193\n"
    "v_add_f32 v224, v224, v195\n"
    "v_mfma_f32_16x16x32_bf16 v[200:203], a[64:67], a[32:35], v[216:219]\n"
    "v_exp_f32 v196, v196\n"
    "v_mfma_f32_16x16x32_bf16 v[200:203], a[68:71], a[36:39], v[200:203]\n"
    "ds_read_b64_tr_b16 a[96:97], v229 offset:49408\n"
    "v_cvt_pk_bf16_f32 v129, v194, v195\n"
    "v_mfma_f32_16x16x32_bf16 v[200:203], a[72:75], a[40:43], v[200:203]\n"
    "s_mov_b32 m0, s56\n"
    "v_exp_f32 v197, v197\n"
    "v_mfma_f32_16x16x32_bf16 v[200:203], a[76:79], a[44:47], v[200:203]\n"
    "buffer_load_dwordx4 v232, s[16:19], s58 offen offset:3072 nt lds\n"
    "v_mfma_f32_16x16x32_bf16 v[204:207], a[76:79], a[60:63], v[220:223]\n"
    "v_exp_f32 v198, v198\n"
    "v_mfma_f32_16x16x32_bf16 v[204:207], a[72:75], a[56:59], v[204:207]\n"
    "s_add_u32 s58, s58, 16384\n"
    "ds_read_b64_tr_b16 a[98:99], v229 offset:53504\n"
    "v_cvt_pk_bf16_f32 v132, v196, v197\n"
    "v_mfma_f32_16x16x32_bf16 v[204:207], a[68:71], a[52:55], v[204:207]\n"
    "s_and_b32 s58, s58, 0x3ffff\n"
    "v_exp_f32 v199, v199\n"
    "v_mfma_f32_16x16x32_bf16 v[204:207], a[64:67], a[48:51], v[204:207]\n"
    "s_waitcnt lgkmcnt(6)\n"
    "ds_read_b128 a[92:95], v228 offset:4864\n"
    "v_cvt_pk_bf16_f32 v133, v198, v199\n"
    "v_mfma_f32_16x16x32_bf16 v[16:19], a[104:107], v[160:163], v[16:19]\n"
    "s_waitcnt lgkmcnt(4)\n"
    "v_exp_f32 v200, v200\n"
    "v_mfma_f32_16x16x32_bf16 v[16:19], a[108:111], v[176:179], v[16:19]\n"
    "s_add_u32 m0, s51, 147456\n"
    "ds_read_b64_tr_b16 a[100:101], v229 offset:57600\n"
    "v_add_f32 v226, v226, v200\n"
    "v_mfma_f32_16x16x32_bf16 v[20:23], a[108:111], v[180:183], v[20:23]\n"
    "buffer_load_dwordx4 v235, s[24:27], s53 offen offset:1024  lds\n"
    "v_exp_f32 v201, v201\n"
    "v_mfma_f32_16x16x32_bf16 v[20:23], a[104:107], v[164:167], v[20:23]\n"
    "s_add_u32 s56, s56, 16384\n"
    "ds_read_b64_tr_b16 a[102:103], v229 offset:61696\n"
    "v_add_f32 v226, v226, v201\n"
    "v_mfma_f32_16x16x32_bf16 v[24:27], a[104:107], v[168:171], v[24:27]\n"
    "s_and_b32 s56, s56, 0xffff\n"
    "v_exp_f32 v202, v202\n"
    "v_mfma_f32_16x16x32_bf16 v[24:27], a[108:111], v[184:187], v[24:27]\n"
    "ds_read_b128 a[64:67], v228 offset:8192\n"
    "v_add_f32 v226, v226, v202\n"
    "v_mfma_f32_16x16x32_bf16 v[28:31], a[108:111], v[188:191], v[28:31]\n"
    "v_exp_f32 v203, v203\n"
    "v_mfma_f32_16x16x32_bf16 v[28:31], a[104:107], v[172:175], v[28:31]\n"
    "v_cvt_pk_bf16_f32 v136, v200, v201\n"
    "v_add_f32 v226, v226, v203\n"
    "v_mfma_f32_16x16x32_bf16 v[192:195], a[80:83], a[0:3], v[208:211]\n"
    "s_mov_b32 m0, s57\n"
    "v_exp_f32 v204, v204\n"
    "v_mfma_f32_16x16x32_bf16 v[192:195], a[84:87], a[4:7], v[192:195]\n"
    "s_waitcnt lgkmcnt(6)\n"
    "ds_read_b64_tr_b16 a[104:105], v230 offset:49408\n"
    "v_cvt_pk_bf16_f32 v137, v202, v203\n"
    "v_mfma_f32_16x16x32_bf16 v[192:195], a[88:91], a[8:11], v[192:195]\n"
    "s_waitcnt lgkmcnt(4)\n"
    "v_exp_f32 v205, v205\n"
    "v_mfma_f32_16x16x32_bf16 v[192:195], a[92:95], a[12:15], v[192:195]\n"
    "buffer_load_dwordx4 v231, s[20:23], s59 offen offset:0 nt lds\n"
    "v_mfma_f32_16x16x32_bf16 v[196:199], a[92:95], a[28:31], v[212:215]\n"
    "v_exp_f32 v206, v206\n"
    "v_mfma_f32_16x16x32_bf16 v[196:199], a[88:91], a[24:27], v[196:199]\n"
    "ds_read_b64_tr_b16 a[106:107], v230 offset:53504\n"
    "v_cvt_pk_bf16_f32 v140, v204, v205\n"
    "v_mfma_f32_16x16x32_bf16 v[196:199], a[84:87], a[20:23], v[196:199]\n"
    "v_exp_f32 v207, v207\n"
    "v_mfma_f32_16x16x32_bf16 v[196:199], a[80:83], a[16:19], v[196:199]\n"
    "ds_read_b128 a[68:71], v228 offset:8448\n"
    "v_cvt_pk_bf16_f32 v141, v206, v207\n"
    "v_mfma_f32_16x16x32_bf16 a[116:119], a[112:115], v[164:167], a[116:119]\n"
    "s_waitcnt vmcnt(9)\n"
    "ds_read_b64_tr_b16 a[160:161], v242 offset:0\n"
    "ds_read_b64_tr_b16 a[164:165], v243 offset:0\n"
    "v_mfma_f32_16x16x32_bf16 a[116:119], a[112:115], v[180:183], a[116:119]\n"
    "ds_read_b64_tr_b16 a[168:169], v242 offset:256\n"
    "ds_read_b64_tr_b16 a[172:173], v243 offset:256\n"
    "v_mfma_f32_16x16x32_bf16 v[32:35], a[96:99], v[160:163], v[32:35]\n"
    "s_waitcnt lgkmcnt(8)\n"
    "v_exp_f32 v192, v192\n"
    "v_mfma_f32_16x16x32_bf16 v[32:35], a[100:103], v[176:179], v[32:35]\n"
    "s_add_u32 m0, s51, 147456\n"
    "ds_read_b64_tr_b16 a[108:109], v230 offset:57600\n"
    "v_add_f32 v224, v224, v192\n"
    "v_mfma_f32_16x16x32_bf16 v[36:39], a[100:103], v[180:183], v[36:39]\n"
    "buffer_load_dwordx4 v236, s[24:27], s53 offen offset:2048  lds\n"
    "v_exp_f32 v193, v193\n"
    "v_mfma_f32_16x16x32_bf16 v[36:39], a[96:99], v[164:167], v[36:39]\n"
    "ds_read_b64_tr_b16 a[110:111], v230 offset:61696\n"
    "v_add_f32 v224, v224, v193\n"
    "v_mfma_f32_16x16x32_bf16 v[40:43], a[96:99], v[168:171], v[40:43]\n"
    "v_exp_f32 v194, v194\n"
    "v_mfma_f32_16x16x32_bf16 v[40:43], a[100:103], v[184:187], v[40:43]\n"
    "ds_read_b128 a[72:75], v228 offset:8704\n"
    "v_add_f32 v224, v224, v194\n"
    "v_mfma_f32_16x16x32_bf16 v[44:47], a[100:103], v[188:191], v[44:47]\n"
    "v_exp_f32 v195, v195\n"
    "v_mfma_f32_16x16x32_bf16 v[44:47], a[96:99], v[172:175], v[44:47]\n"
    "v_cvt_pk_bf16_f32 v130, v192, v193\n"
    "v_add_f32 v224, v224, v195\n"
    "v_mfma_f32_16x16x32_bf16 v[200:203], a[80:83], a[32:35], v[216:219]\n"
    "v_exp_f32 v196, v196\n"
    "v_mfma_f32_16x16x32_bf16 v[200:203], a[84:87], a[36:39], v[200:203]\n"
    "ds_read_b64_tr_b16 a[96:97], v229 offset:49664\n"
    "v_cvt_pk_bf16_f32 v131, v194, v195\n"
    "v_mfma_f32_16x16x32_bf16 v[200:203], a[88:91], a[40:43], v[200:203]\n"
    "s_mov_b32 m0, s57\n"
    "v_exp_f32 v197, v197\n"
    "v_mfma_f32_16x16x32_bf16 v[200:203], a[92:95], a[44:47], v[200:203]\n"
    "buffer_load_dwordx4 v232, s[20:23], s59 offen offset:1024 nt lds\n"
    "v_mfma_f32_16x16x32_bf16 v[204:207], a[92:95], a[60:63], v[220:223]\n"
    "v_exp_f32 v198, v198\n"
    "v_mfma_f32_16x16x32_bf16 v[204:207], a[88:91], a[56:59], v[204:207]\n"
    "ds_read_b64_tr_b16 a[98:99], v229 offset:53760\n"
    "v_cvt_pk_bf16_f32 v134, v196, v197\n"
    "v_mfma_f32_16x16x32_bf16 v[204:207], a[84:87], a[52:55], v[204:207]\n"
    "v_exp_f32 v199, v199\n"
    "v_mfma_f32_16x16x32_bf16 v[204:207], a[80:83], a[48:51], v[204:207]\n"
    "s_waitcnt lgkmcnt(10)\n"
    "ds_read_b128 a[76:79], v228 offset:8960\n"
    "v_cvt_pk_bf16_f32 v135, v198, v199\n"
    "v_mfma_f32_16x16x32_bf16 v[48:51], a[104:107], v[160:163], v[48:51]\n"
    "s_waitcnt lgkmcnt(4)\n"
    "v_exp_f32 v200, v200\n"
    "v_mfma_f32_16x16x32_bf16 v[48:51], a[108:111], v[176:179], v[48:51]\n"
    "s_add_u32 m0, s51, 147456\n"
    "ds_read_b64_tr_b16 a[100:101], v229 offset:57856\n"
    "v_add_f32 v226, v226, v200\n"
    "v_mfma_f32_16x16x32_bf16 v[52:55], a[108:111], v[180:183], v[52:55]\n"
    "s_sub_u32 s11, s60, s8\n"
    "buffer_load_dwordx4 v237, s[24:27], s53 offen offset:3072  lds\n"
    "v_exp_f32 v201, v201\n"
    "v_mfma_f32_16x16x32_bf16 v[52:55], a[104:107], v[164:167], v[52:55]\n"
    "s_add_u32 s60, s60, 142\n"
    "ds_read_b64_tr_b16 a[102:103], v229 offset:61952\n"
    "v_add_f32 v226, v226, v201\n"
    "v_mfma_f32_16x16x32_bf16 v[56:59], a[104:107], v[168:171], v[56:59]\n"
    "s_cmp_gt_i32 s11, 2\n"
    "v_exp_f32 v202, v202\n"
    "v_mfma_f32_16x16x32_bf16 v[56:59], a[108:111], v[184:187], v[56:59]\n"
    "s_cbranch_scc1 Lpa3x%=\n"
    "Lpb3x%=:\n"
    "ds_read_b128 a[80:83], v228 offset:12288\n"
    "v_add_f32 v226, v226, v202\n"
    "v_mfma_f32_16x16x32_bf16 v[60:63], a[108:111], v[188:191], v[60:63]\n"
    "v_exp_f32 v203, v203\n"
    "v_mfma_f32_16x16x32_bf16 v[60:63], a[104:107], v[172:175], v[60:63]\n"
    "v_cvt_pk_bf16_f32 v138, v200, v201\n"
    "v_add_f32 v226, v226, v203\n"
    "v_mfma_f32_16x16x32_bf16 v[192:195], a[64:67], a[0:3], v[208:211]\n"
    "s_mov_b32 m0, s57\n"
    "v_exp_f32 v204, v204\n"
    "v_mfma_f32_16x16x32_bf16 v[192:195], a[68:71], a[4:7], v[192:195]\n"
    "s_waitcnt lgkmcnt(6)\n"
    "ds_read_b64_tr_b16 a[104:105], v230 offset:49664\n"
    "v_cvt_pk_bf16_f32 v139, v202, v203\n"
    "v_mfma_f32_16x16x32_bf16 v[192:195], a[72:75], a[8:11], v[192:195]\n"
    "s_waitcnt lgkmcnt(4)\n"
    "v_exp_f32 v205, v205\n"
    "v_mfma_f32_16x16x32_bf16 v[192:195], a[76:79], a[12:15], v[192:195]\n"
    "buffer_load_dwordx4 v231, s[20:23], s59 offen offset:2048 nt lds\n"
    "v_mfma_f32_16x16x32_bf16 v[196:199], a[76:79], a[28:31], v[212:215]\n"
    "v_exp_f32 v206, v206\n"
    "v_mfma_f32_16x16x32_bf16 v[196:199], a[72:75], a[24:27], v[196:199]\n"
    "ds_read_b64_tr_b16 a[106:107], v230 offset:53760\n"
    "v_cvt_pk_bf16_f32 v142, v204, v205\n"
    "v_mfma_f32_16x16x32_bf16 v[196:199], a[68:71], a[20:23], v[196:199]\n"
    "v_exp_f32 v207, v207\n"
    "v_mfma_f32_16x16x32_bf16 v[196:199], a[64:67], a[16:19], v[196:199]\n"
    "ds_read_b128 a[84:87], v228 offset:12544\n"
    "v_cvt_pk_bf16_f32 v143, v206, v207\n"
    "v_mfma_f32_16x16x32_bf16 v[64:67], a[96:99], v[160:163], v[64:67]\n"
    "s_waitcnt lgkmcnt(4)\n"
    "v_exp_f32 v192, v192\n"
    "v_mfma_f32_16x16x32_bf16 v[64:67], a[100:103], v[176:179], v[64:67]\n"
    "ds_read_b64_tr_b16 a[108:109], v230 offset:57856\n"
    "v_add_f32 v224, v224, v192\n"
    "v_mfma_f32_16x16x32_bf16 v[68:71], a[100:103], v[180:183], v[68:71]\n"
    "v_exp_f32 v193, v193\n"
    "v_mfma_f32_16x16x32_bf16 v[68:71], a[96:99], v[164:167], v[68:71]\n"
    "ds_read_b64_tr_b16 a[110:111], v230 offset:61952\n"
    "v_add_f32 v224, v224, v193\n"
    "v_mfma_f32_16x16x32_bf16 v[72:75], a[96:99], v[168:171], v[72:75]\n"
    "v_exp_f32 v194, v194\n"
    "v_mfma_f32_16x16x32_bf16 v[72:75], a[100:103], v[184:187], v[72:75]\n"
    "ds_read_b128 a[88:91], v228 offset:12800\n"
    "v_add_f32 v224, v224, v194\n"
    "v_mfma_f32_16x16x32_bf16 v[76:79], a[100:103], v[188:191], v[76:79]\n"
    "v_exp_f32 v195, v195\n"
    "v_mfma_f32_16x16x32_bf16 v[76:79], a[96:99], v[172:175], v[76:79]\n"
    "v_cvt_pk_bf16_f32 v144, v192, v193\n"
    "v_add_f32 v224, v224, v195\n"
    "v_mfma_f32_16x16x32_bf16 v[200:203], a[64:67], a[32:35], v[216:219]\n"
    "v_exp_f32 v196, v196\n"
    "v_mfma_f32_16x16x32_bf16 v[200:203], a[68:71], a[36:39], v[200:203]\n"
    "ds_read_b64_tr_b16 a[96:97], v229 offset:49920\n"
    "v_cvt_pk_bf16_f32 v145, v194, v195\n"
    "v_mfma_f32_16x16x32_bf16 v[200:203], a[72:75], a[40:43], v[200:203]\n"
    "v_exp_f32 v197, v197\n"
    "v_mfma_f32_16x16x32_bf16 v[200:203], a[76:79], a[44:47], v[200:203]\n"
    "buffer_load_dwordx4 v232, s[20:23], s59 offen offset:3072 nt lds\n"
    "v_mfma_f32_16x16x32_bf16 v[204:207], a[76:79], a[60:63], v[220:223]\n"
    "v_exp_f32 v198, v198\n"
    "v_mfma_f32_16x16x32_bf16 v[204:207], a[72:75], a[56:59], v[204:207]\n"
    "s_add_u32 s59, s59, 16384\n"
    "ds_read_b64_tr_b16 a[98:99], v229 offset:54016\n"
    "v_cvt_pk_bf16_f32 v148, v196, v197\n"
    "v_mfma_f32_16x16x32_bf16 v[204:207], a[68:71], a[52:55], v[204:207]\n"
    "s_and_b32 s59, s59, 0x3ffff\n"
    "v_exp_f32 v199, v199\n"
    "v_mfma_f32_16x16x32_bf16 v[204:207], a[64:67], a[48:51], v[204:207]\n"
    "ds_read_b128 a[92:95], v228 offset:13056\n"
    "v_cvt_pk_bf16_f32 v149, v198, v199\n"
    "v_mfma_f32_16x16x32_bf16 a[120:123], a[112:115], v[172:175], a[120:123]\n"
    "ds_read_b64_tr_b16 a[176:177], v242 offset:512\n"
    "ds_read_b64_tr_b16 a[180:181], v243 offset:512\n"
    "v_mfma_f32_16x16x32_bf16 a[120:123], a[112:115], v[188:191], a[120:123]\n"
    "s_waitcnt lgkmcnt(9)\n"
    "ds_read_b64_tr_b16 a[184:185], v242 offset:768\n"
    "ds_read_b64_tr_b16 a[188:189], v243 offset:768\n"
    "v_mfma_f32_16x16x32_bf16 v[80:83], a[104:107], v[160:163], v[80:83]\n"
    "s_waitcnt lgkmcnt(8)\n"
    "v_exp_f32 v200, v200\n"
    "v_mfma_f32_16x16x32_bf16 v[80:83], a[108:111], v[176:179], v[80:83]\n"
    "s_add_u32 s57, s57, 16384\n"
    "ds_read_b64_tr_b16 a[100:101], v229 offset:58112\n"
    "v_add_f32 v226, v226, v200\n"
    "v_mfma_f32_16x16x32_bf16 v[84:87], a[108:111], v[180:183], v[84:87]\n"
    "s_bitset0_b32 s57, 17\n"
    "v_exp_f32 v201, v201\n"
    "v_mfma_f32_16x16x32_bf16 v[84:87], a[104:107], v[164:167], v[84:87]\n"
    "ds_read_b64_tr_b16 a[102:103], v229 offset:62208\n"
    "v_add_f32 v226, v226, v201\n"
    "v_mfma_f32_16x16x32_bf16 v[88:91], a[104:107], v[168:171], v[88:91]\n"
    "s_waitcnt vmcnt(12) lgkmcnt(6)\n"
    "v_exp_f32 v202, v202\n"
    "v_mfma_f32_16x16x32_bf16 v[88:91], a[108:111], v[184:187], v[88:91]\n"
    "s_barrier\n"
    "ds_read_b128 a[64:67], v228 offset:16384\n"
    "v_add_f32 v226, v226, v202\n"
    "v_mfma_f32_16x16x32_bf16 v[92:95], a[108:111], v[188:191], v[92:95]\n"
    "s_bitset1_b32 s57, 16\n"
    "v_exp_f32 v203, v203\n"
    "v_mfma_f32_16x16x32_bf16 v[92:95], a[104:107], v[172:175], v[92:95]\n"
    "v_cvt_pk_bf16_f32 v152, v200, v201\n"
    "v_add_f32 v226, v226, v203\n"
    "v_mfma_f32_16x16x32_bf16 v[192:195], a[80:83], a[0:3], v[208:211]\n"
    "s_mov_b32 m0, s56\n"
    "v_exp_f32 v204, v204\n"
    "v_mfma_f32_16x16x32_bf16 v[192:195], a[84:87], a[4:7], v[192:195]\n"
    "ds_read_b64_tr_b16 a[104:105], v230 offset:49920\n"
    "v_cvt_pk_bf16_f32 v153, v202, v203\n"
    "v_mfma_f32_16x16x32_bf16 v[192:195], a[88:91], a[8:11], v[192:195]\n"
    "v_exp_f32 v205, v205\n"
    "v_mfma_f32_16x16x32_bf16 v[192:195], a[92:95], a[12:15], v[192:195]\n"
    "buffer_load_dwordx4 v231, s[16:19], s58 offen offset:0 nt lds\n"
    "v_mfma_f32_16x16x32_bf16 v[196:199], a[92:95], a[28:31], v[212:215]\n"
    "v_exp_f32 v206, v206\n"
    "v_mfma_f32_16x16x32_bf16 v[196:199], a[88:91], a[24:27], v[196:199]\n"
    "ds_read_b64_tr_b16 a[106:107], v230 offset:54016\n"
    "v_cvt_pk_bf16_f32 v156, v204, v205\n"
    "v_mfma_f32_16x16x32_bf16 v[196:199], a[84:87], a[20:23], v[196:199]\n"
    "v_exp_f32 v207, v207\n"
    "v_mfma_f32_16x16x32_bf16 v[196:199], a[80:83], a[16:19], v[196:199]\n"
    "ds_read_b128 a[68:71], v228 offset:16640\n"
    "v_cvt_pk_bf16_f32 v157, v206, v207\n"
    "v_mfma_f32_16x16x32_bf16 v[96:99], a[96:99], v[160:163], v[96:99]\n"
    "s_waitcnt lgkmcnt(4)\n"
    "v_exp_f32 v192, v192\n"
    "v_mfma_f32_16x16x32_bf16 v[96:99], a[100:103], v[176:179], v[96:99]\n"
    "ds_read_b64_tr_b16 a[108:109], v230 offset:58112\n"
    "v_add_f32 v224, v224, v192\n"
    "v_mfma_f32_16x16x32_bf16 v[100:103], a[100:103], v[180:183], v[100:103]\n"
    "v_exp_f32 v193, v193\n"
    "v_mfma_f32_16x16x32_bf16 v[100:103], a[96:99], v[164:167], v[100:103]\n"
    "ds_read_b64_tr_b16 a[110:111], v230 offset:62208\n"
    "v_add_f32 v224, v224, v193\n"
    "v_mfma_f32_16x16x32_bf16 v[104:107], a[96:99], v[168:171], v[104:107]\n"
    "v_exp_f32 v194, v194\n"
    "v_mfma_f32_16x16x32_bf16 v[104:107], a[100:103], v[184:187], v[104:107]\n"
    "ds_read_b128 a[72:75], v228 offset:16896\n"
    "v_add_f32 v224, v224, v194\n"
    "v_mfma_f32_16x16x32_bf16 v[108:111], a[100:103], v[188:191], v[108:111]\n"
    "v_exp_f32 v195, v195\n"
    "v_mfma_f32_16x16x32_bf16 v[108:111], a[96:99], v[172:175], v[108:111]\n"
    "v_cvt_pk_bf16_f32 v146, v192, v193\n"
    "v_add_f32 v224, v224, v195\n"
    "v_mfma_f32_16x16x32_bf16 v[200:203], a[80:83], a[32:35], v[216:219]\n"
    "v_exp_f32 v196, v196\n"
    "v_mfma_f32_16x16x32_bf16 v[200:203], a[84:87], a[36:39], v[200:203]\n"
    "ds_read_b64_tr_b16 a[96:97], v229 offset:0\n"
    "v_cvt_pk_bf16_f32 v147, v194, v195\n"
    "v_mfma_f32_16x16x32_bf16 v[200:203], a[88:91], a[40:43], v[200:203]\n"
    "v_exp_f32 v197, v197\n"
    "v_mfma_f32_16x16x32_bf16 v[200:203], a[92:95], a[44:47], v[200:203]\n"
    "buffer_load_dwordx4 v232, s[16:19], s58 offen offset:1024 nt lds\n"
    "v_mfma_f32_16x16x32_bf16 v[204:207], a[92:95], a[60:63], v[220:223]\n"
    "v_exp_f32 v198, v198\n"
    "v_mfma_f32_16x16x32_bf16 v[204:207], a[88:91], a[56:59], v[204:207]\n"
    "ds_read_b64_tr_b16 a[98:99], v229 offset:4096\n"
    "v_cvt_pk_bf16_f32 v150, v196, v197\n"
    "v_mfma_f32_16x16x32_bf16 v[204:207], a[84:87], a[52:55], v[204:207]\n"
    "v_exp_f32 v199, v199\n"
    "v_mfma_f32_16x16x32_bf16 v[204:207], a[80:83], a[48:51], v[204:207]\n"
    "s_waitcnt lgkmcnt(6)\n"
    "ds_read_b128 a[76:79], v228 offset:17152\n"
    "v_cvt_pk_bf16_f32 v151, v198, v199\n"
    "v_mfma_f32_16x16x32_bf16 v[112:115], a[104:107], v[160:163], v[112:115]\n"
    "s_waitcnt lgkmcnt(4)\n"
    "v_exp_f32 v200, v200\n"
    "v_mfma_f32_16x16x32_bf16 v[112:115], a[108:111], v[176:179], v[112:115]\n"
    "ds_read_b64_tr_b16 a[100:101], v229 offset:8192\n"
    "v_add_f32 v226, v226, v200\n"
    "v_mfma_f32_16x16x32_bf16 v[116:119], a[108:111], v[180:183], v[116:119]\n"
    "v_exp_f32 v201, v201\n"
    "v_mfma_f32_16x16x32_bf16 v[116:119], a[104:107], v[164:167], v[116:119]\n"
    "ds_read_b64_tr_b16 a[102:103], v229 offset:12288\n"
    "v_add_f32 v226, v226, v201\n"
    "v_mfma_f32_16x16x32_bf16 v[120:123], a[104:107], v[168:171], v[120:123]\n"
    "v_exp_f32 v202, v202\n"
    "v_mfma_f32_16x16x32_bf16 v[120:123], a[108:111], v[184:187], v[120:123]\n"
    "ds_read_b128 a[80:83], v228 offset:20480\n"
    "v_add_f32 v226, v226, v202\n"
    "v_mfma_f32_16x16x32_bf16 v[124:127], a[108:111], v[188:191], v[124:127]\n"
    "v_exp_f32 v203, v203\n"
    "v_mfma_f32_16x16x32_bf16 v[124:127], a[104:107], v[172:175], v[124:127]\n"
    "v_cvt_pk_bf16_f32 v154, v200, v201\n"
    "v_add_f32 v226, v226, v203\n"
    "v_mfma_f32_16x16x32_bf16 v[192:195], a[64:67], a[0:3], v[208:211]\n"
    "s_mov_b32 m0, s56\n"
    "v_exp_f32 v204, v204\n"
    "v_mfma_f32_16x16x32_bf16 v[192:195], a[68:71], a[4:7], v[192:195]\n"
    "s_waitcnt lgkmcnt(6)\n"
    "ds_read_b64_tr_b16 a[104:105], v230 offset:0\n"
    "v_cvt_pk_bf16_f32 v155, v202, v203\n"
    "v_mfma_f32_16x16x32_bf16 v[192:195], a[72:75], a[8:11], v[192:195]\n"
    "s_waitcnt lgkmcnt(4)\n"
    "v_exp_f32 v205, v205\n"
    "v_mfma_f32_16x16x32_bf16 v[192:195], a[76:79], a[12:15], v[192:195]\n"
    "s_nop 0\n"
    "buffer_load_dwordx4 v231, s[16:19], s58 offen offset:2048 nt lds\n"
    "v_mfma_f32_16x16x32_bf16 v[196:199], a[76:79], a[28:31], v[212:215]\n"
    "v_exp_f32 v206, v206\n"
    "v_mfma_f32_16x16x32_bf16 v[196:199], a[72:75], a[24:27], v[196:199]\n"
    "ds_read_b64_tr_b16 a[106:107], v230 offset:4096\n"
    "v_cvt_pk_bf16_f32 v158, v204, v205\n"
    "v_mfma_f32_16x16x32_bf16 v[196:199], a[68:71], a[20:23], v[196:199]\n"
    "s_add_u32 m0, s51, 131072\n"
    "v_exp_f32 v207, v207\n"
    "v_mfma_f32_16x16x32_bf16 v[196:199], a[64:67], a[16:19], v[196:199]\n"
    "ds_read_b128 a[84:87], v228 offset:20736\n"
    "v_cvt_pk_bf16_f32 v159, v206, v207\n"
    "v_mfma_f32_16x16x32_bf16 v[0:3], a[96:99], v[128:131], v[0:3]\n"
    "s_waitcnt lgkmcnt(4)\n"
    "v_exp_f32 v192, v192\n"
    "v_mfma_f32_16x16x32_bf16 v[0:3], a[100:103], v[144:147], v[0:3]\n"
    "s_xor_b32 s53, s88, 65536\n"
    "ds_read_b64_tr_b16 a[108:109], v230 offset:8192\n"
    "v_add_f32 v224, v224, v192\n"
    "v_mfma_f32_16x16x32_bf16 v[4:7], a[100:103], v[148:151], v[4:7]\n"
    "s_memrealtime s[8:9]\n"
    "buffer_load_dwordx4 v234, s[24:27], s53 offen offset:0  lds\n"
    "v_exp_f32 v193, v193\n"
    "v_mfma_f32_16x16x32_bf16 v[4:7], a[96:99], v[132:135], v[4:7]\n"
    "ds_read_b64_tr_b16 a[110:111], v230 offset:12288\n"
    "v_add_f32 v224, v224, v193\n"
    "v_mfma_f32_16x16x32_bf16 v[8:11], a[96:99], v[136:139], v[8:11]\n"
    "v_exp_f32 v194, v194\n"
    "v_mfma_f32_16x16x32_bf16 v[8:11], a[100:103], v[152:155], v[8:11]\n"
    "ds_read_b128 a[88:91], v228 offset:20992\n"
    "v_add_f32 v224, v224, v194\n"
    "v_mfma_f32_16x16x32_bf16 v[12:15], a[100:103], v[156:159], v[12:15]\n"
    "v_exp_f32 v195, v195\n"
    "v_mfma_f32_16x16x32_bf16 v[12:15], a[96:99], v[140:143], v[12:15]\n"
    "v_cvt_pk_bf16_f32 v160, v192, v193\n"
    "v_add_f32 v224, v224, v195\n"
    "v_mfma_f32_16x16x32_bf16 v[200:203], a[64:67], a[32:35], v[216:219]\n"
    "v_exp_f32 v196, v196\n"
    "v_mfma_f32_16x16x32_bf16 v[200:203], a[68:71], a[36:39], v[200:203]\n"
    "ds_read_b64_tr_b16 a[96:97], v229 offset:256\n"
    "v_cvt_pk_bf16_f32 v161, v194, v195\n"
    "v_mfma_f32_16x16x32_bf16 v[200:203], a[72:75], a[40:43], v[200:203]\n"
    "s_mov_b32 m0, s56\n"
    "v_exp_f32 v197, v197\n"
    "v_mfma_f32_16x16x32_bf16 v[200:203], a[76:79], a[44:47], v[200:203]\n"
    "buffer_load_dwordx4 v232, s[16:19], s58 offen offset:3072 nt lds\n"
    "v_mfma_f32_16x16x32_bf16 v[204:207], a[76:79], a[60:63], v[220:223]\n"
    "v_exp_f32 v198, v198\n"
    "v_mfma_f32_16x16x32_bf16 v[204:207], a[72:75], a[56:59], v[204:207]\n"
    "s_add_u32 s58, s58, 16384\n"
    "ds_read_b64_tr_b16 a[98:99], v229 offset:4352\n"
    "v_cvt_pk_bf16_f32 v164, v196, v197\n"
    "v_mfma_f32_16x16x32_bf16 v[204:207], a[68:71], a[52:55], v[204:207]\n"
    "s_and_b32 s58, s58, 0x3ffff\n"
    "v_exp_f32 v199, v199\n"
    "v_mfma_f32_16x16x32_bf16 v[204:207], a[64:67], a[48:51], v[204:207]\n"
    "s_waitcnt lgkmcnt(6)\n"
    "ds_read_b128 a[92:95], v228 offset:21248\n"
    "v_cvt_pk_bf16_f32 v165, v198, v199\n"
    "v_mfma_f32_16x16x32_bf16 v[16:19], a[104:107], v[128:131], v[16:19]\n"
    "s_waitcnt lgkmcnt(4)\n"
    "v_exp_f32 v200, v200\n"
    "v_mfma_f32_16x16x32_bf16 v[16:19], a[108:111], v[144:147], v[16:19]\n"
    "s_add_u32 m0, s51, 131072\n"
    "ds_read_b64_tr_b16 a[100:101], v229 offset:8448\n"
    "v_add_f32 v226, v226, v200\n"
    "v_mfma_f32_16x16x32_bf16 v[20:23], a[108:111], v[148:151], v[20:23]\n"
    "buffer_load_dwordx4 v235, s[24:27], s53 offen offset:1024  lds\n"
    "v_exp_f32 v201, v201\n"
    "v_mfma_f32_16x16x32_bf16 v[20:23], a[104:107], v[132:135], v[20:23]\n"
    "s_add_u32 s56, s56, 16384\n"
    "ds_read_b64_tr_b16 a[102:103], v229 offset:12544\n"
    "v_add_f32 v226, v226, v201\n"
    "v_mfma_f32_16x16x32_bf16 v[24:27], a[104:107], v[136:139], v[24:27]\n"
    "s_and_b32 s56, s56, 0xffff\n"
    "v_exp_f32 v202, v202\n"
    "v_mfma_f32_16x16x32_bf16 v[24:27], a[108:111], v[152:155], v[24:27]\n"
    "ds_read_b128 a[64:67], v228 offset:24576\n"
    "v_add_f32 v226, v226, v202\n"
    "v_mfma_f32_16x16x32_bf16 v[28:31], a[108:111], v[156:159], v[28:31]\n"
    "v_exp_f32 v203, v203\n"
    "v_mfma_f32_16x16x32_bf16 v[28:31], a[104:107], v[140:143], v[28:31]\n"
    "v_cvt_pk_bf16_f32 v168, v200, v201\n"
    "v_add_f32 v226, v226, v203\n"
    "v_mfma_f32_16x16x32_bf16 v[192:195], a[80:83], a[0:3], v[208:211]\n"
    "s_mov_b32 m0, s57\n"
    "v_exp_f32 v204, v204\n"
    "v_mfma_f32_16x16x32_bf16 v[192:195], a[84:87], a[4:7], v[192:195]\n"
    "s_waitcnt lgkmcnt(6)\n"
    "ds_read_b64_tr_b16 a[104:105], v230 offset:256\n"
    "v_cvt_pk_bf16_f32 v169, v202, v203\n"
    "v_mfma_f32_16x16x32_bf16 v[192:195], a[88:91], a[8:11], v[192:195]\n"
    "s_waitcnt lgkmcnt(4)\n"
    "v_exp_f32 v205, v205\n"
    "v_mfma_f32_16x16x32_bf16 v[192:195], a[92:95], a[12:15], v[192:195]\n"
    "buffer_load_dwordx4 v231, s[20:23], s59 offen offset:0 nt lds\n"
    "v_mfma_f32_16x16x32_bf16 v[196:199], a[92:95], a[28:31], v[212:215]\n"
    "v_exp_f32 v206, v206\n"
    "v_mfma_f32_16x16x32_bf16 v[196:199], a[88:91], a[24:27], v[196:199]\n"
    "ds_read_b64_tr_b16 a[106:107], v230 offset:4352\n"
    "v_cvt_pk_bf16_f32 v172, v204, v205\n"
    "v_mfma_f32_16x16x32_bf16 v[196:199], a[84:87], a[20:23], v[196:199]\n"
    "v_exp_f32 v207, v207\n"
    "v_mfma_f32_16x16x32_bf16 v[196:199], a[80:83], a[16:19], v[196:199]\n"
    "ds_read_b128 a[68:71], v228 offset:24832\n"
    "v_cvt_pk_bf16_f32 v173, v206, v207\n"
    "v_mfma_f32_16x16x32_bf16 a[116:119], a[112:115], v[132:135], a[116:119]\n"
    "s_waitcnt vmcnt(9)\n"
    "ds_read_b64_tr_b16 a[162:163], v242 offset:16384\n"
    "ds_read_b64_tr_b16 a[166:167], v243 offset:16384\n"
    "v_mfma_f32_16x16x32_bf16 a[116:119], a[112:115], v[148:151], a[116:119]\n"
    "ds_read_b64_tr_b16 a[170:171], v242 offset:16640\n"
    "ds_read_b64_tr_b16 a[174:175], v243 offset:16640\n"
    "v_mfma_f32_16x16x32_bf16 v[32:35], a[96:99], v[128:131], v[32:35]\n"
    "s_waitcnt lgkmcnt(8)\n"
    "v_exp_f32 v192, v192\n"
    "v_mfma_f32_16x16x32_bf16 v[32:35], a[100:103], v[144:147], v[32:35]\n"
    "s_add_u32 m0, s51, 131072\n"
    "ds_read_b64_tr_b16 a[108:109], v230 offset:8448\n"
    "v_add_f32 v224, v224, v192\n"
    "v_mfma_f32_16x16x32_bf16 v[36:39], a[100:103], v[148:151], v[36:39]\n"
    "buffer_load_dwordx4 v236, s[24:27], s53 offen offset:2048  lds\n"
    "v_exp_f32 v193, v193\n"
    "v_mfma_f32_16x16x32_bf16 v[36:39], a[96:99], v[132:135], v[36:39]\n"
    "ds_read_b64_tr_b16 a[110:111], v230 offset:12544\n"
    "v_add_f32 v224, v224, v193\n"
    "v_mfma_f32_16x16x32_bf16 v[40:43], a[96:99], v[136:139], v[40:43]\n"
    "v_exp_f32 v194, v194\n"
    "v_mfma_f32_16x16x32_bf16 v[40:43], a[100:103], v[152:155], v[40:43]\n"
    "ds_read_b128 a[72:75], v228 offset:25088\n"
    "v_add_f32 v224, v224, v194\n"
    "v_mfma_f32_16x16x32_bf16 v[44:47], a[100:103], v[156:159], v[44:47]\n"
    "v_exp_f32 v195, v195\n"
    "v_mfma_f32_16x16x32_bf16 v[44:47], a[96:99], v[140:143], v[44:47]\n"
    "v_cvt_pk_bf16_f32 v162, v192, v193\n"
    "v_add_f32 v224, v224, v195\n"
    "v_mfma_f32_16x16x32_bf16 v[200:203], a[80:83], a[32:35], v[216:219]\n"
    "Lpg6%=:\n"
    "s_cbranch_execz Lwb0%=\n"
    "v_exp_f32 v196, v196\n"
    "v_mfma_f32_16x16x32_bf16 v[200:203], a[84:87], a[36:39], v[200:203]\n"
    "ds_read_b64_tr_b16 a[96:97], v229 offset:512\n"
    "v_cvt_pk_bf16_f32 v163, v194, v195\n"
    "v_mfma_f32_16x16x32_bf16 v[200:203], a[88:91], a[40:43], v[200:203]\n"
    "s_mov_b32 m0, s57\n"
    "v_exp_f32 v197, v197\n"
    "v_mfma_f32_16x16x32_bf16 v[200:203], a[92:95], a[44:47], v[200:203]\n"
    "buffer_load_dwordx4 v232, s[20:23], s59 offen offset:1024 nt lds\n"
    "v_mfma_f32_16x16x32_bf16 v[204:207], a[92:95], a[60:63], v[220:223]\n"
    "v_exp_f32 v198, v198\n"
    "v_mfma_f32_16x16x32_bf16 v[204:207], a[88:91], a[56:59], v[204:207]\n"
    "ds_read_b64_tr_b16 a[98:99], v229 offset:4608\n"
    "v_cvt_pk_bf16_f32 v166, v196, v197\n"
    "v_mfma_f32_16x16x32_bf16 v[204:207], a[84:87], a[52:55], v[204:207]\n"
    "v_exp_f32 v199, v199\n"
    "v_mfma_f32_16x16x32_bf16 v[204:207], a[80:83], a[48:51], v[204:207]\n"
    "s_waitcnt lgkmcnt(10)\n"
    "ds_read_b128 a[76:79], v228 offset:25344\n"
    "v_cvt_pk_bf16_f32 v167, v198, v199\n"
    "v_mfma_f32_16x16x32_bf16 v[48:51], a[104:107], v[128:131], v[48:51]\n"
    "s_waitcnt lgkmcnt(4)\n"
    "v_exp_f32 v200, v200\n"
    "v_mfma_f32_16x16x32_bf16 v[48:51], a[108:111], v[144:147], v[48:51]\n"
    "s_add_u32 m0, s51, 131072\n"
    "ds_read_b64_tr_b16 a[100:101], v229 offset:8704\n"
    "v_add_f32 v226, v226, v200\n"
    "v_mfma_f32_16x16x32_bf16 v[52:55], a[108:111], v[148:151], v[52:55]\n"
    "s_sub_u32 s11, s60, s8\n"
    "buffer_load_dwordx4 v237, s[24:27], s53 offen offset:3072  lds\n"
    "v_exp_f32 v201, v201\n"
    "v_mfma_f32_16x16x32_bf16 v[52:55], a[104:107], v[132:135], v[52:55]\n"
    "s_add_u32 s60, s60, 142\n"
    "ds_read_b64_tr_b16 a[102:103], v229 offset:12800\n"
    "v_add_f32 v226, v226, v201\n"
    "v_mfma_f32_16x16x32_bf16 v[56:59], a[104:107], v[136:139], v[56:59]\n"
    "s_cmp_gt_i32 s11, 2\n"
    "v_exp_f32 v202, v202\n"
    "v_mfma_f32_16x16x32_bf16 v[56:59], a[108:111], v[152:155], v[56:59]\n"
    "s_cbranch_scc1 Lpa4x%=\n"
    "Lpb4x%=:\n"
    "ds_read_b128 a[80:83], v228 offset:28672\n"
    "v_add_f32 v226, v226, v202\n"
    "v_mfma_f32_16x16x32_bf16 v[60:63], a[108:111], v[156:159], v[60:63]\n"
    "v_exp_f32 v203, v203\n"
    "v_mfma_f32_16x16x32_bf16 v[60:63], a[104:107], v[140:143], v[60:63]\n"
    "v_cvt_pk_bf16_f32 v170, v200, v201\n"
    "v_add_f32 v226, v226, v203\n"
    "v_mfma_f32_16x16x32_bf16 v[192:195], a[64:67], a[0:3], v[208:211]\n"
    "s_mov_b32 m0, s57\n"
    "v_exp_f32 v204, v204\n"
    "v_mfma_f32_16x16x32_bf16 v[192:195], a[68:71], a[4:7], v[192:195]\n"
    "s_waitcnt lgkmcnt(6)\n"
    "ds_read_b64_tr_b16 a[104:105], v230 offset:512\n"
    "v_cvt_pk_bf16_f32 v171, v202, v203\n"
    "v_mfma_f32_16x16x32_bf16 v[192:195], a[72:75], a[8:11], v[192:195]\n"
    "s_waitcnt lgkmcnt(4)\n"
    "v_exp_f32 v205, v205\n"
    "v_mfma_f32_16x16x32_bf16 v[192:195], a[76:79], a[12:15], v[192:195]\n"
    "buffer_load_dwordx4 v231, s[20:23], s59 offen offset:2048 nt lds\n"
    "v_mfma_f32_16x16x32_bf16 v[196:199], a[76:79], a[28:31], v[212:215]\n"
    "v_exp_f32 v206, v206\n"
    "v_mfma_f32_16x16x32_bf16 v[196:199], a[72:75], a[24:27], v[196:199]\n"
    "ds_read_b64_tr_b16 a[106:107], v230 offset:4608\n"
    "v_cvt_pk_bf16_f32 v174, v204, v205\n"
    "v_mfma_f32_16x16x32_bf16 v[196:199], a[68:71], a[20:23], v[196:199]\n"
    "v_exp_f32 v207, v207\n"
    "v_mfma_f32_16x16x32_bf16 v[196:199], a[64:67], a[16:19], v[196:199]\n"
    "ds_read_b128 a[84:87], v228 offset:28928\n"
    "v_cvt_pk_bf16_f32 v175, v206, v207\n"
    "v_mfma_f32_16x16x32_bf16 v[64:67], a[96:99], v[128:131], v[64:67]\n"
    "s_waitcnt lgkmcnt(4)\n"
    "v_exp_f32 v192, v192\n"
    "v_mfma_f32_16x16x32_bf16 v[64:67], a[100:103], v[144:147], v[64:67]\n"
    "ds_read_b64_tr_b16 a[108:109], v230 offset:8704\n"
    "v_add_f32 v224, v224, v192\n"
    "v_mfma_f32_16x16x32_bf16 v[68:71], a[100:103], v[148:151], v[68:71]\n"
    "v_exp_f32 v193, v193\n"
    "v_mfma_f32_16x16x32_bf16 v[68:71], a[96:99], v[132:135], v[68:71]\n"
    "ds_read_b64_tr_b16 a[110:111], v230 offset:12800\n"
    "v_add_f32 v224, v224, v193\n"
    "v_mfma_f32_16x16x32_bf16 v[72:75], a[96:99], v[136:139], v[72:75]\n"
    "v_exp_f32 v194, v194\n"
    "v_mfma_f32_16x16x32_bf16 v[72:75], a[100:103], v[152:155], v[72:75]\n"
    "ds_read_b128 a[88:91], v228 offset:29184\n"
    "v_add_f32 v224, v224, v194\n"
    "v_mfma_f32_16x16x32_bf16 v[76:79], a[100:103], v[156:159], v[76:79]\n"
    "v_exp_f32 v195, v195\n"
    "v_mfma_f32_16x16x32_bf16 v[76:79], a[96:99], v[140:143], v[76:79]\n"
    "v_cvt_pk_bf16_f32 v176, v192, v193\n"
    "v_add_f32 v224, v224, v195\n"
    "v_mfma_f32_16x16x32_bf16 v[200:203], a[64:67], a[32:35], v[216:219]\n"
    "v_exp_f32 v196, v196\n"
    "v_mfma_f32_16x16x32_bf16 v[200:203], a[68:71], a[36:39], v[200:203]\n"
    "ds_read_b64_tr_b16 a[96:97], v229 offset:768\n"
    "v_cvt_pk_bf16_f32 v177, v194, v195\n"
    "v_mfma_f32_16x16x32_bf16 v[200:203], a[72:75], a[40:43], v[200:203]\n"
    "v_exp_f32 v197, v197\n"
    "v_mfma_f32_16x16x32_bf16 v[200:203], a[76:79], a[44:47], v[200:203]\n"
    "buffer_load_dwordx4 v232, s[20:23], s59 offen offset:3072 nt lds\n"
    "v_mfma_f32_16x16x32_bf16 v[204:207], a[76:79], a[60:63], v[220:223]\n"
    "v_exp_f32 v198, v198\n"
    "v_mfma_f32_16x16x32_bf16 v[204:207], a[72:75], a[56:59], v[204:207]\n"
    "s_add_u32 s59, s59, 16384\n"
    "ds_read_b64_tr_b16 a[98:99], v229 offset:4864\n"
    "v_cvt_pk_bf16_f32 v180, v196, v197\n"
    "v_mfma_f32_16x16x32_bf16 v[204:207], a[68:71], a[52:55], v[204:207]\n"
    "s_and_b32 s59, s59, 0x3ffff\n"
    "v_exp_f32 v199, v199\n"
    "v_mfma_f32_16x16x32_bf16 v[204:207], a[64:67], a[48:51], v[204:207]\n"
    "ds_read_b128 a[92:95], v228 offset:29440\n"
    "v_cvt_pk_bf16_f32 v181, v198, v199\n"
    "v_mfma_f32_16x16x32_bf16 a[120:123], a[112:115], v[140:143], a[120:123]\n"
    "ds_read_b64_tr_b16 a[178:179], v242 offset:16896\n"
    "ds_read_b64_tr_b16 a[182:183], v243 offset:16896\n"
    "v_mfma_f32_16x16x32_bf16 a[120:123], a[112:115], v[156:159], a[120:123]\n"
    "s_waitcnt lgkmcnt(9)\n"
    "ds_read_b64_tr_b16 a[186:187], v242 offset:17152\n"
    "ds_read_b64_tr_b16 a[190:191], v243 offset:17152\n"
    "v_mfma_f32_16x16x32_bf16 v[80:83], a[104:107], v[128:131], v[80:83]\n"
    "s_waitcnt lgkmcnt(8)\n"
    "v_exp_f32 v200, v200\n"
    "v_mfma_f32_16x16x32_bf16 v[80:83], a[108:111], v[144:147], v[80:83]\n"
    "s_add_u32 s57, s57, 16384\n"
    "ds_read_b64_tr_b16 a[100:101], v229 offset:8960\n"
    "v_add_f32 v226, v226, v200\n"
    "v_mfma_f32_16x16x32_bf16 v[84:87], a[108:111], v[148:151], v[84:87]\n"
    "s_bitset0_b32 s57, 17\n"
    "v_exp_f32 v201, v201\n"
    "v_mfma_f32_16x16x32_bf16 v[84:87], a[104:107], v[132:135], v[84:87]\n"
    "ds_read_b64_tr_b16 a[102:103], v229 offset:13056\n"
    "v_add_f32 v226, v226, v201\n"
    "v_mfma_f32_16x16x32_bf16 v[88:91], a[104:107], v[136:139], v[88:91]\n"
    "s_waitcnt vmcnt(12) lgkmcnt(6)\n"
    "v_exp_f32 v202, v202\n"
    "v_mfma_f32_16x16x32_bf16 v[88:91], a[108:111], v[152:155], v[88:91]\n"
    "s_barrier\n"
    "ds_read_b128 a[64:67], v228 offset:32768\n"
    "v_add_f32 v226, v226, v202\n"
    "v_mfma_f32_16x16x32_bf16 v[92:95], a[108:111], v[156:159], v[92:95]\n"
    "s_bitset1_b32 s57, 16\n"
    "v_exp_f32 v203, v203\n"
    "v_mfma_f32_16x16x32_bf16 v[92:95], a[104:107], v[140:143], v[92:95]\n"
    "v_cvt_pk_bf16_f32 v184, v200, v201\n"
    "v_add_f32 v226, v226, v203\n"
    "v_mfma_f32_16x16x32_bf16 v[192:195], a[80:83], a[0:3], v[208:211]\n"
    "s_mov_b32 m0, s56\n"
    "v_exp_f32 v204, v204\n"
    "v_mfma_f32_16x16x32_bf16 v[192:195], a[84:87], a[4:7], v[192:195]\n"
    "ds_read_b64_tr_b16 a[104:105], v230 offset:768\n"
    "v_cvt_pk_bf16_f32 v185, v202, v203\n"
    "v_mfma_f32_16x16x32_bf16 v[192:195], a[88:91], a[8:11], v[192:195]\n"
    "v_exp_f32 v205, v205\n"
    "v_mfma_f32_16x16x32_bf16 v[192:195], a[92:95], a[12:15], v[192:195]\n"
    "buffer_load_dwordx4 v231, s[16:19], s58 offen offset:0 nt lds\n"
    "v_mfma_f32_16x16x32_bf16 v[196:199], a[92:95], a[28:31], v[212:215]\n"
    "v_exp_f32 v206, v206\n"
    "v_mfma_f32_16x16x32_bf16 v[196:199], a[88:91], a[24:27], v[196:199]\n"
    "ds_read_b64_tr_b16 a[106:107], v230 offset:4864\n"
    "v_cvt_pk_bf16_f32 v188, v204, v205\n"
    "v_mfma_f32_16x16x32_bf16 v[196:199], a[84:87], a[20:23], v[196:199]\n"
    "v_exp_f32 v207, v207\n"
    "v_mfma_f32_16x16x32_bf16 v[196:199], a[80:83], a[16:19], v[196:199]\n"
    "ds_read_b128 a[68:71], v228 offset:33024\n"
    "v_cvt_pk_bf16_f32 v189, v206, v207\n"
    "v_mfma_f32_16x16x32_bf16 v[96:99], a[96:99], v[128:131], v[96:99]\n"
    "s_waitcnt lgkmcnt(4)\n"
    "v_exp_f32 v192, v192\n"
    "v_mfma_f32_16x16x32_bf16 v[96:99], a[100:103], v[144:147], v[96:99]\n"
    "ds_read_b64_tr_b16 a[108:109], v230 offset:8960\n"
    "v_add_f32 v224, v224, v192\n"
    "v_mfma_f32_16x16x32_bf16 v[100:103], a[100:103], v[148:151], v[100:103]\n"
    "v_exp_f32 v193, v193\n"
    "v_mfma_f32_16x16x32_bf16 v[100:103], a[96:99], v[132:135], v[100:103]\n"
    "ds_read_b64_tr_b16 a[110:111], v230 offset:13056\n"
    "v_add_f32 v224, v224, v193\n"
    "v_mfma_f32_16x16x32_bf16 v[104:107], a[96:99], v[136:139], v[104:107]\n"
    "v_exp_f32 v194, v194\n"
    "v_mfma_f32_16x16x32_bf16 v[104:107], a[100:103], v[152:155], v[104:107]\n"
    "ds_read_b128 a[72:75], v228 offset:33280\n"
    "v_add_f32 v224, v224, v194\n"
    "v_mfma_f32_16x16x32_bf16 v[108:111], a[100:103], v[156:159], v[108:111]\n"
    "v_exp_f32 v195, v195\n"
    "v_mfma_f32_16x16x32_bf16 v[108:111], a[96:99], v[140:143], v[108:111]\n"
    "v_cvt_pk_bf16_f32 v178, v192, v193\n"
    "v_add_f32 v224, v224, v195\n"
    "v_mfma_f32_16x16x32_bf16 v[200:203], a[80:83], a[32:35], v[216:219]\n"
    "v_exp_f32 v196, v196\n"
    "v_mfma_f32_16x16x32_bf16 v[200:203], a[84:87], a[36:39], v[200:203]\n"
    "ds_read_b64_tr_b16 a[96:97], v229 offset:16384\n"
    "v_cvt_pk_bf16_f32 v179, v194, v195\n"
    "v_mfma_f32_16x16x32_bf16 v[200:203], a[88:91], a[40:43], v[200:203]\n"
    "v_exp_f32 v197, v197\n"
    "v_mfma_f32_16x16x32_bf16 v[200:203], a[92:95], a[44:47], v[200:203]\n"
    "buffer_load_dwordx4 v232, s[16:19], s58 offen offset:1024 nt lds\n"
    "v_mfma_f32_16x16x32_bf16 v[204:207], a[92:95], a[60:63], v[220:223]\n"
    "v_exp_f32 v198, v198\n"
    "v_mfma_f32_16x16x32_bf16 v[204:207], a[88:91], a[56:59], v[204:207]\n"
    "ds_read_b64_tr_b16 a[98:99], v229 offset:20480\n"
    "v_cvt_pk_bf16_f32 v182, v196, v197\n"
    "v_mfma_f32_16x16x32_bf16 v[204:207], a[84:87], a[52:55], v[204:207]\n"
    "v_exp_f32 v199, v199\n"
    "v_mfma_f32_16x16x32_bf16 v[204:207], a[80:83], a[48:51], v[204:207]\n"
    "s_waitcnt lgkmcnt(6)\n"
    "ds_read_b128 a[76:79], v228 offset:33536\n"
    "v_cvt_pk_bf16_f32 v183, v198, v199\n"
    "v_mfma_f32_16x16x32_bf16 v[112:115], a[104:107], v[128:131], v[112:115]\n"
    "s_waitcnt lgkmcnt(4)\n"
    "v_exp_f32 v200, v200\n"
    "v_mfma_f32_16x16x32_bf16 v[112:115], a[108:111], v[144:147], v[112:115]\n"
    "ds_read_b64_tr_b16 a[100:101], v229 offset:24576\n"
    "v_add_f32 v226, v226, v200\n"
    "v_mfma_f32_16x16x32_bf16 v[116:119], a[108:111], v[148:151], v[116:119]\n"
    "v_exp_f32 v201, v201\n"
    "v_mfma_f32_16x16x32_bf16 v[116:119], a[104:107], v[132:135], v[116:119]\n"
    "ds_read_b64_tr_b16 a[102:103], v229 offset:28672\n"
    "v_add_f32 v226, v226, v201\n"
    "v_mfma_f32_16x16x32_bf16 v[120:123], a[104:107], v[136:139], v[120:123]\n"
    "v_exp_f32 v202, v202\n"
    "v_mfma_f32_16x16x32_bf16 v[120:123], a[108:111], v[152:155], v[120:123]\n"
    "ds_read_b128 a[80:83], v228 offset:36864\n"
    "v_add_f32 v226, v226, v202\n"
    "v_mfma_f32_16x16x32_bf16 v[124:127], a[108:111], v[156:159], v[124:127]\n"
    "v_exp_f32 v203, v203\n"
    "v_mfma_f32_16x16x32_bf16 v[124:127], a[104:107], v[140:143], v[124:127]\n"
    "v_cvt_pk_bf16_f32 v186, v200, v201\n"
    "v_add_f32 v226, v226, v203\n"
    "v_mfma_f32_16x16x32_bf16 v[192:195], a[64:67], a[0:3], v[208:211]\n"
    "s_mov_b32 m0, s56\n"
    "v_exp_f32 v204, v204\n"
    "v_mfma_f32_16x16x32_bf16 v[192:195], a[68:71], a[4:7], v[192:195]\n"
    "s_waitcnt lgkmcnt(6)\n"
    "ds_read_b64_tr_b16 a[104:105], v230 offset:16384\n"
    "v_cvt_pk_bf16_f32 v187, v202, v203\n"
    "v_mfma_f32_16x16x32_bf16 v[192:195], a[72:75], a[8:11], v[192:195]\n"
    "s_waitcnt lgkmcnt(4)\n"
    "v_exp_f32 v205, v205\n"
    "v_mfma_f32_16x16x32_bf16 v[192:195], a[76:79], a[12:15], v[192:195]\n"
    "s_nop 0\n"
    "buffer_load_dwordx4 v231, s[16:19], s58 offen offset:2048 nt lds\n"
    "v_mfma_f32_16x16x32_bf16 v[196:199], a[76:79], a[28:31], v[212:215]\n"
    "v_exp_f32 v206, v206\n"
    "v_mfma_f32_16x16x32_bf16 v[196:199], a[72:75], a[24:27], v[196:199]\n"
    "ds_read_b64_tr_b16 a[106:107], v230 offset:20480\n"
    "v_cvt_pk_bf16_f32 v190, v204, v205\n"
    "v_mfma_f32_16x16x32_bf16 v[196:199], a[68:71], a[20:23], v[196:199]\n"
    "s_add_u32 m0, s51, 147456\n"
    "v_exp_f32 v207, v207\n"
    "v_mfma_f32_16x16x32_bf16 v[196:199], a[64:67], a[16:19], v[196:199]\n"
    "ds_read_b128 a[84:87], v228 offset:37120\n"
    "v_cvt_pk_bf16_f32 v191, v206, v207\n"
    "v_mfma_f32_16x16x32_bf16 v[0:3], a[96:99], v[160:163], v[0:3]\n"
    "s_waitcnt lgkmcnt(4)\n"
    "v_exp_f32 v192, v192\n"
    "v_mfma_f32_16x16x32_bf16 v[0:3], a[100:103], v[176:179], v[0:3]\n"
    "s_xor_b32 s53, s88, 81920\n"
    "ds_read_b64_tr_b16 a[108:109], v230 offset:24576\n"
    "v_add_f32 v224, v224, v192\n"
    "v_mfma_f32_16x16x32_bf16 v[4:7], a[100:103], v[180:183], v[4:7]\n"
    "s_memrealtime s[8:9]\n"
    "buffer_load_dwordx4 v234, s[24:27], s53 offen offset:0  lds\n"
    "v_exp_f32 v193, v193\n"
    "v_mfma_f32_16x16x32_bf16 v[4:7], a[96:99], v[164:167], v[4:7]\n"
    "ds_read_b64_tr_b16 a[110:111], v230 offset:28672\n"
    "v_add_f32 v224, v224, v193\n"
    "v_mfma_f32_16x16x32_bf16 v[8:11], a[96:99], v[168:171], v[8:11]\n"
    "v_exp_f32 v194, v194\n"
    "v_mfma_f32_16x16x32_bf16 v[8:11], a[100:103], v[184:187], v[8:11]\n"
    "ds_read_b128 a[88:91], v228 offset:37376\n"
    "v_add_f32 v224, v224, v194\n"
    "v_mfma_f32_16x16x32_bf16 v[12:15], a[100:103], v[188:191], v[12:15]\n"
    "v_exp_f32 v195, v195\n"
    "v_mfma_f32_16x16x32_bf16 v[12:15], a[96:99], v[172:175], v[12:15]\n"
    "v_cvt_pk_bf16_f32 v128, v192, v193\n"
    "v_add_f32 v224, v224, v195\n"
    "v_mfma_f32_16x16x32_bf16 v[200:203], a[64:67], a[32:35], v[216:219]\n"
    "v_exp_f32 v196, v196\n"
    "v_mfma_f32_16x16x32_bf16 v[200:203], a[68:71], a[36:39], v[200:203]\n"
    "ds_read_b64_tr_b16 a[96:97], v229 offset:16640\n"
    "v_cvt_pk_bf16_f32 v129, v194, v195\n"
    "v_mfma_f32_16x16x32_bf16 v[200:203], a[72:75], a[40:43], v[200:203]\n"
    "s_mov_b32 m0, s56\n"
    "v_exp_f32 v197, v197\n"
    "v_mfma_f32_16x16x32_bf16 v[200:203], a[76:79], a[44:47], v[200:203]\n"
    "buffer_load_dwordx4 v232, s[16:19], s58 offen offset:3072 nt lds\n"
    "v_mfma_f32_16x16x32_bf16 v[204:207], a[76:79], a[60:63], v[220:223]\n"
    "v_exp_f32 v198, v198\n"
    "v_mfma_f32_16x16x32_bf16 v[204:207], a[72:75], a[56:59], v[204:207]\n"
    "s_add_u32 s58, s58, 16384\n"
    "ds_read_b64_tr_b16 a[98:99], v229 offset:20736\n"
    "v_cvt_pk_bf16_f32 v132, v196, v197\n"
    "v_mfma_f32_16x16x32_bf16 v[204:207], a[68:71], a[52:55], v[204:207]\n"
    "s_and_b32 s58, s58, 0x3ffff\n"
    "v_exp_f32 v199, v199\n"
    "v_mfma_f32_16x16x32_bf16 v[204:207], a[64:67], a[48:51], v[204:207]\n"
    "s_waitcnt lgkmcnt(6)\n"
    "ds_read_b128 a[92:95], v228 offset:37632\n"
    "v_cvt_pk_bf16_f32 v133, v198, v199\n"
    "v_mfma_f32_16x16x32_bf16 v[16:19], a[104:107], v[160:163], v[16:19]\n"
    "s_waitcnt lgkmcnt(4)\n"
    "v_exp_f32 v200, v200\n"
    "v_mfma_f32_16x16x32_bf16 v[16:19], a[108:111], v[176:179], v[16:19]\n"
    "s_add_u32 m0, s51, 147456\n"
    "ds_read_b64_tr_b16 a[100:101], v229 offset:24832\n"
    "v_add_f32 v226, v226, v200\n"
    "v_mfma_f32_16x16x32_bf16 v[20:23], a[108:111], v[180:183], v[20:23]\n"
    "buffer_load_dwordx4 v235, s[24:27], s53 offen offset:1024  lds\n"
    "v_exp_f32 v201, v201\n"
    "v_mfma_f32_16x16x32_bf16 v[20:23], a[104:107], v[164:167], v[20:23]\n"
    "s_add_u32 s56, s56, 16384\n"
    "ds_read_b64_tr_b16 a[102:103], v229 offset:28928\n"
    "v_add_f32 v226, v226, v201\n"
    "v_mfma_f32_16x16x32_bf16 v[24:27], a[104:107], v[168:171], v[24:27]\n"
    "s_and_b32 s56, s56, 0xffff\n"
    "v_exp_f32 v202, v202\n"
    "v_mfma_f32_16x16x32_bf16 v[24:27], a[108:111], v[184:187], v[24:27]\n"
    "ds_read_b128 a[64:67], v228 offset:40960\n"
    "v_add_f32 v226, v226, v202\n"
    "v_mfma_f32_16x16x32_bf16 v[28:31], a[108:111], v[188:191], v[28:31]\n"
    "v_exp_f32 v203, v203\n"
    "v_mfma_f32_16x16x32_bf16 v[28:31], a[104:107], v[172:175], v[28:31]\n"
    "v_cvt_pk_bf16_f32 v136, v200, v201\n"
    "v_add_f32 v226, v226, v203\n"
    "v_mfma_f32_16x16x32_bf16 v[192:195], a[80:83], a[0:3], v[208:211]\n"
    "s_mov_b32 m0, s57\n"
    "v_exp_f32 v204, v204\n"
    "v_mfma_f32_16x16x32_bf16 v[192:195], a[84:87], a[4:7], v[192:195]\n"
    "s_waitcnt lgkmcnt(6)\n"
    "ds_read_b64_tr_b16 a[104:105], v230 offset:16640\n"
    "v_cvt_pk_bf16_f32 v137, v202, v203\n"
    "v_mfma_f32_16x16x32_bf16 v[192:195], a[88:91], a[8:11], v[192:195]\n"
    "s_waitcnt lgkmcnt(4)\n"
    "v_exp_f32 v205, v205\n"
    "v_mfma_f32_16x16x32_bf16 v[192:195], a[92:95], a[12:15], v[192:195]\n"
    "buffer_load_dwordx4 v231, s[20:23], s59 offen offset:0 nt lds\n"
    "v_mfma_f32_16x16x32_bf16 v[196:199], a[92:95], a[28:31], v[212:215]\n"
    "v_exp_f32 v206, v206\n"
    "v_mfma_f32_16x16x32_bf16 v[196:199], a[88:91], a[24:27], v[196:199]\n"
    "ds_read_b64_tr_b16 a[106:107], v230 offset:20736\n"
    "v_cvt_pk_bf16_f32 v140, v204, v205\n"
    "v_mfma_f32_16x16x32_bf16 v[196:199], a[84:87], a[20:23], v[196:199]\n"
    "v_exp_f32 v207, v207\n"
    "v_mfma_f32_16x16x32_bf16 v[196:199], a[80:83], a[16:19], v[196:199]\n"
    "ds_read_b128 a[68:71], v228 offset:41216\n"
    "v_cvt_pk_bf16_f32 v141, v206, v207\n"
    "v_mfma_f32_16x16x32_bf16 a[116:119], a[112:115], v[164:167], a[116:119]\n"
    "s_waitcnt vmcnt(9)\n"
    "ds_read_b64_tr_b16 a[192:193], v242 offset:0\n"
    "ds_read_b64_tr_b16 a[196:197], v243 offset:0\n"
    "v_mfma_f32_16x16x32_bf16 a[116:119], a[112:115], v[180:183], a[116:119]\n"
    "ds_read_b64_tr_b16 a[200:201], v242 offset:256\n"
    "ds_read_b64_tr_b16 a[204:205], v243 offset:256\n"
    "v_mfma_f32_16x16x32_bf16 v[32:35], a[96:99], v[160:163], v[32:35]\n"
    "s_waitcnt lgkmcnt(8)\n"
    "v_exp_f32 v192, v192\n"
    "v_mfma_f32_16x16x32_bf16 v[32:35], a[100:103], v[176:179], v[32:35]\n"
    "s_add_u32 m0, s51, 147456\n"
    "ds_read_b64_tr_b16 a[108:109], v230 offset:24832\n"
    "v_add_f32 v224, v224, v192\n"
    "v_mfma_f32_16x16x32_bf16 v[36:39], a[100:103], v[180:183], v[36:39]\n"
    "buffer_load_dwordx4 v236, s[24:27], s53 offen offset:2048  lds\n"
    "v_exp_f32 v193, v193\n"
    "v_mfma_f32_16x16x32_bf16 v[36:39], a[96:99], v[164:167], v[36:39]\n"
    "ds_read_b64_tr_b16 a[110:111], v230 offset:28928\n"
    "v_add_f32 v224, v224, v193\n"
    "v_mfma_f32_16x16x32_bf16 v[40:43], a[96:99], v[168:171], v[40:43]\n"
    "v_exp_f32 v194, v194\n"
    "v_mfma_f32_16x16x32_bf16 v[40:43], a[100:103], v[184:187], v[40:43]\n"
    "ds_read_b128 a[72:75], v228 offset:41472\n"
    "v_add_f32 v224, v224, v194\n"
    "v_mfma_f32_16x16x32_bf16 v[44:47], a[100:103], v[188:191], v[44:47]\n"
    "v_exp_f32 v195, v195\n"
    "v_mfma_f32_16x16x32_bf16 v[44:47], a[96:99], v[172:175], v[44:47]\n"
    "v_cvt_pk_bf16_f32 v130, v192, v193\n"
    "v_add_f32 v224, v224, v195\n"
    "v_mfma_f32_16x16x32_bf16 v[200:203], a[80:83], a[32:35], v[216:219]\n"
    "v_exp_f32 v196, v196\n"
    "v_mfma_f32_16x16x32_bf16 v[200:203], a[84:87], a[36:39], v[200:203]\n"
    "ds_read_b64_tr_b16 a[96:97], v229 offset:16896\n"
    "v_cvt_pk_bf16_f32 v131, v194, v195\n"
    "v_mfma_f32_16x16x32_bf16 v[200:203], a[88:91], a[40:43], v[200:203]\n"
    "s_mov_b32 m0, s57\n"
    "v_exp_f32 v197, v197\n"
    "v_mfma_f32_16x16x32_bf16 v[200:203], a[92:95], a[44:47], v[200:203]\n"
    "buffer_load_dwordx4 v232, s[20:23], s59 offen offset:1024 nt lds\n"
    "v_mfma_f32_16x16x32_bf16 v[204:207], a[92:95], a[60:63], v[220:223]\n"
    "v_exp_f32 v198, v198\n"
    "v_mfma_f32_16x16x32_bf16 v[204:207], a[88:91], a[56:59], v[204:207]\n"
    "ds_read_b64_tr_b16 a[98:99], v229 offset:20992\n"
    "v_cvt_pk_bf16_f32 v134, v196, v197\n"
    "v_mfma_f32_16x16x32_bf16 v[204:207], a[84:87], a[52:55], v[204:207]\n"
    "v_exp_f32 v199, v199\n"
    "v_mfma_f32_16x16x32_bf16 v[204:207], a[80:83], a[48:51], v[204:207]\n"
    "s_waitcnt lgkmcnt(10)\n"
    "ds_read_b128 a[76:79], v228 offset:41728\n"
    "v_cvt_pk_bf16_f32 v135, v198, v199\n"
    "v_mfma_f32_16x16x32_bf16 v[48:51], a[104:107], v[160:163], v[48:51]\n"
    "s_waitcnt lgkmcnt(4)\n"
    "v_exp_f32 v200, v200\n"
    "v_mfma_f32_16x16x32_bf16 v[48:51], a[108:111], v[176:179], v[48:51]\n"
    "s_add_u32 m0, s51, 147456\n"
    "ds_read_b64_tr_b16 a[100:101], v229 offset:25088\n"
    "v_add_f32 v226, v226, v200\n"
    "v_mfma_f32_16x16x32_bf16 v[52:55], a[108:111], v[180:183], v[52:55]\n"
    "s_sub_u32 s11, s60, s8\n"
    "buffer_load_dwordx4 v237, s[24:27], s53 offen offset:3072  lds\n"
    "v_exp_f32 v201, v201\n"
    "v_mfma_f32_16x16x32_bf16 v[52:55], a[104:107], v[164:167], v[52:55]\n"
    "s_add_u32 s60, s60, 142\n"
    "ds_read_b64_tr_b16 a[102:103], v229 offset:29184\n"
    "v_add_f32 v226, v226, v201\n"
    "v_mfma_f32_16x16x32_bf16 v[56:59], a[104:107], v[168:171], v[56:59]\n"
    "s_cmp_gt_i32 s11, 2\n"
    "buffer_load_dword v249, v254, s[36:39], 0 offen sc1\n"
    "v_exp_f32 v202, v202\n"
    "v_mfma_f32_16x16x32_bf16 v[56:59], a[108:111], v[184:187], v[56:59]\n"
    "s_cbranch_scc1 Lpa5x%=\n"
    "Lpb5x%=:\n"
    "ds_read_b128 a[80:83], v228 offset:45056\n"
    "v_add_f32 v226, v226, v202\n"
    "v_mfma_f32_16x16x32_bf16 v[60:63], a[108:111], v[188:191], v[60:63]\n"
    "v_exp_f32 v203, v203\n"
    "v_mfma_f32_16x16x32_bf16 v[60:63], a[104:107], v[172:175], v[60:63]\n"
    "v_cvt_pk_bf16_f32 v138, v200, v201\n"
    "v_add_f32 v226, v226, v203\n"
    "v_mfma_f32_16x16x32_bf16 v[192:195], a[64:67], a[0:3], v[208:211]\n"
    "s_mov_b32 m0, s57\n"
    "v_exp_f32 v204, v204\n"
    "v_mfma_f32_16x16x32_bf16 v[192:195], a[68:71], a[4:7], v[192:195]\n"
    "s_waitcnt lgkmcnt(6)\n"
    "ds_read_b64_tr_b16 a[104:105], v230 offset:16896\n"
    "v_cvt_pk_bf16_f32 v139, v202, v203\n"
    "v_mfma_f32_16x16x32_bf16 v[192:195], a[72:75], a[8:11], v[192:195]\n"
    "s_waitcnt lgkmcnt(4)\n"
    "v_exp_f32 v205, v205\n"
    "v_mfma_f32_16x16x32_bf16 v[192:195], a[76:79], a[12:15], v[192:195]\n"
    "buffer_load_dwordx4 v231, s[20:23], s59 offen offset:2048 nt lds\n"
    "v_mfma_f32_16x16x32_bf16 v[196:199], a[76:79], a[28:31], v[212:215]\n"
    "v_exp_f32 v206, v206\n"
    "v_mfma_f32_16x16x32_bf16 v[196:199], a[72:75], a[24:27], v[196:199]\n"
    "ds_read_b64_tr_b16 a[106:107], v230 offset:20992\n"
    "v_cvt_pk_bf16_f32 v142, v204, v205\n"
    "v_mfma_f32_16x16x32_bf16 v[196:199], a[68:71], a[20:23], v[196:199]\n"
    "v_exp_f32 v207, v207\n"
    "v_mfma_f32_16x16x32_bf16 v[196:199], a[64:67], a[16:19], v[196:199]\n"
    "ds_read_b128 a[84:87], v228 offset:45312\n"
    "v_cvt_pk_bf16_f32 v143, v206, v207\n"
    "v_mfma_f32_16x16x32_bf16 v[64:67], a[96:99], v[160:163], v[64:67]\n"
    "s_waitcnt lgkmcnt(4)\n"
    "v_exp_f32 v192, v192\n"
    "v_mfma_f32_16x16x32_bf16 v[64:67], a[100:103], v[176:179], v[64:67]\n"
    "ds_read_b64_tr_b16 a[108:109], v230 offset:25088\n"
    "v_add_f32 v224, v224, v192\n"
    "v_mfma_f32_16x16x32_bf16 v[68:71], a[100:103], v[180:183], v[68:71]\n"
    "v_exp_f32 v193, v193\n"
    "v_mfma_f32_16x16x32_bf16 v[68:71], a[96:99], v[164:167], v[68:71]\n"
    "ds_read_b64_tr_b16 a[110:111], v230 offset:29184\n"
    "v_add_f32 v224, v224, v193\n"
    "v_mfma_f32_16x16x32_bf16 v[72:75], a[96:99], v[168:171], v[72:75]\n"
    "v_exp_f32 v194, v194\n"
    "v_mfma_f32_16x16x32_bf16 v[72:75], a[100:103], v[184:187], v[72:75]\n"
    "ds_read_b128 a[88:91], v228 offset:45568\n"
    "v_add_f32 v224, v224, v194\n"
    "v_mfma_f32_16x16x32_bf16 v[76:79], a[100:103], v[188:191], v[76:79]\n"
    "v_exp_f32 v195, v195\n"
    "v_mfma_f32_16x16x32_bf16 v[76:79], a[96:99], v[172:175], v[76:79]\n"
    "v_cvt_pk_bf16_f32 v144, v192, v193\n"
    "v_add_f32 v224, v224, v195\n"
    "v_mfma_f32_16x16x32_bf16 v[200:203], a[64:67], a[32:35], v[216:219]\n"
    "v_exp_f32 v196, v196\n"
    "v_mfma_f32_16x16x32_bf16 v[200:203], a[68:71], a[36:39], v[200:203]\n"
    "ds_read_b64_tr_b16 a[96:97], v229 offset:17152\n"
    "v_cvt_pk_bf16_f32 v145, v194, v195\n"
    "v_mfma_f32_16x16x32_bf16 v[200:203], a[72:75], a[40:43], v[200:203]\n"
    "v_exp_f32 v197, v197\n"
    "v_mfma_f32_16x16x32_bf16 v[200:203], a[76:79], a[44:47], v[200:203]\n"
    "buffer_load_dwordx4 v232, s[20:23], s59 offen offset:3072 nt lds\n"
    "v_mfma_f32_16x16x32_bf16 v[204:207], a[76:79], a[60:63], v[220:223]\n"
    "v_exp_f32 v198, v198\n"
    "v_mfma_f32_16x16x32_bf16 v[204:207], a[72:75], a[56:59], v[204:207]\n"
    "s_add_u32 s59, s59, 16384\n"
    "ds_read_b64_tr_b16 a[98:99], v229 offset:21248\n"
    "v_cvt_pk_bf16_f32 v148, v196, v197\n"
    "v_mfma_f32_16x16x32_bf16 v[204:207], a[68:71], a[52:55], v[204:207]\n"
    "s_and_b32 s59, s59, 0x3ffff\n"
    "v_exp_f32 v199, v199\n"
    "v_mfma_f32_16x16x32_bf16 v[204:207], a[64:67], a[48:51], v[204:207]\n"
    "ds_read_b128 a[92:95], v228 offset:45824\n"
    "v_cvt_pk_bf16_f32 v149, v198, v199\n"
    "v_mfma_f32_16x16x32_bf16 a[120:123], a[112:115], v[172:175], a[120:123]\n"
    "ds_read_b64_tr_b16 a[208:209], v242 offset:512\n"
    "ds_read_b64_tr_b16 a[212:213], v243 offset:512\n"
    "v_mfma_f32_16x16x32_bf16 a[120:123], a[112:115], v[188:191], a[120:123]\n"
    "s_waitcnt lgkmcnt(9)\n"
    "ds_read_b64_tr_b16 a[216:217], v242 offset:768\n"
    "ds_read_b64_tr_b16 a[220:221], v243 offset:768\n"
    "v_mfma_f32_16x16x32_bf16 v[80:83], a[104:107], v[160:163], v[80:83]\n"
    "s_waitcnt lgkmcnt(8)\n"
    "v_exp_f32 v200, v200\n"
    "v_mfma_f32_16x16x32_bf16 v[80:83], a[108:111], v[176:179], v[80:83]\n"
    "s_add_u32 s57, s57, 16384\n"
    "ds_read_b64_tr_b16 a[100:101], v229 offset:25344\n"
    "v_add_f32 v226, v226, v200\n"
    "v_mfma_f32_16x16x32_bf16 v[84:87], a[108:111], v[180:183], v[84:87]\n"
    "s_bitset0_b32 s57, 17\n"
    "v_exp_f32 v201, v201\n"
    "v_mfma_f32_16x16x32_bf16 v[84:87], a[104:107], v[164:167], v[84:87]\n"
    "ds_read_b64_tr_b16 a[102:103], v229 offset:29440\n"
    "v_add_f32 v226, v226, v201\n"
    "v_mfma_f32_16x16x32_bf16 v[88:91], a[104:107], v[168:171], v[88:91]\n"
    "s_waitcnt vmcnt(13) lgkmcnt(6)\n"
    "v_exp_f32 v202, v202\n"
    "v_mfma_f32_16x16x32_bf16 v[88:91], a[108:111], v[184:187], v[88:91]\n"
    "s_barrier\n"
    "ds_read_b128 a[64:67], v228 offset:49152\n"
    "v_add_f32 v226, v226, v202\n"
    "v_mfma_f32_16x16x32_bf16 v[92:95], a[108:111], v[188:191], v[92:95]\n"
    "s_bitset1_b32 s57, 16\n"
    "v_exp_f32 v203, v203\n"
    "v_mfma_f32_16x16x32_bf16 v[92:95], a[104:107], v[172:175], v[92:95]\n"
    "v_cvt_pk_bf16_f32 v152, v200, v201\n"
    "v_add_f32 v226, v226, v203\n"
    "v_mfma_f32_16x16x32_bf16 v[192:195], a[80:83], a[0:3], v[208:211]\n"
    "s_mov_b32 m0, s56\n"
    "v_exp_f32 v204, v204\n"
    "v_mfma_f32_16x16x32_bf16 v[192:195], a[84:87], a[4:7], v[192:195]\n"
    "ds_read_b64_tr_b16 a[104:105], v230 offset:17152\n"
    "v_cvt_pk_bf16_f32 v153, v202, v203\n"
    "v_mfma_f32_16x16x32_bf16 v[192:195], a[88:91], a[8:11], v[192:195]\n"
    "v_exp_f32 v205, v205\n"
    "v_mfma_f32_16x16x32_bf16 v[192:195], a[92:95], a[12:15], v[192:195]\n"
    "buffer_load_dwordx4 v231, s[16:19], s58 offen offset:0 nt lds\n"
    "v_mfma_f32_16x16x32_bf16 v[196:199], a[92:95], a[28:31], v[212:215]\n"
    "v_exp_f32 v206, v206\n"
    "v_mfma_f32_16x16x32_bf16 v[196:199], a[88:91], a[24:27], v[196:199]\n"
    "ds_read_b64_tr_b16 a[106:107], v230 offset:21248\n"
    "v_cvt_pk_bf16_f32 v156, v204, v205\n"
    "v_mfma_f32_16x16x32_bf16 v[196:199], a[84:87], a[20:23], v[196:199]\n"
    "v_exp_f32 v207, v207\n"
    "v_mfma_f32_16x16x32_bf16 v[196:199], a[80:83], a[16:19], v[196:199]\n"
    "ds_read_b128 a[68:71], v228 offset:49408\n"
    "v_cvt_pk_bf16_f32 v157, v206, v207\n"
    "v_mfma_f32_16x16x32_bf16 v[96:99], a[96:99], v[160:163], v[96:99]\n"
    "s_waitcnt lgkmcnt(4)\n"
    "v_exp_f32 v192, v192\n"
    "v_mfma_f32_16x16x32_bf16 v[96:99], a[100:103], v[176:179], v[96:99]\n"
    "ds_read_b64_tr_b16 a[108:109], v230 offset:25344\n"
    "v_add_f32 v224, v224, v192\n"
    "v_mfma_f32_16x16x32_bf16 v[100:103], a[100:103], v[180:183], v[100:103]\n"
    "v_exp_f32 v193, v193\n"
    "v_mfma_f32_16x16x32_bf16 v[100:103], a[96:99], v[164:167], v[100:103]\n"
    "ds_read_b64_tr_b16 a[110:111], v230 offset:29440\n"
    "v_add_f32 v224, v224, v193\n"
    "v_mfma_f32_16x16x32_bf16 v[104:107], a[96:99], v[168:171], v[104:107]\n"
    "v_exp_f32 v194, v194\n"
    "v_mfma_f32_16x16x32_bf16 v[104:107], a[100:103], v[184:187], v[104:107]\n"
    "ds_read_b128 a[72:75], v228 offset:49664\n"
    "v_add_f32 v224, v224, v194\n"
    "v_mfma_f32_16x16x32_bf16 v[108:111], a[100:103], v[188:191], v[108:111]\n"
    "v_exp_f32 v195, v195\n"
    "v_mfma_f32_16x16x32_bf16 v[108:111], a[96:99], v[172:175], v[108:111]\n"
    "v_cvt_pk_bf16_f32 v146, v192, v193\n"
    "v_add_f32 v224, v224, v195\n"
    "v_mfma_f32_16x16x32_bf16 v[200:203], a[80:83], a[32:35], v[216:219]\n"
    "v_exp_f32 v196, v196\n"
    "v_mfma_f32_16x16x32_bf16 v[200:203], a[84:87], a[36:39], v[200:203]\n"
    "ds_read_b64_tr_b16 a[96:97], v229 offset:32768\n"
    "v_cvt_pk_bf16_f32 v147, v194, v195\n"
    "v_mfma_f32_16x16x32_bf16 v[200:203], a[88:91], a[40:43], v[200:203]\n"
    "v_exp_f32 v197, v197\n"
    "v_mfma_f32_16x16x32_bf16 v[200:203], a[92:95], a[44:47], v[200:203]\n"
    "buffer_load_dwordx4 v232, s[16:19], s58 offen offset:1024 nt lds\n"
    "v_mfma_f32_16x16x32_bf16 v[204:207], a[92:95], a[60:63], v[220:223]\n"
    "v_exp_f32 v198, v198\n"
    "v_mfma_f32_16x16x32_bf16 v[204:207], a[88:91], a[56:59], v[204:207]\n"
    "ds_read_b64_tr_b16 a[98:99], v229 offset:36864\n"
    "v_cvt_pk_bf16_f32 v150, v196, v197\n"
    "v_mfma_f32_16x16x32_bf16 v[204:207], a[84:87], a[52:55], v[204:207]\n"
    "v_exp_f32 v199, v199\n"
    "v_mfma_f32_16x16x32_bf16 v[204:207], a[80:83], a[48:51], v[204:207]\n"
    "s_waitcnt lgkmcnt(6)\n"
    "ds_read_b128 a[76:79], v228 offset:49920\n"
    "v_cvt_pk_bf16_f32 v151, v198, v199\n"
    "v_mfma_f32_16x16x32_bf16 v[112:115], a[104:107], v[160:163], v[112:115]\n"
    "s_waitcnt lgkmcnt(4)\n"
    "v_exp_f32 v200, v200\n"
    "v_mfma_f32_16x16x32_bf16 v[112:115], a[108:111], v[176:179], v[112:115]\n"
    "ds_read_b64_tr_b16 a[100:101], v229 offset:40960\n"
    "v_add_f32 v226, v226, v200\n"
    "v_mfma_f32_16x16x32_bf16 v[116:119], a[108:111], v[180:183], v[116:119]\n"
    "v_exp_f32 v201, v201\n"
    "v_mfma_f32_16x16x32_bf16 v[116:119], a[104:107], v[164:167], v[116:119]\n"
    "ds_read_b64_tr_b16 a[102:103], v229 offset:45056\n"
    "v_add_f32 v226, v226, v201\n"
    "v_mfma_f32_16x16x32_bf16 v[120:123], a[104:107], v[168:171], v[120:123]\n"
    "v_exp_f32 v202, v202\n"
    "v_mfma_f32_16x16x32_bf16 v[120:123], a[108:111], v[184:187], v[120:123]\n"
    "ds_read_b128 a[80:83], v228 offset:53248\n"
    "v_add_f32 v226, v226, v202\n"
    "v_mfma_f32_16x16x32_bf16 v[124:127], a[108:111], v[188:191], v[124:127]\n"
    "v_exp_f32 v203, v203\n"
    "v_mfma_f32_16x16x32_bf16 v[124:127], a[104:107], v[172:175], v[124:127]\n"
    "v_cvt_pk_bf16_f32 v154, v200, v201\n"
    "v_add_f32 v226, v226, v203\n"
    "v_mfma_f32_16x16x32_bf16 v[192:195], a[64:67], a[0:3], v[208:211]\n"
    "s_mov_b32 m0, s56\n"
    "v_exp_f32 v204, v204\n"
    "v_mfma_f32_16x16x32_bf16 v[192:195], a[68:71], a[4:7], v[192:195]\n"
    "s_waitcnt lgkmcnt(6)\n"
    "ds_read_b64_tr_b16 a[104:105], v230 offset:32768\n"
    "v_cvt_pk_bf16_f32 v155, v202, v203\n"
    "v_mfma_f32_16x16x32_bf16 v[192:195], a[72:75], a[8:11], v[192:195]\n"
    "s_waitcnt lgkmcnt(4)\n"
    "v_exp_f32 v205, v205\n"
    "v_mfma_f32_16x16x32_bf16 v[192:195], a[76:79], a[12:15], v[192:195]\n"
    "s_nop 0\n"
    "buffer_load_dwordx4 v231, s[16:19], s58 offen offset:2048 nt lds\n"
    "v_mfma_f32_16x16x32_bf16 v[196:199], a[76:79], a[28:31], v[212:215]\n"
    "v_exp_f32 v206, v206\n"
    "v_mfma_f32_16x16x32_bf16 v[196:199], a[72:75], a[24:27], v[196:199]\n"
    "Lpg7%=:\n"
    "s_cbranch_execz Lwb0%=\n"
    "ds_read_b64_tr_b16 a[106:107], v230 offset:36864\n"
    "v_cvt_pk_bf16_f32 v158, v204, v205\n"
    "v_mfma_f32_16x16x32_bf16 v[196:199], a[68:71], a[20:23], v[196:199]\n"
    "s_add_u32 m0, s51, 131072\n"
    "v_exp_f32 v207, v207\n"
    "v_mfma_f32_16x16x32_bf16 v[196:199], a[64:67], a[16:19], v[196:199]\n"
    "ds_read_b128 a[84:87], v228 offset:53504\n"
    "v_cvt_pk_bf16_f32 v159, v206, v207\n"
    "v_mfma_f32_16x16x32_bf16 v[0:3], a[96:99], v[128:131], v[0:3]\n"
    "s_waitcnt lgkmcnt(4)\n"
    "v_exp_f32 v192, v192\n"
    "v_mfma_f32_16x16x32_bf16 v[0:3], a[100:103], v[144:147], v[0:3]\n"
    "s_xor_b32 s53, s88, 98304\n"
    "ds_read_b64_tr_b16 a[108:109], v230 offset:40960\n"
    "v_add_f32 v224, v224, v192\n"
    "v_mfma_f32_16x16x32_bf16 v[4:7], a[100:103], v[148:151], v[4:7]\n"
    "s_memrealtime s[8:9]\n"
    "buffer_load_dwordx4 v234, s[24:27], s53 offen offset:0  lds\n"
    "v_exp_f32 v193, v193\n"
    "v_mfma_f32_16x16x32_bf16 v[4:7], a[96:99], v[132:135], v[4:7]\n"
    "ds_read_b64_tr_b16 a[110:111], v230 offset:45056\n"
    "v_add_f32 v224, v224, v193\n"
    "v_mfma_f32_16x16x32_bf16 v[8:11], a[96:99], v[136:139], v[8:11]\n"
    "v_exp_f32 v194, v194\n"
    "v_mfma_f32_16x16x32_bf16 v[8:11], a[100:103], v[152:155], v[8:11]\n"
    "ds_read_b128 a[88:91], v228 offset:53760\n"
    "v_add_f32 v224, v224, v194\n"
    "v_mfma_f32_16x16x32_bf16 v[12:15], a[100:103], v[156:159], v[12:15]\n"
    "v_exp_f32 v195, v195\n"
    "v_mfma_f32_16x16x32_bf16 v[12:15], a[96:99], v[140:143], v[12:15]\n"
    "v_cvt_pk_bf16_f32 v160, v192, v193\n"
    "v_add_f32 v224, v224, v195\n"
    "v_mfma_f32_16x16x32_bf16 v[200:203], a[64:67], a[32:35], v[216:219]\n"
    "v_exp_f32 v196, v196\n"
    "v_mfma_f32_16x16x32_bf16 v[200:203], a[68:71], a[36:39], v[200:203]\n"
    "ds_read_b64_tr_b16 a[96:97], v229 offset:33024\n"
    "v_cvt_pk_bf16_f32 v161, v194, v195\n"
    "v_mfma_f32_16x16x32_bf16 v[200:203], a[72:75], a[40:43], v[200:203]\n"
    "s_mov_b32 m0, s56\n"
    "v_exp_f32 v197, v197\n"
    "v_mfma_f32_16x16x32_bf16 v[200:203], a[76:79], a[44:47], v[200:203]\n"
    "buffer_load_dwordx4 v232, s[16:19], s58 offen offset:3072 nt lds\n"
    "v_mfma_f32_16x16x32_bf16 v[204:207], a[76:79], a[60:63], v[220:223]\n"
    "v_exp_f32 v198, v198\n"
    "v_mfma_f32_16x16x32_bf16 v[204:207], a[72:75], a[56:59], v[204:207]\n"
    "s_add_u32 s58, s58, 16384\n"
    "ds_read_b64_tr_b16 a[98:99], v229 offset:37120\n"
    "v_cvt_pk_bf16_f32 v164, v196, v197\n"
    "v_mfma_f32_16x16x32_bf16 v[204:207], a[68:71], a[52:55], v[204:207]\n"
    "s_and_b32 s58, s58, 0x3ffff\n"
    "v_exp_f32 v199, v199\n"
    "v_mfma_f32_16x16x32_bf16 v[204:207], a[64:67], a[48:51], v[204:207]\n"
    "s_waitcnt lgkmcnt(6)\n"
    "ds_read_b128 a[92:95], v228 offset:54016\n"
    "v_cvt_pk_bf16_f32 v165, v198, v199\n"
    "v_mfma_f32_16x16x32_bf16 v[16:19], a[104:107], v[128:131], v[16:19]\n"
    "s_waitcnt lgkmcnt(4)\n"
    "v_exp_f32 v200, v200\n"
    "v_mfma_f32_16x16x32_bf16 v[16:19], a[108:111], v[144:147], v[16:19]\n"
    "s_add_u32 m0, s51, 131072\n"
    "ds_read_b64_tr_b16 a[100:101], v229 offset:41216\n"
    "v_add_f32 v226, v226, v200\n"
    "v_mfma_f32_16x16x32_bf16 v[20:23], a[108:111], v[148:151], v[20:23]\n"
    "buffer_load_dwordx4 v235, s[24:27], s53 offen offset:1024  lds\n"
    "v_exp_f32 v201, v201\n"
    "v_mfma_f32_16x16x32_bf16 v[20:23], a[104:107], v[132:135], v[20:23]\n"
    "s_add_u32 s56, s56, 16384\n"
    "ds_read_b64_tr_b16 a[102:103], v229 offset:45312\n"
    "v_add_f32 v226, v226, v201\n"
    "v_mfma_f32_16x16x32_bf16 v[24:27], a[104:107], v[136:139], v[24:27]\n"
    "s_and_b32 s56, s56, 0xffff\n"
    "v_exp_f32 v202, v202\n"
    "v_mfma_f32_16x16x32_bf16 v[24:27], a[108:111], v[152:155], v[24:27]\n"
    "ds_read_b128 a[64:67], v228 offset:57344\n"
    "v_add_f32 v226, v226, v202\n"
    "v_mfma_f32_16x16x32_bf16 v[28:31], a[108:111], v[156:159], v[28:31]\n"
    "v_exp_f32 v203, v203\n"
    "v_mfma_f32_16x16x32_bf16 v[28:31], a[104:107], v[140:143], v[28:31]\n"
    "v_cvt_pk_bf16_f32 v168, v200, v201\n"
    "v_add_f32 v226, v226, v203\n"
    "v_mfma_f32_16x16x32_bf16 v[192:195], a[80:83], a[0:3], v[208:211]\n"
    "s_mov_b32 m0, s57\n"
    "v_exp_f32 v204, v204\n"
    "v_mfma_f32_16x16x32_bf16 v[192:195], a[84:87], a[4:7], v[192:195]\n"
    "s_waitcnt lgkmcnt(6)\n"
    "ds_read_b64_tr_b16 a[104:105], v230 offset:33024\n"
    "v_cvt_pk_bf16_f32 v169, v202, v203\n"
    "v_mfma_f32_16x16x32_bf16 v[192:195], a[88:91], a[8:11], v[192:195]\n"
    "s_waitcnt lgkmcnt(4)\n"
    "v_exp_f32 v205, v205\n"
    "v_mfma_f32_16x16x32_bf16 v[192:195], a[92:95], a[12:15], v[192:195]\n"
    "buffer_load_dwordx4 v231, s[20:23], s59 offen offset:0 nt lds\n"
    "v_mfma_f32_16x16x32_bf16 v[196:199], a[92:95], a[28:31], v[212:215]\n"
    "v_exp_f32 v206, v206\n"
    "v_mfma_f32_16x16x32_bf16 v[196:199], a[88:91], a[24:27], v[196:199]\n"
    "ds_read_b64_tr_b16 a[106:107], v230 offset:37120\n"
    "v_cvt_pk_bf16_f32 v172, v204, v205\n"
    "v_mfma_f32_16x16x32_bf16 v[196:199], a[84:87], a[20:23], v[196:199]\n"
    "v_exp_f32 v207, v207\n"
    "v_mfma_f32_16x16x32_bf16 v[196:199], a[80:83], a[16:19], v[196:199]\n"
    "ds_read_b128 a[68:71], v228 offset:57600\n"
    "v_cvt_pk_bf16_f32 v173, v206, v207\n"
    "v_mfma_f32_16x16x32_bf16 a[116:119], a[112:115], v[132:135], a[116:119]\n"
    "s_waitcnt vmcnt(10)\n"
    "ds_read_b64_tr_b16 a[194:195], v242 offset:16384\n"
    "ds_read_b64_tr_b16 a[198:199], v243 offset:16384\n"
    "v_mfma_f32_16x16x32_bf16 a[116:119], a[112:115], v[148:151], a[116:119]\n"
    "ds_read_b64_tr_b16 a[202:203], v242 offset:16640\n"
    "ds_read_b64_tr_b16 a[206:207], v243 offset:16640\n"
    "v_mfma_f32_16x16x32_bf16 v[32:35], a[96:99], v[128:131], v[32:35]\n"
    "s_waitcnt lgkmcnt(8)\n"
    "v_exp_f32 v192, v192\n"
    "v_mfma_f32_16x16x32_bf16 v[32:35], a[100:103], v[144:147], v[32:35]\n"
    "s_add_u32 m0, s51, 131072\n"
    "ds_read_b64_tr_b16 a[108:109], v230 offset:41216\n"
    "v_add_f32 v224, v224, v192\n"
    "v_mfma_f32_16x16x32_bf16 v[36:39], a[100:103], v[148:151], v[36:39]\n"
    "buffer_load_dwordx4 v236, s[24:27], s53 offen offset:2048  lds\n"
    "v_exp_f32 v193, v193\n"
    "v_mfma_f32_16x16x32_bf16 v[36:39], a[96:99], v[132:135], v[36:39]\n"
    "ds_read_b64_tr_b16 a[110:111], v230 offset:45312\n"
    "v_add_f32 v224, v224, v193\n"
    "v_mfma_f32_16x16x32_bf16 v[40:43], a[96:99], v[136:139], v[40:43]\n"
    "v_exp_f32 v194, v194\n"
    "v_mfma_f32_16x16x32_bf16 v[40:43], a[100:103], v[152:155], v[40:43]\n"
    "ds_read_b128 a[72:75], v228 offset:57856\n"
    "v_add_f32 v224, v224, v194\n"
    "v_mfma_f32_16x16x32_bf16 v[44:47], a[100:103], v[156:159], v[44:47]\n"
    "v_exp_f32 v195, v195\n"
    "v_mfma_f32_16x16x32_bf16 v[44:47], a[96:99], v[140:143], v[44:47]\n"
    "v_cvt_pk_bf16_f32 v162, v192, v193\n"
    "v_add_f32 v224, v224, v195\n"
    "v_mfma_f32_16x16x32_bf16 v[200:203], a[80:83], a[32:35], v[216:219]\n"
    "v_exp_f32 v196, v196\n"
    "v_mfma_f32_16x16x32_bf16 v[200:203], a[84:87], a[36:39], v[200:203]\n"
    "ds_read_b64_tr_b16 a[96:97], v229 offset:33280\n"
    "v_cvt_pk_bf16_f32 v163, v194, v195\n"
    "v_mfma_f32_16x16x32_bf16 v[200:203], a[88:91], a[40:43], v[200:203]\n"
    "s_mov_b32 m0, s57\n"
    "v_exp_f32 v197, v197\n"
    "v_mfma_f32_16x16x32_bf16 v[200:203], a[92:95], a[44:47], v[200:203]\n"
    "buffer_load_dwordx4 v232, s[20:23], s59 offen offset:1024 nt lds\n"
    "v_mfma_f32_16x16x32_bf16 v[204:207], a[92:95], a[60:63], v[220:223]\n"
    "v_exp_f32 v198, v198\n"
    "v_mfma_f32_16x16x32_bf16 v[204:207], a[88:91], a[56:59], v[204:207]\n"
    "ds_read_b64_tr_b16 a[98:99], v229 offset:37376\n"
    "v_cvt_pk_bf16_f32 v166, v196, v197\n"
    "v_mfma_f32_16x16x32_bf16 v[204:207], a[84:87], a[52:55], v[204:207]\n"
    "v_exp_f32 v199, v199\n"
    "v_mfma_f32_16x16x32_bf16 v[204:207], a[80:83], a[48:51], v[204:207]\n"
    "s_waitcnt lgkmcnt(10)\n"
    "ds_read_b128 a[76:79], v228 offset:58112\n"
    "v_cvt_pk_bf16_f32 v167, v198, v199\n"
    "v_mfma_f32_16x16x32_bf16 v[48:51], a[104:107], v[128:131], v[48:51]\n"
    "s_waitcnt lgkmcnt(4)\n"
    "v_exp_f32 v200, v200\n"
    "v_mfma_f32_16x16x32_bf16 v[48:51], a[108:111], v[144:147], v[48:51]\n"
    "s_add_u32 m0, s51, 131072\n"
    "ds_read_b64_tr_b16 a[100:101], v229 offset:41472\n"
    "v_add_f32 v226, v226, v200\n"
    "v_mfma_f32_16x16x32_bf16 v[52:55], a[108:111], v[148:151], v[52:55]\n"
    "s_sub_u32 s11, s60, s8\n"
    "buffer_load_dwordx4 v237, s[24:27], s53 offen offset:3072  lds\n"
    "v_exp_f32 v201, v201\n"
    "v_mfma_f32_16x16x32_bf16 v[52:55], a[104:107], v[132:135], v[52:55]\n"
    "s_add_u32 s60, s60, 142\n"
    "ds_read_b64_tr_b16 a[102:103], v229 offset:45568\n"
    "v_add_f32 v226, v226, v201\n"
    "v_mfma_f32_16x16x32_bf16 v[56:59], a[104:107], v[136:139], v[56:59]\n"
    "s_cmp_gt_i32 s11, 2\n"
    "v_exp_f32 v202, v202\n"
    "v_mfma_f32_16x16x32_bf16 v[56:59], a[108:111], v[152:155], v[56:59]\n"
    "s_cbranch_scc1 Lpa6x%=\n"
    "Lpb6x%=:\n"
    "ds_read_b128 a[80:83], v228 offset:61440\n"
    "v_add_f32 v226, v226, v202\n"
    "v_mfma_f32_16x16x32_bf16 v[60:63], a[108:111], v[156:159], v[60:63]\n"
    "v_exp_f32 v203, v203\n"
    "v_mfma_f32_16x16x32_bf16 v[60:63], a[104:107], v[140:143], v[60:63]\n"
    "v_cvt_pk_bf16_f32 v170, v200, v201\n"
    "v_add_f32 v226, v226, v203\n"
    "v_mfma_f32_16x16x32_bf16 v[192:195], a[64:67], a[0:3], v[208:211]\n"
    "s_mov_b32 m0, s57\n"
    "v_exp_f32 v204, v204\n"
    "v_mfma_f32_16x16x32_bf16 v[192:195], a[68:71], a[4:7], v[192:195]\n"
    "s_waitcnt lgkmcnt(6)\n"
    "ds_read_b64_tr_b16 a[104:105], v230 offset:33280\n"
    "v_cvt_pk_bf16_f32 v171, v202, v203\n"
    "v_mfma_f32_16x16x32_bf16 v[192:195], a[72:75], a[8:11], v[192:195]\n"
    "s_waitcnt lgkmcnt(4)\n"
    "v_exp_f32 v205, v205\n"
    "v_mfma_f32_16x16x32_bf16 v[192:195], a[76:79], a[12:15], v[192:195]\n"
    "buffer_load_dwordx4 v231, s[20:23], s59 offen offset:2048 nt lds\n"
    "v_mfma_f32_16x16x32_bf16 v[196:199], a[76:79], a[28:31], v[212:215]\n"
    "v_exp_f32 v206, v206\n"
    "v_mfma_f32_16x16x32_bf16 v[196:199], a[72:75], a[24:27], v[196:199]\n"
    "ds_read_b64_tr_b16 a[106:107], v230 offset:37376\n"
    "v_cvt_pk_bf16_f32 v174, v204, v205\n"
    "v_mfma_f32_16x16x32_bf16 v[196:199], a[68:71], a[20:23], v[196:199]\n"
    "v_exp_f32 v207, v207\n"
    "v_mfma_f32_16x16x32_bf16 v[196:199], a[64:67], a[16:19], v[196:199]\n"
    "ds_read_b128 a[84:87], v228 offset:61696\n"
    "v_cvt_pk_bf16_f32 v175, v206, v207\n"
    "v_mfma_f32_16x16x32_bf16 v[64:67], a[96:99], v[128:131], v[64:67]\n"
    "s_waitcnt lgkmcnt(4)\n"
    "v_exp_f32 v192, v192\n"
    "v_mfma_f32_16x16x32_bf16 v[64:67], a[100:103], v[144:147], v[64:67]\n"
    "ds_read_b64_tr_b16 a[108:109], v230 offset:41472\n"
    "v_add_f32 v224, v224, v192\n"
    "v_mfma_f32_16x16x32_bf16 v[68:71], a[100:103], v[148:151], v[68:71]\n"
    "v_exp_f32 v193, v193\n"
    "v_mfma_f32_16x16x32_bf16 v[68:71], a[96:99], v[132:135], v[68:71]\n"
    "ds_read_b64_tr_b16 a[110:111], v230 offset:45568\n"
    "v_add_f32 v224, v224, v193\n"
    "v_mfma_f32_16x16x32_bf16 v[72:75], a[96:99], v[136:139], v[72:75]\n"
    "v_exp_f32 v194, v194\n"
    "v_mfma_f32_16x16x32_bf16 v[72:75], a[100:103], v[152:155], v[72:75]\n"
    "ds_read_b128 a[88:91], v228 offset:61952\n"
    "v_add_f32 v224, v224, v194\n"
    "v_mfma_f32_16x16x32_bf16 v[76:79], a[100:103], v[156:159], v[76:79]\n"
    "v_exp_f32 v195, v195\n"
    "v_mfma_f32_16x16x32_bf16 v[76:79], a[96:99], v[140:143], v[76:79]\n"
    "v_cvt_pk_bf16_f32 v176, v192, v193\n"
    "v_add_f32 v224, v224, v195\n"
    "v_mfma_f32_16x16x32_bf16 v[200:203], a[64:67], a[32:35], v[216:219]\n"
    "v_exp_f32 v196, v196\n"
    "v_mfma_f32_16x16x32_bf16 v[200:203], a[68:71], a[36:39], v[200:203]\n"
    "ds_read_b64_tr_b16 a[96:97], v229 offset:33536\n"
    "v_cvt_pk_bf16_f32 v177, v194, v195\n"
    "v_mfma_f32_16x16x32_bf16 v[200:203], a[72:75], a[40:43], v[200:203]\n"
    "v_exp_f32 v197, v197\n"
    "v_mfma_f32_16x16x32_bf16 v[200:203], a[76:79], a[44:47], v[200:203]\n"
    "buffer_load_dwordx4 v232, s[20:23], s59 offen offset:3072 nt lds\n"
    "v_mfma_f32_16x16x32_bf16 v[204:207], a[76:79], a[60:63], v[220:223]\n"
    "v_exp_f32 v198, v198\n"
    "v_mfma_f32_16x16x32_bf16 v[204:207], a[72:75], a[56:59], v[204:207]\n"
    "s_add_u32 s59, s59, 16384\n"
    "ds_read_b64_tr_b16 a[98:99], v229 offset:37632\n"
    "v_cvt_pk_bf16_f32 v180, v196, v197\n"
    "v_mfma_f32_16x16x32_bf16 v[204:207], a[68:71], a[52:55], v[204:207]\n"
    "s_and_b32 s59, s59, 0x3ffff\n"
    "v_exp_f32 v199, v199\n"
    "v_mfma_f32_16x16x32_bf16 v[204:207], a[64:67], a[48:51], v[204:207]\n"
    "ds_read_b128 a[92:95], v228 offset:62208\n"
    "v_cvt_pk_bf16_f32 v181, v198, v199\n"
    "v_mfma_f32_16x16x32_bf16 a[120:123], a[112:115], v[140:143], a[120:123]\n"
    "ds_read_b64_tr_b16 a[210:211], v242 offset:16896\n"
    "ds_read_b64_tr_b16 a[214:215], v243 offset:16896\n"
    "v_mfma_f32_16x16x32_bf16 a[120:123], a[112:115], v[156:159], a[120:123]\n"
    "s_waitcnt lgkmcnt(9)\n"
    "ds_read_b64_tr_b16 a[218:219], v242 offset:17152\n"
    "ds_read_b64_tr_b16 a[222:223], v243 offset:17152\n"
    "v_mfma_f32_16x16x32_bf16 v[80:83], a[104:107], v[128:131], v[80:83]\n"
    "s_waitcnt lgkmcnt(8)\n"
    "v_exp_f32 v200, v200\n"
    "v_mfma_f32_16x16x32_bf16 v[80:83], a[108:111], v[144:147], v[80:83]\n"
    "s_add_u32 s57, s57, 16384\n"
    "ds_read_b64_tr_b16 a[100:101], v229 offset:41728\n"
    "v_add_f32 v226, v226, v200\n"
    "v_mfma_f32_16x16x32_bf16 v[84:87], a[108:111], v[148:151], v[84:87]\n"
    "s_bitset0_b32 s57, 17\n"
    "v_exp_f32 v201, v201\n"
    "v_mfma_f32_16x16x32_bf16 v[84:87], a[104:107], v[132:135], v[84:87]\n"
    "ds_read_b64_tr_b16 a[102:103], v229 offset:45824\n"
    "v_add_f32 v226, v226, v201\n"
    "v_mfma_f32_16x16x32_bf16 v[88:91], a[104:107], v[136:139], v[88:91]\n"
    "s_waitcnt vmcnt(12) lgkmcnt(6)\n"
    "v_exp_f32 v202, v202\n"
    "v_mfma_f32_16x16x32_bf16 v[88:91], a[108:111], v[152:155], v[88:91]\n"
    "s_barrier\n"
    "ds_read_b128 a[64:67], v228 offset:0\n"
    "v_add_f32 v226, v226, v202\n"
    "v_mfma_f32_16x16x32_bf16 v[92:95], a[108:111], v[156:159], v[92:95]\n"
    "s_bitset1_b32 s57, 16\n"
    "v_exp_f32 v203, v203\n"
    "v_mfma_f32_16x16x32_bf16 v[92:95], a[104:107], v[140:143], v[92:95]\n"
    "s_waitcnt vmcnt(14)\n"
    "v_cvt_pk_bf16_f32 v184, v200, v201\n"
    "v_add_f32 v226, v226, v203\n"
    "v_mfma_f32_16x16x32_bf16 v[192:195], a[80:83], a[0:3], v[208:211]\n"
    "s_mov_b32 m0, s56\n"
    "v_exp_f32 v204, v204\n"
    "v_lshrrev_b32 v249, v253, v249\n"
    "v_mfma_f32_16x16x32_bf16 v[192:195], a[84:87], a[4:7], v[192:195]\n"
    "ds_read_b64_tr_b16 a[104:105], v230 offset:33536\n"
    "v_cvt_pk_bf16_f32 v185, v202, v203\n"
    "v_mfma_f32_16x16x32_bf16 v[192:195], a[88:91], a[8:11], v[192:195]\n"
    "v_exp_f32 v205, v205\n"
    "v_and_b32 v249, 1, v249\n"
    "v_mfma_f32_16x16x32_bf16 v[192:195], a[92:95], a[12:15], v[192:195]\n"
    "buffer_load_dwordx4 v231, s[16:19], s58 offen offset:0 nt lds\n"
    "v_mfma_f32_16x16x32_bf16 v[196:199], a[92:95], a[28:31], v[212:215]\n"
    "v_exp_f32 v206, v206\n"
    "v_mfma_f32_16x16x32_bf16 v[196:199], a[88:91], a[24:27], v[196:199]\n"
    "ds_read_b64_tr_b16 a[106:107], v230 offset:37632\n"
    "v_cvt_pk_bf16_f32 v188, v204, v205\n"
    "v_mfma_f32_16x16x32_bf16 v[196:199], a[84:87], a[20:23], v[196:199]\n"
    "v_exp_f32 v207, v207\n"
    "v_mfma_f32_16x16x32_bf16 v[196:199], a[80:83], a[16:19], v[196:199]\n"
    "ds_read_b128 a[68:71], v228 offset:256\n"
    "v_cvt_pk_bf16_f32 v189, v206, v207\n"
    "v_mfma_f32_16x16x32_bf16 v[96:99], a[96:99], v[128:131], v[96:99]\n"
    "s_waitcnt lgkmcnt(4)\n"
    "v_exp_f32 v192, v192\n"
    "v_mfma_f32_16x16x32_bf16 v[96:99], a[100:103], v[144:147], v[96:99]\n"
    "ds_read_b64_tr_b16 a[108:109], v230 offset:41728\n"
    "v_add_f32 v224, v224, v192\n"
    "v_mfma_f32_16x16x32_bf16 v[100:103], a[100:103], v[148:151], v[100:103]\n"
    "v_exp_f32 v193, v193\n"
    "v_mfma_f32_16x16x32_bf16 v[100:103], a[96:99], v[132:135], v[100:103]\n"
    "ds_read_b64_tr_b16 a[110:111], v230 offset:45824\n"
    "v_add_f32 v224, v224, v193\n"
    "v_mfma_f32_16x16x32_bf16 v[104:107], a[96:99], v[136:139], v[104:107]\n"
    "v_exp_f32 v194, v194\n"
    "v_mfma_f32_16x16x32_bf16 v[104:107], a[100:103], v[152:155], v[104:107]\n"
    "ds_read_b128 a[72:75], v228 offset:512\n"
    "v_add_f32 v224, v224, v194\n"
    "v_mfma_f32_16x16x32_bf16 v[108:111], a[100:103], v[156:159], v[108:111]\n"
    "v_exp_f32 v195, v195\n"
    "v_mfma_f32_16x16x32_bf16 v[108:111], a[96:99], v[140:143], v[108:111]\n"
    "v_cvt_pk_bf16_f32 v178, v192, v193\n"
    "v_add_f32 v224, v224, v195\n"
    "v_mfma_f32_16x16x32_bf16 v[200:203], a[80:83], a[32:35], v[216:219]\n"
    "v_exp_f32 v196, v196\n"
    "v_mfma_f32_16x16x32_bf16 v[200:203], a[84:87], a[36:39], v[200:203]\n"
    "ds_read_b64_tr_b16 a[96:97], v229 offset:49152\n"
    "v_cvt_pk_bf16_f32 v179, v194, v195\n"
    "v_mfma_f32_16x16x32_bf16 v[200:203], a[88:91], a[40:43], v[200:203]\n"
    "v_exp_f32 v197, v197\n"
    "v_mfma_f32_16x16x32_bf16 v[200:203], a[92:95], a[44:47], v[200:203]\n"
    "buffer_load_dwordx4 v232, s[16:19], s58 offen offset:1024 nt lds\n"
    "v_mfma_f32_16x16x32_bf16 v[204:207], a[92:95], a[60:63], v[220:223]\n"
    "v_exp_f32 v198, v198\n"
    "v_mfma_f32_16x16x32_bf16 v[204:207], a[88:91], a[56:59], v[204:207]\n"
    "ds_read_b64_tr_b16 a[98:99], v229 offset:53248\n"
    "v_cvt_pk_bf16_f32 v182, v196, v197\n"
    "v_mfma_f32_16x16x32_bf16 v[204:207], a[84:87], a[52:55], v[204:207]\n"
    "v_exp_f32 v199, v199\n"
    "v_mfma_f32_16x16x32_bf16 v[204:207], a[80:83], a[48:51], v[204:207]\n"
    "s_waitcnt lgkmcnt(6)\n"
    "ds_read_b128 a[76:79], v228 offset:768\n"
    "v_cvt_pk_bf16_f32 v183, v198, v199\n"
    "v_mfma_f32_16x16x32_bf16 v[112:115], a[104:107], v[128:131], v[112:115]\n"
    "s_waitcnt lgkmcnt(4)\n"
    "v_exp_f32 v200, v200\n"
    "v_mfma_f32_16x16x32_bf16 v[112:115], a[108:111], v[144:147], v[112:115]\n"
    "ds_read_b64_tr_b16 a[100:101], v229 offset:57344\n"
    "v_add_f32 v226, v226, v200\n"
    "v_mfma_f32_16x16x32_bf16 v[116:119], a[108:111], v[148:151], v[116:119]\n"
    "v_exp_f32 v201, v201\n"
    "v_mfma_f32_16x16x32_bf16 v[116:119], a[104:107], v[132:135], v[116:119]\n"
    "ds_read_b64_tr_b16 a[102:103], v229 offset:61440\n"
    "v_add_f32 v226, v226, v201\n"
    "v_mfma_f32_16x16x32_bf16 v[120:123], a[104:107], v[136:139], v[120:123]\n"
    "v_exp_f32 v202, v202\n"
    "v_mfma_f32_16x16x32_bf16 v[120:123], a[108:111], v[152:155], v[120:123]\n"
    "ds_read_b128 a[80:83], v228 offset:4096\n"
    "v_add_f32 v226, v226, v202\n"
    "v_mfma_f32_16x16x32_bf16 v[124:127], a[108:111], v[156:159], v[124:127]\n"
    "v_exp_f32 v203, v203\n"
    "v_mfma_f32_16x16x32_bf16 v[124:127], a[104:107], v[140:143], v[124:127]\n"
    "v_cvt_pk_bf16_f32 v186, v200, v201\n"
    "v_add_f32 v226, v226, v203\n"
    "v_mfma_f32_16x16x32_bf16 v[192:195], a[64:67], a[0:3], v[208:211]\n"
    "s_mov_b32 m0, s56\n"
    "v_exp_f32 v204, v204\n"
    "v_mfma_f32_16x16x32_bf16 v[192:195], a[68:71], a[4:7], v[192:195]\n"
    "s_waitcnt lgkmcnt(6)\n"
    "ds_read_b64_tr_b16 a[104:105], v230 offset:49152\n"
    "v_cvt_pk_bf16_f32 v187, v202, v203\n"
    "v_mfma_f32_16x16x32_bf16 v[192:195], a[72:75], a[8:11], v[192:195]\n"
    "s_waitcnt lgkmcnt(4)\n"
    "v_exp_f32 v205, v205\n"
    "v_mfma_f32_16x16x32_bf16 v[192:195], a[76:79], a[12:15], v[192:195]\n"
    "s_nop 0\n"
    "buffer_load_dwordx4 v231, s[16:19], s58 offen offset:2048 nt lds\n"
    "v_mfma_f32_16x16x32_bf16 v[196:199], a[76:79], a[28:31], v[212:215]\n"
    "v_exp_f32 v206, v206\n"
    "v_mfma_f32_16x16x32_bf16 v[196:199], a[72:75], a[24:27], v[196:199]\n"
    "ds_read_b64_tr_b16 a[106:107], v230 offset:53248\n"
    "v_cvt_pk_bf16_f32 v190, v204, v205\n"
    "v_mfma_f32_16x16x32_bf16 v[196:199], a[68:71], a[20:23], v[196:199]\n"
    "s_add_u32 m0, s51, 147456\n"
    "v_exp_f32 v207, v207\n"
    "v_mfma_f32_16x16x32_bf16 v[196:199], a[64:67], a[16:19], v[196:199]\n"
    "ds_read_b128 a[84:87], v228 offset:4352\n"
    "v_cvt_pk_bf16_f32 v191, v206, v207\n"
    "v_mfma_f32_16x16x32_bf16 v[0:3], a[96:99], v[160:163], v[0:3]\n"
    "s_waitcnt lgkmcnt(4)\n"
    "v_exp_f32 v192, v192\n"
    "v_mfma_f32_16x16x32_bf16 v[0:3], a[100:103], v[176:179], v[0:3]\n"
    "s_xor_b32 s53, s88, 114688\n"
    "ds_read_b64_tr_b16 a[108:109], v230 offset:57344\n"
    "v_add_f32 v224, v224, v192\n"
    "v_mfma_f32_16x16x32_bf16 v[4:7], a[100:103], v[180:183], v[4:7]\n"
    "s_memrealtime s[8:9]\n"
    "buffer_load_dwordx4 v234, s[24:27], s53 offen offset:0  lds\n"
    "v_exp_f32 v193, v193\n"
    "v_mfma_f32_16x16x32_bf16 v[4:7], a[96:99], v[164:167], v[4:7]\n"
    "ds_read_b64_tr_b16 a[110:111], v230 offset:61440\n"
    "v_add_f32 v224, v224, v193\n"
    "v_mfma_f32_16x16x32_bf16 v[8:11], a[96:99], v[168:171], v[8:11]\n"
    "buffer_load_dwordx4 v235, s[24:27], s53 offen offset:1024  lds\n"
    "v_exp_f32 v194, v194\n"
    "v_mfma_f32_16x16x32_bf16 v[8:11], a[100:103], v[184:187], v[8:11]\n"
    "ds_read_b128 a[88:91], v228 offset:4608\n"
    "v_add_f32 v224, v224, v194\n"
    "v_mfma_f32_16x16x32_bf16 v[12:15], a[100:103], v[188:191], v[12:15]\n"
    "v_exp_f32 v195, v195\n"
    "v_mfma_f32_16x16x32_bf16 v[12:15], a[96:99], v[172:175], v[12:15]\n"
    "v_cvt_pk_bf16_f32 v128, v192, v193\n"
    "v_add_f32 v224, v224, v195\n"
    "v_mfma_f32_16x16x32_bf16 v[200:203], a[64:67], a[32:35], v[216:219]\n"
    "v_exp_f32 v196, v196\n"
    "v_mfma_f32_16x16x32_bf16 v[200:203], a[68:71], a[36:39], v[200:203]\n"
    "ds_read_b64_tr_b16 a[96:97], v229 offset:49408\n"
    "v_cvt_pk_bf16_f32 v129, v194, v195\n"
    "v_mfma_f32_16x16x32_bf16 v[200:203], a[72:75], a[40:43], v[200:203]\n"
    "s_mov_b32 m0, s56\n"
    "v_exp_f32 v197, v197\n"
    "v_mfma_f32_16x16x32_bf16 v[200:203], a[76:79], a[44:47], v[200:203]\n"
    "buffer_load_dwordx4 v232, s[16:19], s58 offen offset:3072 nt lds\n"
    "v_mfma_f32_16x16x32_bf16 v[204:207], a[76:79], a[60:63], v[220:223]\n"
    "v_exp_f32 v198, v198\n"
    "v_mfma_f32_16x16x32_bf16 v[204:207], a[72:75], a[56:59], v[204:207]\n"
    "s_add_u32 s58, s58, 16384\n"
    "ds_read_b64_tr_b16 a[98:99], v229 offset:53504\n"
    "v_cvt_pk_bf16_f32 v132, v196, v197\n"
    "v_mfma_f32_16x16x32_bf16 v[204:207], a[68:71], a[52:55], v[204:207]\n"
    "s_and_b32 s58, s58, 0x3ffff\n"
    "v_exp_f32 v199, v199\n"
    "v_mfma_f32_16x16x32_bf16 v[204:207], a[64:67], a[48:51], v[204:207]\n"
    "s_waitcnt lgkmcnt(6)\n"
    "ds_read_b128 a[92:95], v228 offset:4864\n"
    "v_cvt_pk_bf16_f32 v133, v198, v199\n"
    "v_mfma_f32_16x16x32_bf16 v[16:19], a[104:107], v[160:163], v[16:19]\n"
    "s_waitcnt lgkmcnt(4)\n"
    "v_exp_f32 v200, v200\n"
    "v_mfma_f32_16x16x32_bf16 v[16:19], a[108:111], v[176:179], v[16:19]\n"
    "s_add_u32 m0, s51, 147456\n"
    "ds_read_b64_tr_b16 a[100:101], v229 offset:57600\n"
    "v_add_f32 v226, v226, v200\n"
    "v_mfma_f32_16x16x32_bf16 v[20:23], a[108:111], v[180:183], v[20:23]\n"
    "buffer_load_dwordx4 v236, s[24:27], s53 offen offset:2048  lds\n"
    "v_exp_f32 v201, v201\n"
    "v_mfma_f32_16x16x32_bf16 v[20:23], a[104:107], v[164:167], v[20:23]\n"
    "s_add_u32 s56, s56, 16384\n"
    "ds_read_b64_tr_b16 a[102:103], v229 offset:61696\n"
    "v_add_f32 v226, v226, v201\n"
    "v_mfma_f32_16x16x32_bf16 v[24:27], a[104:107], v[168:171], v[24:27]\n"
    "s_and_b32 s56, s56, 0xffff\n"
    "buffer_load_dwordx4 v237, s[24:27], s53 offen offset:3072  lds\n"
    "v_exp_f32 v202, v202\n"
    "v_mfma_f32_16x16x32_bf16 v[24:27], a[108:111], v[184:187], v[24:27]\n"
    "ds_read_b128 a[64:67], v228 offset:8192\n"
    "v_add_f32 v226, v226, v202\n"
    "v_mfma_f32_16x16x32_bf16 v[28:31], a[108:111], v[188:191], v[28:31]\n"
    "v_exp_f32 v203, v203\n"
    "v_cmp_eq_u32 vcc, 0, v249\n"
    "v_mfma_f32_16x16x32_bf16 v[28:31], a[104:107], v[172:175], v[28:31]\n"
    "s_nop 0\n"
    "v_cvt_pk_bf16_f32 v136, v200, v201\n"
    "v_add_f32 v226, v226, v203\n"
    "v_mfma_f32_16x16x32_bf16 v[192:195], a[80:83], a[0:3], v[208:211]\n"
    "s_cmp_lg_u32 s64, 0\n"
    "v_exp_f32 v204, v204\n"
    "v_mfma_f32_16x16x32_bf16 v[192:195], a[84:87], a[4:7], v[192:195]\n"
    "s_waitcnt lgkmcnt(6)\n"
    "ds_read_b64_tr_b16 a[104:105], v230 offset:49408\n"
    "v_cvt_pk_bf16_f32 v137, v202, v203\n"
    "v_mfma_f32_16x16x32_bf16 v[192:195], a[88:91], a[8:11], v[192:195]\n"
    "s_mov_b32 m0, s57\n"
    "v_exp_f32 v205, v205\n"
    "s_waitcnt lgkmcnt(4)\n"
    "v_mfma_f32_16x16x32_bf16 v[192:195], a[92:95], a[12:15], v[192:195]\n"
    "s_cselect_b32 s66, vcc_lo, s66\n"
    "buffer_load_dwordx4 v231, s[20:23], s59 offen offset:0 nt lds\n"
    "v_mfma_f32_16x16x32_bf16 v[196:199], a[92:95], a[28:31], v[212:215]\n"
    "v_exp_f32 v206, v206\n"
    "v_mov_b32 v249, s66\n"
    "v_mfma_f32_16x16x32_bf16 v[196:199], a[88:91], a[24:27], v[196:199]\n"
    "s_nop 0\n"
    "ds_read_b64_tr_b16 a[106:107], v230 offset:53504\n"
    "v_cvt_pk_bf16_f32 v140, v204, v205\n"
    "v_mfma_f32_16x16x32_bf16 v[196:199], a[84:87], a[20:23], v[196:199]\n"
    "buffer_store_dword v249, v225, s[36:39], 0 offen sc1\n"
    "v_exp_f32 v207, v207\n"
    "v_mfma_f32_16x16x32_bf16 v[196:199], a[80:83], a[16:19], v[196:199]\n"
    "ds_read_b128 a[68:71], v228 offset:8448\n"
    "v_cvt_pk_bf16_f32 v141, v206, v207\n"
    "v_mfma_f32_16x16x32_bf16 a[116:119], a[112:115], v[164:167], a[116:119]\n"
    "s_waitcnt vmcnt(12)\n"
    "ds_read_b64_tr_b16 a[224:225], v242 offset:0\n"
    "ds_read_b64_tr_b16 a[228:229], v243 offset:0\n"
    "v_mfma_f32_16x16x32_bf16 a[116:119], a[112:115], v[180:183], a[116:119]\n"
    "ds_read_b64_tr_b16 a[232:233], v242 offset:256\n"
    "ds_read_b64_tr_b16 a[236:237], v243 offset:256\n"
    "v_mfma_f32_16x16x32_bf16 v[32:35], a[96:99], v[160:163], v[32:35]\n"
    "s_waitcnt lgkmcnt(8)\n"
    "v_exp_f32 v192, v192\n"
    "v_mfma_f32_16x16x32_bf16 v[32:35], a[100:103], v[176:179], v[32:35]\n"
    "ds_read_b64_tr_b16 a[108:109], v230 offset:57600\n"
    "v_add_f32 v224, v224, v192\n"
    "v_mfma_f32_16x16x32_bf16 v[36:39], a[100:103], v[180:183], v[36:39]\n"
    "v_exp_f32 v193, v193\n"
    "v_mfma_f32_16x16x32_bf16 v[36:39], a[96:99], v[164:167], v[36:39]\n"
    "ds_read_b64_tr_b16 a[110:111], v230 offset:61696\n"
    "v_add_f32 v224, v224, v193\n"
    "v_mfma_f32_16x16x32_bf16 v[40:43], a[96:99], v[168:171], v[40:43]\n"
    "v_exp_f32 v194, v194\n"
    "v_mfma_f32_16x16x32_bf16 v[40:43], a[100:103], v[184:187], v[40:43]\n"
    "ds_read_b128 a[72:75], v228 offset:8704\n"
    "v_add_f32 v224, v224, v194\n"
    "v_mfma_f32_16x16x32_bf16 v[44:47], a[100:103], v[188:191], v[44:47]\n"
    "v_exp_f32 v195, v195\n"
    "v_mfma_f32_16x16x32_bf16 v[44:47], a[96:99], v[172:175], v[44:47]\n"
    "v_cvt_pk_bf16_f32 v130, v192, v193\n"
    "v_add_f32 v224, v224, v195\n"
    "v_mfma_f32_16x16x32_bf16 v[200:203], a[80:83], a[32:35], v[216:219]\n"
    "v_exp_f32 v196, v196\n"
    "v_mfma_f32_16x16x32_bf16 v[200:203], a[84:87], a[36:39], v[200:203]\n"
    "ds_read_b64_tr_b16 a[96:97], v229 offset:49664\n"
    "v_cvt_pk_bf16_f32 v131, v194, v195\n"
    "v_mfma_f32_16x16x32_bf16 v[200:203], a[88:91], a[40:43], v[200:203]\n"
    "v_exp_f32 v197, v197\n"
    "v_mfma_f32_16x16x32_bf16 v[200:203], a[92:95], a[44:47], v[200:203]\n"
    "buffer_load_dwordx4 v232, s[20:23], s59 offen offset:1024 nt lds\n"
    "v_mfma_f32_16x16x32_bf16 v[204:207], a[92:95], a[60:63], v[220:223]\n"
    "v_exp_f32 v198, v198\n"
    "v_mfma_f32_16x16x32_bf16 v[204:207], a[88:91], a[56:59], v[204:207]\n"
    "ds_read_b64_tr_b16 a[98:99], v229 offset:53760\n"
    "v_cvt_pk_bf16_f32 v134, v196, v197\n"
    "v_mfma_f32_16x16x32_bf16 v[204:207], a[84:87], a[52:55], v[204:207]\n"
    "v_exp_f32 v199, v199\n"
    "v_mfma_f32_16x16x32_bf16 v[204:207], a[80:83], a[48:51], v[204:207]\n"
    "s_waitcnt lgkmcnt(10)\n"
    "ds_read_b128 a[76:79], v228 offset:8960\n"
    "v_cvt_pk_bf16_f32 v135, v198, v199\n"
    "v_mfma_f32_16x16x32_bf16 v[48:51], a[104:107], v[160:163], v[48:51]\n"
    "s_waitcnt lgkmcnt(4)\n"
    "v_exp_f32 v200, v200\n"
    "v_mfma_f32_16x16x32_bf16 v[48:51], a[108:111], v[176:179], v[48:51]\n"
    "s_sub_u32 s11, s60, s8\n"
    "ds_read_b64_tr_b16 a[100:101], v229 offset:57856\n"
    "v_add_f32 v226, v226, v200\n"
    "v_mfma_f32_16x16x32_bf16 v[52:55], a[108:111], v[180:183], v[52:55]\n"
    "s_add_u32 s60, s60, 142\n"
    "v_exp_f32 v201, v201\n"
    "v_mfma_f32_16x16x32_bf16 v[52:55], a[104:107], v[164:167], v[52:55]\n"
    "s_cmp_gt_i32 s11, 2\n"
    "ds_read_b64_tr_b16 a[102:103], v229 offset:61952\n"
    "v_add_f32 v226, v226, v201\n"
    "v_mfma_f32_16x16x32_bf16 v[56:59], a[104:107], v[168:171], v[56:59]\n"
    "s_cbranch_scc1 Lpa7x%=\n"
    "Lpb7x%=:\n"
    "v_exp_f32 v202, v202\n"
    "v_mfma_f32_16x16x32_bf16 v[56:59], a[108:111], v[184:187], v[56:59]\n"
    "ds_read_b128 a[80:83], v228 offset:12288\n"
    "v_add_f32 v226, v226, v202\n"
    "v_mfma_f32_16x16x32_bf16 v[60:63], a[108:111], v[188:191], v[60:63]\n"
    "v_exp_f32 v203, v203\n"
    "v_mfma_f32_16x16x32_bf16 v[60:63], a[104:107], v[172:175], v[60:63]\n"
    "v_cvt_pk_bf16_f32 v138, v200, v201\n"
    "v_add_f32 v226, v226, v203\n"
    "v_mfma_f32_16x16x32_bf16 v[192:195], a[64:67], a[0:3], v[208:211]\n"
    "v_exp_f32 v204, v204\n"
    "v_mfma_f32_16x16x32_bf16 v[192:195], a[68:71], a[4:7], v[192:195]\n"
    "s_waitcnt lgkmcnt(6)\n"
    "ds_read_b64_tr_b16 a[104:105], v230 offset:49664\n"
    "v_cvt_pk_bf16_f32 v139, v202, v203\n"
    "v_mfma_f32_16x16x32_bf16 v[192:195], a[72:75], a[8:11], v[192:195]\n"
    "s_waitcnt lgkmcnt(4)\n"
    "v_exp_f32 v205, v205\n"
    "v_mfma_f32_16x16x32_bf16 v[192:195], a[76:79], a[12:15], v[192:195]\n"
    "buffer_load_dwordx4 v231, s[20:23], s59 offen offset:2048 nt lds\n"
    "v_mfma_f32_16x16x32_bf16 v[196:199], a[76:79], a[28:31], v[212:215]\n"
    "v_exp_f32 v206, v206\n"
    "v_mfma_f32_16x16x32_bf16 v[196:199], a[72:75], a[24:27], v[196:199]\n"
    "ds_read_b64_tr_b16 a[106:107], v230 offset:53760\n"
    "v_cvt_pk_bf16_f32 v142, v204, v205\n"
    "v_mfma_f32_16x16x32_bf16 v[196:199], a[68:71], a[20:23], v[196:199]\n"
    "v_exp_f32 v207, v207\n"
    "v_mfma_f32_16x16x32_bf16 v[196:199], a[64:67], a[16:19], v[196:199]\n"
    "ds_read_b128 a[84:87], v228 offset:12544\n"
    "v_cvt_pk_bf16_f32 v143, v206, v207\n"
    "v_mfma_f32_16x16x32_bf16 v[64:67], a[96:99], v[160:163], v[64:67]\n"
    "s_waitcnt lgkmcnt(4)\n"
    "v_exp_f32 v192, v192\n"
    "v_mfma_f32_16x16x32_bf16 v[64:67], a[100:103], v[176:179], v[64:67]\n"
    "ds_read_b64_tr_b16 a[108:109], v230 offset:57856\n"
    "v_add_f32 v224, v224, v192\n"
    "v_mfma_f32_16x16x32_bf16 v[68:71], a[100:103], v[180:183], v[68:71]\n"
    "v_exp_f32 v193, v193\n"
    "v_mfma_f32_16x16x32_bf16 v[68:71], a[96:99], v[164:167], v[68:71]\n"
    "ds_read_b64_tr_b16 a[110:111], v230 offset:61952\n"
    "v_add_f32 v224, v224, v193\n"
    "v_mfma_f32_16x16x32_bf16 v[72:75], a[96:99], v[168:171], v[72:75]\n"
    "v_exp_f32 v194, v194\n"
    "v_mfma_f32_16x16x32_bf16 v[72:75], a[100:103], v[184:187], v[72:75]\n"
    "ds_read_b128 a[88:91], v228 offset:12800\n"
    "v_add_f32 v224, v224, v194\n"
    "v_mfma_f32_16x16x32_bf16 v[76:79], a[100:103], v[188:191], v[76:79]\n"
    "v_exp_f32 v195, v195\n"
    "v_mfma_f32_16x16x32_bf16 v[76:79], a[96:99], v[172:175], v[76:79]\n"
    "v_cvt_pk_bf16_f32 v144, v192, v193\n"
    "v_add_f32 v224, v224, v195\n"
    "v_mfma_f32_16x16x32_bf16 v[200:203], a[64:67], a[32:35], v[216:219]\n"
    "v_exp_f32 v196, v196\n"
    "v_mfma_f32_16x16x32_bf16 v[200:203], a[68:71], a[36:39], v[200:203]\n"
    "ds_read_b64_tr_b16 a[96:97], v229 offset:49920\n"
    "v_cvt_pk_bf16_f32 v145, v194, v195\n"
    "v_mfma_f32_16x16x32_bf16 v[200:203], a[72:75], a[40:43], v[200:203]\n"
    "v_exp_f32 v197, v197\n"
    "v_mfma_f32_16x16x32_bf16 v[200:203], a[76:79], a[44:47], v[200:203]\n"
    "buffer_load_dwordx4 v232, s[20:23], s59 offen offset:3072 nt lds\n"
    "v_mfma_f32_16x16x32_bf16 v[204:207], a[76:79], a[60:63], v[220:223]\n"
    "v_exp_f32 v198, v198\n"
    "v_mfma_f32_16x16x32_bf16 v[204:207], a[72:75], a[56:59], v[204:207]\n"
    "s_add_u32 s59, s59, 16384\n"
    "ds_read_b64_tr_b16 a[98:99], v229 offset:54016\n"
    "v_cvt_pk_bf16_f32 v148, v196, v197\n"
    "v_mfma_f32_16x16x32_bf16 v[204:207], a[68:71], a[52:55], v[204:207]\n"
    "s_and_b32 s59, s59, 0x3ffff\n"
    "v_exp_f32 v199, v199\n"
    "v_mfma_f32_16x16x32_bf16 v[204:207], a[64:67], a[48:51], v[204:207]\n"
    "Lpg8%=:\n"
    "s_cbranch_execz Lwb0%=\n"
    "ds_read_b128 a[92:95], v228 offset:13056\n"
    "v_cvt_pk_bf16_f32 v149, v198, v199\n"
    "v_mfma_f32_16x16x32_bf16 a[120:123], a[112:115], v[172:175], a[120:123]\n"
    "ds_read_b64_tr_b16 a[240:241], v242 offset:512\n"
    "ds_read_b64_tr_b16 a[244:245], v243 offset:512\n"
    "v_mfma_f32_16x16x32_bf16 a[120:123], a[112:115], v[188:191], a[120:123]\n"
    "s_waitcnt lgkmcnt(9)\n"
    "ds_read_b64_tr_b16 a[248:249], v242 offset:768\n"
    "ds_read_b64_tr_b16 a[252:253], v243 offset:768\n"
    "v_mfma_f32_16x16x32_bf16 v[80:83], a[104:107], v[160:163], v[80:83]\n"
    "s_waitcnt lgkmcnt(8)\n"
    "v_exp_f32 v200, v200\n"
    "v_mfma_f32_16x16x32_bf16 v[80:83], a[108:111], v[176:179], v[80:83]\n"
    "s_add_u32 s57, s57, 16384\n"
    "ds_read_b64_tr_b16 a[100:101], v229 offset:58112\n"
    "v_add_f32 v226, v226, v200\n"
    "v_mfma_f32_16x16x32_bf16 v[84:87], a[108:111], v[180:183], v[84:87]\n"
    "s_bitset0_b32 s57, 17\n"
    "v_exp_f32 v201, v201\n"
    "v_mfma_f32_16x16x32_bf16 v[84:87], a[104:107], v[164:167], v[84:87]\n"
    "ds_read_b64_tr_b16 a[102:103], v229 offset:62208\n"
    "v_add_f32 v226, v226, v201\n"
    "v_mfma_f32_16x16x32_bf16 v[88:91], a[104:107], v[168:171], v[88:91]\n"
    "s_waitcnt vmcnt(13) lgkmcnt(6)\n"
    "v_exp_f32 v202, v202\n"
    "v_mfma_f32_16x16x32_bf16 v[88:91], a[108:111], v[184:187], v[88:91]\n"
    "s_barrier\n"
    "ds_read_b128 a[64:67], v228 offset:16384\n"
    "v_add_f32 v226, v226, v202\n"
    "v_mfma_f32_16x16x32_bf16 v[92:95], a[108:111], v[188:191], v[92:95]\n"
    "s_bitset1_b32 s57, 16\n"
    "v_exp_f32 v203, v203\n"
    "v_mfma_f32_16x16x32_bf16 v[92:95], a[104:107], v[172:175], v[92:95]\n"
    "v_cvt_pk_bf16_f32 v152, v200, v201\n"
    "v_add_f32 v226, v226, v203\n"
    "v_mfma_f32_16x16x32_bf16 v[192:195], a[80:83], a[0:3], v[208:211]\n"
    "s_mov_b32 m0, s56\n"
    "v_exp_f32 v204, v204\n"
    "v_mfma_f32_16x16x32_bf16 v[192:195], a[84:87], a[4:7], v[192:195]\n"
    "ds_read_b64_tr_b16 a[104:105], v230 offset:49920\n"
    "v_cvt_pk_bf16_f32 v153, v202, v203\n"
    "v_mfma_f32_16x16x32_bf16 v[192:195], a[88:91], a[8:11], v[192:195]\n"
    "v_exp_f32 v205, v205\n"
    "v_mfma_f32_16x16x32_bf16 v[192:195], a[92:95], a[12:15], v[192:195]\n"
    "buffer_load_dwordx4 v231, s[16:19], s58 offen offset:0 nt lds\n"
    "v_mfma_f32_16x16x32_bf16 v[196:199], a[92:95], a[28:31], v[212:215]\n"
    "v_exp_f32 v206, v206\n"
    "v_mfma_f32_16x16x32_bf16 v[196:199], a[88:91], a[24:27], v[196:199]\n"
    "ds_read_b64_tr_b16 a[106:107], v230 offset:54016\n"
    "v_cvt_pk_bf16_f32 v156, v204, v205\n"
    "v_mfma_f32_16x16x32_bf16 v[196:199], a[84:87], a[20:23], v[196:199]\n"
    "v_exp_f32 v207, v207\n"
    "v_mfma_f32_16x16x32_bf16 v[196:199], a[80:83], a[16:19], v[196:199]\n"
    "ds_read_b128 a[68:71], v228 offset:16640\n"
    "v_cvt_pk_bf16_f32 v157, v206, v207\n"
    "v_mfma_f32_16x16x32_bf16 v[96:99], a[96:99], v[160:163], v[96:99]\n"
    "s_waitcnt lgkmcnt(4)\n"
    "v_exp_f32 v192, v192\n"
    "v_mfma_f32_16x16x32_bf16 v[96:99], a[100:103], v[176:179], v[96:99]\n"
    "ds_read_b64_tr_b16 a[108:109], v230 offset:58112\n"
    "v_add_f32 v224, v224, v192\n"
    "v_mfma_f32_16x16x32_bf16 v[100:103], a[100:103], v[180:183], v[100:103]\n"
    "v_exp_f32 v193, v193\n"
    "v_mfma_f32_16x16x32_bf16 v[100:103], a[96:99], v[164:167], v[100:103]\n"
    "ds_read_b64_tr_b16 a[110:111], v230 offset:62208\n"
    "v_add_f32 v224, v224, v193\n"
    "v_mfma_f32_16x16x32_bf16 v[104:107], a[96:99], v[168:171], v[104:107]\n"
    "v_exp_f32 v194, v194\n"
    "v_mfma_f32_16x16x32_bf16 v[104:107], a[100:103], v[184:187], v[104:107]\n"
    "ds_read_b128 a[72:75], v228 offset:16896\n"
    "v_add_f32 v224, v224, v194\n"
    "v_mfma_f32_16x16x32_bf16 v[108:111], a[100:103], v[188:191], v[108:111]\n"
    "v_exp_f32 v195, v195\n"
    "v_mfma_f32_16x16x32_bf16 v[108:111], a[96:99], v[172:175], v[108:111]\n"
    "v_cvt_pk_bf16_f32 v146, v192, v193\n"
    "v_add_f32 v224, v224, v195\n"
    "v_mfma_f32_16x16x32_bf16 v[200:203], a[80:83], a[32:35], v[216:219]\n"
    "v_exp_f32 v196, v196\n"
    "v_mfma_f32_16x16x32_bf16 v[200:203], a[84:87], a[36:39], v[200:203]\n"
    "ds_read_b64_tr_b16 a[96:97], v229 offset:0\n"
    "v_cvt_pk_bf16_f32 v147, v194, v195\n"
    "v_mfma_f32_16x16x32_bf16 v[200:203], a[88:91], a[40:43], v[200:203]\n"
    "v_exp_f32 v197, v197\n"
    "v_mfma_f32_16x16x32_bf16 v[200:203], a[92:95], a[44:47], v[200:203]\n"
    "buffer_load_dwordx4 v232, s[16:19], s58 offen offset:1024 nt lds\n"
    "v_mfma_f32_16x16x32_bf16 v[204:207], a[92:95], a[60:63], v[220:223]\n"
    "v_exp_f32 v198, v198\n"
    "v_mfma_f32_16x16x32_bf16 v[204:207], a[88:91], a[56:59], v[204:207]\n"
    "ds_read_b64_tr_b16 a[98:99], v229 offset:4096\n"
    "v_cvt_pk_bf16_f32 v150, v196, v197\n"
    "v_mfma_f32_16x16x32_bf16 v[204:207], a[84:87], a[52:55], v[204:207]\n"
    "v_exp_f32 v199, v199\n"
    "v_mfma_f32_16x16x32_bf16 v[204:207], a[80:83], a[48:51], v[204:207]\n"
    "s_waitcnt lgkmcnt(6)\n"
    "ds_read_b128 a[76:79], v228 offset:17152\n"
    "v_cvt_pk_bf16_f32 v151, v198, v199\n"
    "v_mfma_f32_16x16x32_bf16 v[112:115], a[104:107], v[160:163], v[112:115]\n"
    "s_waitcnt lgkmcnt(4)\n"
    "v_exp_f32 v200, v200\n"
    "v_mfma_f32_16x16x32_bf16 v[112:115], a[108:111], v[176:179], v[112:115]\n"
    "ds_read_b64_tr_b16 a[100:101], v229 offset:8192\n"
    "v_add_f32 v226, v226, v200\n"
    "v_mfma_f32_16x16x32_bf16 v[116:119], a[108:111], v[180:183], v[116:119]\n"
    "v_exp_f32 v201, v201\n"
    "v_mfma_f32_16x16x32_bf16 v[116:119], a[104:107], v[164:167], v[116:119]\n"
    "ds_read_b64_tr_b16 a[102:103], v229 offset:12288\n"
    "v_add_f32 v226, v226, v201\n"
    "v_mfma_f32_16x16x32_bf16 v[120:123], a[104:107], v[168:171], v[120:123]\n"
    "v_exp_f32 v202, v202\n"
    "v_mfma_f32_16x16x32_bf16 v[120:123], a[108:111], v[184:187], v[120:123]\n"
    "ds_read_b128 a[80:83], v228 offset:20480\n"
    "v_add_f32 v226, v226, v202\n"
    "v_mfma_f32_16x16x32_bf16 v[124:127], a[108:111], v[188:191], v[124:127]\n"
    "v_exp_f32 v203, v203\n"
    "v_mfma_f32_16x16x32_bf16 v[124:127], a[104:107], v[172:175], v[124:127]\n"
    "v_cvt_pk_bf16_f32 v154, v200, v201\n"
    "v_add_f32 v226, v226, v203\n"
    "v_mfma_f32_16x16x32_bf16 v[192:195], a[64:67], a[0:3], v[208:211]\n"
    "s_mov_b32 m0, s56\n"
    "v_exp_f32 v204, v204\n"
    "v_mfma_f32_16x16x32_bf16 v[192:195], a[68:71], a[4:7], v[192:195]\n"
    "s_waitcnt lgkmcnt(6)\n"
    "ds_read_b64_tr_b16 a[104:105], v230 offset:0\n"
    "v_cvt_pk_bf16_f32 v155, v202, v203\n"
    "v_mfma_f32_16x16x32_bf16 v[192:195], a[72:75], a[8:11], v[192:195]\n"
    "s_waitcnt lgkmcnt(4)\n"
    "v_exp_f32 v205, v205\n"
    "v_mfma_f32_16x16x32_bf16 v[192:195], a[76:79], a[12:15], v[192:195]\n"
    "s_nop 0\n"
    "buffer_load_dwordx4 v231, s[16:19], s58 offen offset:2048 nt lds\n"
    "v_mfma_f32_16x16x32_bf16 v[196:199], a[76:79], a[28:31], v[212:215]\n"
    "v_exp_f32 v206, v206\n"
    "v_mfma_f32_16x16x32_bf16 v[196:199], a[72:75], a[24:27], v[196:199]\n"
    "ds_read_b64_tr_b16 a[106:107], v230 offset:4096\n"
    "v_cvt_pk_bf16_f32 v158, v204, v205\n"
    "v_mfma_f32_16x16x32_bf16 v[196:199], a[68:71], a[20:23], v[196:199]\n"
    "s_add_u32 m0, s51, 131072\n"
    "v_exp_f32 v207, v207\n"
    "v_mfma_f32_16x16x32_bf16 v[196:199], a[64:67], a[16:19], v[196:199]\n"
    "ds_read_b128 a[84:87], v228 offset:20736\n"
    "v_cvt_pk_bf16_f32 v159, v206, v207\n"
    "v_mfma_f32_16x16x32_bf16 v[0:3], a[96:99], v[128:131], v[0:3]\n"
    "s_waitcnt lgkmcnt(4)\n"
    "v_exp_f32 v192, v192\n"
    "v_mfma_f32_16x16x32_bf16 v[0:3], a[100:103], v[144:147], v[0:3]\n"
    "s_xor_b32 s53, s88, 131072\n"
    "ds_read_b64_tr_b16 a[108:109], v230 offset:8192\n"
    "v_add_f32 v224, v224, v192\n"
    "v_mfma_f32_16x16x32_bf16 v[4:7], a[100:103], v[148:151], v[4:7]\n"
    "s_memrealtime s[8:9]\n"
    "buffer_load_dwordx4 v234, s[24:27], s53 offen offset:0  lds\n"
    "v_exp_f32 v193, v193\n"
    "v_mfma_f32_16x16x32_bf16 v[4:7], a[96:99], v[132:135], v[4:7]\n"
    "ds_read_b64_tr_b16 a[110:111], v230 offset:12288\n"
    "v_add_f32 v224, v224, v193\n"
    "v_mfma_f32_16x16x32_bf16 v[8:11], a[96:99], v[136:139], v[8:11]\n"
    "v_exp_f32 v194, v194\n"
    "v_mfma_f32_16x16x32_bf16 v[8:11], a[100:103], v[152:155], v[8:11]\n"
    "ds_read_b128 a[88:91], v228 offset:20992\n"
    "v_add_f32 v224, v224, v194\n"
    "v_mfma_f32_16x16x32_bf16 v[12:15], a[100:103], v[156:159], v[12:15]\n"
    "v_exp_f32 v195, v195\n"
    "v_mfma_f32_16x16x32_bf16 v[12:15], a[96:99], v[140:143], v[12:15]\n"
    "v_cvt_pk_bf16_f32 v160, v192, v193\n"
    "v_add_f32 v224, v224, v195\n"
    "v_mfma_f32_16x16x32_bf16 v[200:203], a[64:67], a[32:35], v[216:219]\n"
    "v_exp_f32 v196, v196\n"
    "v_mfma_f32_16x16x32_bf16 v[200:203], a[68:71], a[36:39], v[200:203]\n"
    "ds_read_b64_tr_b16 a[96:97], v229 offset:256\n"
    "v_cvt_pk_bf16_f32 v161, v194, v195\n"
    "v_mfma_f32_16x16x32_bf16 v[200:203], a[72:75], a[40:43], v[200:203]\n"
    "s_mov_b32 m0, s56\n"
    "v_exp_f32 v197, v197\n"
    "v_mfma_f32_16x16x32_bf16 v[200:203], a[76:79], a[44:47], v[200:203]\n"
    "buffer_load_dwordx4 v232, s[16:19], s58 offen offset:3072 nt lds\n"
    "v_mfma_f32_16x16x32_bf16 v[204:207], a[76:79], a[60:63], v[220:223]\n"
    "v_exp_f32 v198, v198\n"
    "v_mfma_f32_16x16x32_bf16 v[204:207], a[72:75], a[56:59], v[204:207]\n"
    "s_add_u32 s58, s58, 16384\n"
    "ds_read_b64_tr_b16 a[98:99], v229 offset:4352\n"
    "v_cvt_pk_bf16_f32 v164, v196, v197\n"
    "v_mfma_f32_16x16x32_bf16 v[204:207], a[68:71], a[52:55], v[204:207]\n"
    "s_and_b32 s58, s58, 0x3ffff\n"
    "v_exp_f32 v199, v199\n"
    "v_mfma_f32_16x16x32_bf16 v[204:207], a[64:67], a[48:51], v[204:207]\n"
    "s_waitcnt lgkmcnt(6)\n"
    "ds_read_b128 a[92:95], v228 offset:21248\n"
    "v_cvt_pk_bf16_f32 v165, v198, v199\n"
    "v_mfma_f32_16x16x32_bf16 v[16:19], a[104:107], v[128:131], v[16:19]\n"
    "s_waitcnt lgkmcnt(4)\n"
    "v_exp_f32 v200, v200\n"
    "v_mfma_f32_16x16x32_bf16 v[16:19], a[108:111], v[144:147], v[16:19]\n"
    "s_add_u32 m0, s51, 131072\n"
    "ds_read_b64_tr_b16 a[100:101], v229 offset:8448\n"
    "v_add_f32 v226, v226, v200\n"
    "v_mfma_f32_16x16x32_bf16 v[20:23], a[108:111], v[148:151], v[20:23]\n"
    "buffer_load_dwordx4 v235, s[24:27], s53 offen offset:1024  lds\n"
    "v_exp_f32 v201, v201\n"
    "v_mfma_f32_16x16x32_bf16 v[20:23], a[104:107], v[132:135], v[20:23]\n"
    "s_add_u32 s56, s56, 16384\n"
    "ds_read_b64_tr_b16 a[102:103], v229 offset:12544\n"
    "v_add_f32 v226, v226, v201\n"
    "v_mfma_f32_16x16x32_bf16 v[24:27], a[104:107], v[136:139], v[24:27]\n"
    "s_and_b32 s56, s56, 0xffff\n"
    "v_exp_f32 v202, v202\n"
    "v_mfma_f32_16x16x32_bf16 v[24:27], a[108:111], v[152:155], v[24:27]\n"
    "ds_read_b128 a[64:67], v228 offset:24576\n"
    "v_add_f32 v226, v226, v202\n"
    "v_mfma_f32_16x16x32_bf16 v[28:31], a[108:111], v[156:159], v[28:31]\n"
    "v_exp_f32 v203, v203\n"
    "v_mfma_f32_16x16x32_bf16 v[28:31], a[104:107], v[140:143], v[28:31]\n"
    "v_cvt_pk_bf16_f32 v168, v200, v201\n"
    "v_add_f32 v226, v226, v203\n"
    "v_mfma_f32_16x16x32_bf16 v[192:195], a[80:83], a[0:3], v[208:211]\n"
    "s_mov_b32 m0, s57\n"
    "v_exp_f32 v204, v204\n"
    "v_mfma_f32_16x16x32_bf16 v[192:195], a[84:87], a[4:7], v[192:195]\n"
    "s_waitcnt lgkmcnt(6)\n"
    "ds_read_b64_tr_b16 a[104:105], v230 offset:256\n"
    "v_cvt_pk_bf16_f32 v169, v202, v203\n"
    "v_mfma_f32_16x16x32_bf16 v[192:195], a[88:91], a[8:11], v[192:195]\n"
    "s_waitcnt lgkmcnt(4)\n"
    "v_exp_f32 v205, v205\n"
    "v_mfma_f32_16x16x32_bf16 v[192:195], a[92:95], a[12:15], v[192:195]\n"
    "buffer_load_dwordx4 v231, s[20:23], s59 offen offset:0 nt lds\n"
    "v_mfma_f32_16x16x32_bf16 v[196:199], a[92:95], a[28:31], v[212:215]\n"
    "v_exp_f32 v206, v206\n"
    "v_mfma_f32_16x16x32_bf16 v[196:199], a[88:91], a[24:27], v[196:199]\n"
    "ds_read_b64_tr_b16 a[106:107], v230 offset:4352\n"
    "v_cvt_pk_bf16_f32 v172, v204, v205\n"
    "v_mfma_f32_16x16x32_bf16 v[196:199], a[84:87], a[20:23], v[196:199]\n"
    "v_exp_f32 v207, v207\n"
    "v_mfma_f32_16x16x32_bf16 v[196:199], a[80:83], a[16:19], v[196:199]\n"
    "ds_read_b128 a[68:71], v228 offset:24832\n"
    "v_cvt_pk_bf16_f32 v173, v206, v207\n"
    "v_mfma_f32_16x16x32_bf16 a[116:119], a[112:115], v[132:135], a[116:119]\n"
    "s_waitcnt vmcnt(12)\n"
    "ds_read_b64_tr_b16 a[226:227], v242 offset:16384\n"
    "ds_read_b64_tr_b16 a[230:231], v243 offset:16384\n"
    "v_mfma_f32_16x16x32_bf16 a[116:119], a[112:115], v[148:151], a[116:119]\n"
    "ds_read_b64_tr_b16 a[234:235], v242 offset:16640\n"
    "ds_read_b64_tr_b16 a[238:239], v243 offset:16640\n"
    "v_mfma_f32_16x16x32_bf16 v[32:35], a[96:99], v[128:131], v[32:35]\n"
    "s_waitcnt lgkmcnt(8)\n"
    "v_exp_f32 v192, v192\n"
    "v_mfma_f32_16x16x32_bf16 v[32:35], a[100:103], v[144:147], v[32:35]\n"
    "s_add_u32 m0, s51, 131072\n"
    "ds_read_b64_tr_b16 a[108:109], v230 offset:8448\n"
    "v_add_f32 v224, v224, v192\n"
    "v_mfma_f32_16x16x32_bf16 v[36:39], a[100:103], v[148:151], v[36:39]\n"
    "buffer_load_dwordx4 v236, s[24:27], s53 offen offset:2048  lds\n"
    "v_exp_f32 v193, v193\n"
    "v_mfma_f32_16x16x32_bf16 v[36:39], a[96:99], v[132:135], v[36:39]\n"
    "ds_read_b64_tr_b16 a[110:111], v230 offset:12544\n"
    "v_add_f32 v224, v224, v193\n"
    "v_mfma_f32_16x16x32_bf16 v[40:43], a[96:99], v[136:139], v[40:43]\n"
    "v_exp_f32 v194, v194\n"
    "v_mfma_f32_16x16x32_bf16 v[40:43], a[100:103], v[152:155], v[40:43]\n"
    "ds_read_b128 a[72:75], v228 offset:25088\n"
    "v_add_f32 v224, v224, v194\n"
    "v_mfma_f32_16x16x32_bf16 v[44:47], a[100:103], v[156:159], v[44:47]\n"
    "v_exp_f32 v195, v195\n"
    "v_mfma_f32_16x16x32_bf16 v[44:47], a[96:99], v[140:143], v[44:47]\n"
    "v_cvt_pk_bf16_f32 v162, v192, v193\n"
    "v_add_f32 v224, v224, v195\n"
    "v_mfma_f32_16x16x32_bf16 v[200:203], a[80:83], a[32:35], v[216:219]\n"
    "v_exp_f32 v196, v196\n"
    "v_mfma_f32_16x16x32_bf16 v[200:203], a[84:87], a[36:39], v[200:203]\n"
    "ds_read_b64_tr_b16 a[96:97], v229 offset:512\n"
    "v_cvt_pk_bf16_f32 v163, v194, v195\n"
    "v_mfma_f32_16x16x32_bf16 v[200:203], a[88:91], a[40:43], v[200:203]\n"
    "s_mov_b32 m0, s57\n"
    "v_exp_f32 v197, v197\n"
    "v_mfma_f32_16x16x32_bf16 v[200:203], a[92:95], a[44:47], v[200:203]\n"
    "buffer_load_dwordx4 v232, s[20:23], s59 offen offset:1024 nt lds\n"
    "v_mfma_f32_16x16x32_bf16 v[204:207], a[92:95], a[60:63], v[220:223]\n"
    "v_exp_f32 v198, v198\n"
    "v_mfma_f32_16x16x32_bf16 v[204:207], a[88:91], a[56:59], v[204:207]\n"
    "ds_read_b64_tr_b16 a[98:99], v229 offset:4608\n"
    "v_cvt_pk_bf16_f32 v166, v196, v197\n"
    "v_mfma_f32_16x16x32_bf16 v[204:207], a[84:87], a[52:55], v[204:207]\n"
    "v_exp_f32 v199, v199\n"
    "v_mfma_f32_16x16x32_bf16 v[204:207], a[80:83], a[48:51], v[204:207]\n"
    "s_waitcnt lgkmcnt(10)\n"
    "ds_read_b128 a[76:79], v228 offset:25344\n"
    "v_cvt_pk_bf16_f32 v167, v198, v199\n"
    "v_mfma_f32_16x16x32_bf16 v[48:51], a[104:107], v[128:131], v[48:51]\n"
    "s_waitcnt lgkmcnt(4)\n"
    "v_exp_f32 v200, v200\n"
    "v_mfma_f32_16x16x32_bf16 v[48:51], a[108:111], v[144:147], v[48:51]\n"
    "s_add_u32 m0, s51, 131072\n"
    "ds_read_b64_tr_b16 a[100:101], v229 offset:8704\n"
    "v_add_f32 v226, v226, v200\n"
    "v_mfma_f32_16x16x32_bf16 v[52:55], a[108:111], v[148:151], v[52:55]\n"
    "s_sub_u32 s11, s60, s8\n"
    "buffer_load_dwordx4 v237, s[24:27], s53 offen offset:3072  lds\n"
    "v_exp_f32 v201, v201\n"
    "v_mfma_f32_16x16x32_bf16 v[52:55], a[104:107], v[132:135], v[52:55]\n"
    "s_add_u32 s60, s60, 142\n"
    "ds_read_b64_tr_b16 a[102:103], v229 offset:12800\n"
    "v_add_f32 v226, v226, v201\n"
    "v_mfma_f32_16x16x32_bf16 v[56:59], a[104:107], v[136:139], v[56:59]\n"
    "s_cmp_gt_i32 s11, 2\n"
    "v_exp_f32 v202, v202\n"
    "v_mfma_f32_16x16x32_bf16 v[56:59], a[108:111], v[152:155], v[56:59]\n"
    "s_cbranch_scc1 Lpa8x%=\n"
    "Lpb8x%=:\n"
    "ds_read_b128 a[80:83], v228 offset:28672\n"
    "v_add_f32 v226, v226, v202\n"
    "v_mfma_f32_16x16x32_bf16 v[60:63], a[108:111], v[156:159], v[60:63]\n"
    "v_exp_f32 v203, v203\n"
    "v_mfma_f32_16x16x32_bf16 v[60:63], a[104:107], v[140:143], v[60:63]\n"
    "v_cvt_pk_bf16_f32 v170, v200, v201\n"
    "v_add_f32 v226, v226, v203\n"
    "v_mfma_f32_16x16x32_bf16 v[192:195], a[64:67], a[0:3], v[208:211]\n"
    "s_mov_b32 m0, s57\n"
    "v_exp_f32 v204, v204\n"
    "v_mfma_f32_16x16x32_bf16 v[192:195], a[68:71], a[4:7], v[192:195]\n"
    "s_waitcnt lgkmcnt(6)\n"
    "ds_read_b64_tr_b16 a[104:105], v230 offset:512\n"
    "v_cvt_pk_bf16_f32 v171, v202, v203\n"
    "v_mfma_f32_16x16x32_bf16 v[192:195], a[72:75], a[8:11], v[192:195]\n"
    "s_waitcnt lgkmcnt(4)\n"
    "v_exp_f32 v205, v205\n"
    "v_mfma_f32_16x16x32_bf16 v[192:195], a[76:79], a[12:15], v[192:195]\n"
    "buffer_load_dwordx4 v231, s[20:23], s59 offen offset:2048 nt lds\n"
    "v_mfma_f32_16x16x32_bf16 v[196:199], a[76:79], a[28:31], v[212:215]\n"
    "v_exp_f32 v206, v206\n"
    "v_mfma_f32_16x16x32_bf16 v[196:199], a[72:75], a[24:27], v[196:199]\n"
    "ds_read_b64_tr_b16 a[106:107], v230 offset:4608\n"
    "v_cvt_pk_bf16_f32 v174, v204, v205\n"
    "v_mfma_f32_16x16x32_bf16 v[196:199], a[68:71], a[20:23], v[196:199]\n"
    "v_exp_f32 v207, v207\n"
    "v_mfma_f32_16x16x32_bf16 v[196:199], a[64:67], a[16:19], v[196:199]\n"
    "ds_read_b128 a[84:87], v228 offset:28928\n"
    "v_cvt_pk_bf16_f32 v175, v206, v207\n"
    "v_mfma_f32_16x16x32_bf16 v[64:67], a[96:99], v[128:131], v[64:67]\n"
    "s_waitcnt lgkmcnt(4)\n"
    "v_exp_f32 v192, v192\n"
    "v_mfma_f32_16x16x32_bf16 v[64:67], a[100:103], v[144:147], v[64:67]\n"
    "ds_read_b64_tr_b16 a[108:109], v230 offset:8704\n"
    "v_add_f32 v224, v224, v192\n"
    "v_mfma_f32_16x16x32_bf16 v[68:71], a[100:103], v[148:151], v[68:71]\n"
    "v_exp_f32 v193, v193\n"
    "v_mfma_f32_16x16x32_bf16 v[68:71], a[96:99], v[132:135], v[68:71]\n"
    "ds_read_b64_tr_b16 a[110:111], v230 offset:12800\n"
    "v_add_f32 v224, v224, v193\n"
    "v_mfma_f32_16x16x32_bf16 v[72:75], a[96:99], v[136:139], v[72:75]\n"
    "v_exp_f32 v194, v194\n"
    "v_mfma_f32_16x16x32_bf16 v[72:75], a[100:103], v[152:155], v[72:75]\n"
    "ds_read_b128 a[88:91], v228 offset:29184\n"
    "v_add_f32 v224, v224, v194\n"
    "v_mfma_f32_16x16x32_bf16 v[76:79], a[100:103], v[156:159], v[76:79]\n"
    "v_exp_f32 v195, v195\n"
    "v_mfma_f32_16x16x32_bf16 v[76:79], a[96:99], v[140:143], v[76:79]\n"
    "v_cvt_pk_bf16_f32 v176, v192, v193\n"
    "v_add_f32 v224, v224, v195\n"
    "v_mfma_f32_16x16x32_bf16 v[200:203], a[64:67], a[32:35], v[216:219]\n"
    "v_exp_f32 v196, v196\n"
    "v_mfma_f32_16x16x32_bf16 v[200:203], a[68:71], a[36:39], v[200:203]\n"
    "ds_read_b64_tr_b16 a[96:97], v229 offset:768\n"
    "v_cvt_pk_bf16_f32 v177, v194, v195\n"
    "v_mfma_f32_16x16x32_bf16 v[200:203], a[72:75], a[40:43], v[200:203]\n"
    "v_exp_f32 v197, v197\n"
    "v_mfma_f32_16x16x32_bf16 v[200:203], a[76:79], a[44:47], v[200:203]\n"
    "buffer_load_dwordx4 v232, s[20:23], s59 offen offset:3072 nt lds\n"
    "v_mfma_f32_16x16x32_bf16 v[204:207], a[76:79], a[60:63], v[220:223]\n"
    "v_exp_f32 v198, v198\n"
    "v_mfma_f32_16x16x32_bf16 v[204:207], a[72:75], a[56:59], v[204:207]\n"
    "s_add_u32 s59, s59, 16384\n"
    "ds_read_b64_tr_b16 a[98:99], v229 offset:4864\n"
    "v_cvt_pk_bf16_f32 v180, v196, v197\n"
    "v_mfma_f32_16x16x32_bf16 v[204:207], a[68:71], a[52:55], v[204:207]\n"
    "s_and_b32 s59, s59, 0x3ffff\n"
    "v_exp_f32 v199, v199\n"
    "v_mfma_f32_16x16x32_bf16 v[204:207], a[64:67], a[48:51], v[204:207]\n"
    "ds_read_b128 a[92:95], v228 offset:29440\n"
    "v_cvt_pk_bf16_f32 v181, v198, v199\n"
    "v_mfma_f32_16x16x32_bf16 a[120:123], a[112:115], v[140:143], a[120:123]\n"
    "ds_read_b64_tr_b16 a[242:243], v242 offset:16896\n"
    "ds_read_b64_tr_b16 a[246:247], v243 offset:16896\n"
    "v_mfma_f32_16x16x32_bf16 a[120:123], a[112:115], v[156:159], a[120:123]\n"
    "s_waitcnt lgkmcnt(9)\n"
    "ds_read_b64_tr_b16 a[250:251], v242 offset:17152\n"
    "ds_read_b64_tr_b16 a[254:255], v243 offset:17152\n"
    "v_mfma_f32_16x16x32_bf16 v[80:83], a[104:107], v[128:131], v[80:83]\n"
    "s_waitcnt lgkmcnt(8)\n"
    "v_exp_f32 v200, v200\n"
    "v_mfma_f32_16x16x32_bf16 v[80:83], a[108:111], v[144:147], v[80:83]\n"
    "s_add_u32 s57, s57, 16384\n"
    "ds_read_b64_tr_b16 a[100:101], v229 offset:8960\n"
    "v_add_f32 v226, v226, v200\n"
    "v_mfma_f32_16x16x32_bf16 v[84:87], a[108:111], v[148:151], v[84:87]\n"
    "s_bitset0_b32 s57, 17\n"
    "v_exp_f32 v201, v201\n"
    "v_mfma_f32_16x16x32_bf16 v[84:87], a[104:107], v[132:135], v[84:87]\n"
    "ds_read_b64_tr_b16 a[102:103], v229 offset:13056\n"
    "v_add_f32 v226, v226, v201\n"
    "v_mfma_f32_16x16x32_bf16 v[88:91], a[104:107], v[136:139], v[88:91]\n"
    "s_waitcnt vmcnt(12) lgkmcnt(6)\n"
    "v_exp_f32 v202, v202\n"
    "v_mfma_f32_16x16x32_bf16 v[88:91], a[108:111], v[152:155], v[88:91]\n"
    "s_barrier\n"
    "ds_read_b128 a[64:67], v228 offset:32768\n"
    "v_add_f32 v226, v226, v202\n"
    "v_mfma_f32_16x16x32_bf16 v[92:95], a[108:111], v[156:159], v[92:95]\n"
    "s_bitset1_b32 s57, 16\n"
    "v_exp_f32 v203, v203\n"
    "v_mfma_f32_16x16x32_bf16 v[92:95], a[104:107], v[140:143], v[92:95]\n"
    "v_cvt_pk_bf16_f32 v184, v200, v201\n"
    "v_add_f32 v226, v226, v203\n"
    "v_mfma_f32_16x16x32_bf16 v[192:195], a[80:83], a[0:3], v[208:211]\n"
    "s_mov_b32 m0, s56\n"
    "v_exp_f32 v204, v204\n"
    "v_mfma_f32_16x16x32_bf16 v[192:195], a[84:87], a[4:7], v[192:195]\n"
    "ds_read_b64_tr_b16 a[104:105], v230 offset:768\n"
    "v_cvt_pk_bf16_f32 v185, v202, v203\n"
    "v_mfma_f32_16x16x32_bf16 v[192:195], a[88:91], a[8:11], v[192:195]\n"
    "v_exp_f32 v205, v205\n"
    "v_mfma_f32_16x16x32_bf16 v[192:195], a[92:95], a[12:15], v[192:195]\n"
    "buffer_load_dwordx4 v231, s[16:19], s58 offen offset:0 nt lds\n"
    "v_mfma_f32_16x16x32_bf16 v[196:199], a[92:95], a[28:31], v[212:215]\n"
    "v_exp_f32 v206, v206\n"
    "v_mfma_f32_16x16x32_bf16 v[196:199], a[88:91], a[24:27], v[196:199]\n"
    "ds_read_b64_tr_b16 a[106:107], v230 offset:4864\n"
    "v_cvt_pk_bf16_f32 v188, v204, v205\n"
    "v_mfma_f32_16x16x32_bf16 v[196:199], a[84:87], a[20:23], v[196:199]\n"
    "v_exp_f32 v207, v207\n"
    "v_mfma_f32_16x16x32_bf16 v[196:199], a[80:83], a[16:19], v[196:199]\n"
    "ds_read_b128 a[68:71], v228 offset:33024\n"
    "v_cvt_pk_bf16_f32 v189, v206, v207\n"
    "v_mfma_f32_16x16x32_bf16 v[96:99], a[96:99], v[128:131], v[96:99]\n"
    "s_waitcnt lgkmcnt(4)\n"
    "v_exp_f32 v192, v192\n"
    "v_mfma_f32_16x16x32_bf16 v[96:99], a[100:103], v[144:147], v[96:99]\n"
    "ds_read_b64_tr_b16 a[108:109], v230 offset:8960\n"
    "v_add_f32 v224, v224, v192\n"
    "v_mfma_f32_16x16x32_bf16 v[100:103], a[100:103], v[148:151], v[100:103]\n"
    "v_exp_f32 v193, v193\n"
    "v_mfma_f32_16x16x32_bf16 v[100:103], a[96:99], v[132:135], v[100:103]\n"
    "ds_read_b64_tr_b16 a[110:111], v230 offset:13056\n"
    "v_add_f32 v224, v224, v193\n"
    "v_mfma_f32_16x16x32_bf16 v[104:107], a[96:99], v[136:139], v[104:107]\n"
    "v_exp_f32 v194, v194\n"
    "v_mfma_f32_16x16x32_bf16 v[104:107], a[100:103], v[152:155], v[104:107]\n"
    "ds_read_b128 a[72:75], v228 offset:33280\n"
    "v_add_f32 v224, v224, v194\n"
    "v_mfma_f32_16x16x32_bf16 v[108:111], a[100:103], v[156:159], v[108:111]\n"
    "v_exp_f32 v195, v195\n"
    "v_mfma_f32_16x16x32_bf16 v[108:111], a[96:99], v[140:143], v[108:111]\n"
    "v_cvt_pk_bf16_f32 v178, v192, v193\n"
    "v_add_f32 v224, v224, v195\n"
    "v_mfma_f32_16x16x32_bf16 v[200:203], a[80:83], a[32:35], v[216:219]\n"
    "v_exp_f32 v196, v196\n"
    "v_mfma_f32_16x16x32_bf16 v[200:203], a[84:87], a[36:39], v[200:203]\n"
    "ds_read_b64_tr_b16 a[96:97], v229 offset:16384\n"
    "v_cvt_pk_bf16_f32 v179, v194, v195\n"
    "v_mfma_f32_16x16x32_bf16 v[200:203], a[88:91], a[40:43], v[200:203]\n"
    "v_exp_f32 v197, v197\n"
    "v_mfma_f32_16x16x32_bf16 v[200:203], a[92:95], a[44:47], v[200:203]\n"
    "buffer_load_dwordx4 v232, s[16:19], s58 offen offset:1024 nt lds\n"
    "v_mfma_f32_16x16x32_bf16 v[204:207], a[92:95], a[60:63], v[220:223]\n"
    "v_exp_f32 v198, v198\n"
    "v_mfma_f32_16x16x32_bf16 v[204:207], a[88:91], a[56:59], v[204:207]\n"
    "ds_read_b64_tr_b16 a[98:99], v229 offset:20480\n"
    "v_cvt_pk_bf16_f32 v182, v196, v197\n"
    "v_mfma_f32_16x16x32_bf16 v[204:207], a[84:87], a[52:55], v[204:207]\n"
    "v_exp_f32 v199, v199\n"
    "v_mfma_f32_16x16x32_bf16 v[204:207], a[80:83], a[48:51], v[204:207]\n"
    "s_waitcnt lgkmcnt(6)\n"
    "ds_read_b128 a[76:79], v228 offset:33536\n"
    "v_cvt_pk_bf16_f32 v183, v198, v199\n"
    "v_mfma_f32_16x16x32_bf16 v[112:115], a[104:107], v[128:131], v[112:115]\n"
    "s_waitcnt lgkmcnt(4)\n"
    "v_exp_f32 v200, v200\n"
    "v_mfma_f32_16x16x32_bf16 v[112:115], a[108:111], v[144:147], v[112:115]\n"
    "ds_read_b64_tr_b16 a[100:101], v229 offset:24576\n"
    "v_add_f32 v226, v226, v200\n"
    "v_mfma_f32_16x16x32_bf16 v[116:119], a[108:111], v[148:151], v[116:119]\n"
    "v_exp_f32 v201, v201\n"
    "v_mfma_f32_16x16x32_bf16 v[116:119], a[104:107], v[132:135], v[116:119]\n"
    "ds_read_b64_tr_b16 a[102:103], v229 offset:28672\n"
    "v_add_f32 v226, v226, v201\n"
    "v_mfma_f32_16x16x32_bf16 v[120:123], a[104:107], v[136:139], v[120:123]\n"
    "v_exp_f32 v202, v202\n"
    "v_mfma_f32_16x16x32_bf16 v[120:123], a[108:111], v[152:155], v[120:123]\n"
    "ds_read_b128 a[80:83], v228 offset:36864\n"
    "v_add_f32 v226, v226, v202\n"
    "v_mfma_f32_16x16x32_bf16 v[124:127], a[108:111], v[156:159], v[124:127]\n"
    "v_exp_f32 v203, v203\n"
    "v_mfma_f32_16x16x32_bf16 v[124:127], a[104:107], v[140:143], v[124:127]\n"
    "v_cvt_pk_bf16_f32 v186, v200, v201\n"
    "v_add_f32 v226, v226, v203\n"
    "v_mfma_f32_16x16x32_bf16 v[192:195], a[64:67], a[0:3], v[208:211]\n"
    "s_mov_b32 m0, s56\n"
    "v_exp_f32 v204, v204\n"
    "v_mfma_f32_16x16x32_bf16 v[192:195], a[68:71], a[4:7], v[192:195]\n"
    "s_waitcnt lgkmcnt(6)\n"
    "ds_read_b64_tr_b16 a[104:105], v230 offset:16384\n"
    "v_cvt_pk_bf16_f32 v187, v202, v203\n"
    "v_mfma_f32_16x16x32_bf16 v[192:195], a[72:75], a[8:11], v[192:195]\n"
    "s_waitcnt lgkmcnt(4)\n"
    "v_exp_f32 v205, v205\n"
    "v_mfma_f32_16x16x32_bf16 v[192:195], a[76:79], a[12:15], v[192:195]\n"
    "s_nop 0\n"
    "buffer_load_dwordx4 v231, s[16:19], s58 offen offset:2048 nt lds\n"
    "v_mfma_f32_16x16x32_bf16 v[196:199], a[76:79], a[28:31], v[212:215]\n"
    "v_exp_f32 v206, v206\n"
    "v_mfma_f32_16x16x32_bf16 v[196:199], a[72:75], a[24:27], v[196:199]\n"
    "ds_read_b64_tr_b16 a[106:107], v230 offset:20480\n"
    "v_cvt_pk_bf16_f32 v190, v204, v205\n"
    "v_mfma_f32_16x16x32_bf16 v[196:199], a[68:71], a[20:23], v[196:199]\n"
    "v_exp_f32 v207, v207\n"
    "v_mfma_f32_16x16x32_bf16 v[196:199], a[64:67], a[16:19], v[196:199]\n"
    "ds_read_b128 a[84:87], v228 offset:37120\n"
    "v_cvt_pk_bf16_f32 v191, v206, v207\n"
    "v_mfma_f32_16x16x32_bf16 v[0:3], a[96:99], v[160:163], v[0:3]\n"
    "s_waitcnt lgkmcnt(4)\n"
    "v_exp_f32 v192, v192\n"
    "v_mfma_f32_16x16x32_bf16 v[0:3], a[100:103], v[176:179], v[0:3]\n"
    "s_memrealtime s[8:9]\n"
    "ds_read_b64_tr_b16 a[108:109], v230 offset:24576\n"
    "v_add_f32 v224, v224, v192\n"
    "v_mfma_f32_16x16x32_bf16 v[4:7], a[100:103], v[180:183], v[4:7]\n"
    "v_exp_f32 v193, v193\n"
    "v_mfma_f32_16x16x32_bf16 v[4:7], a[96:99], v[164:167], v[4:7]\n"
    "ds_read_b64_tr_b16 a[110:111], v230 offset:28672\n"
    "v_add_f32 v224, v224, v193\n"
    "v_mfma_f32_16x16x32_bf16 v[8:11], a[96:99], v[168:171], v[8:11]\n"
    "v_exp_f32 v194, v194\n"
    "v_mfma_f32_16x16x32_bf16 v[8:11], a[100:103], v[184:187], v[8:11]\n"
    "ds_read_b128 a[88:91], v228 offset:37376\n"
    "v_add_f32 v224, v224, v194\n"
    "v_mfma_f32_16x16x32_bf16 v[12:15], a[100:103], v[188:191], v[12:15]\n"
    "v_exp_f32 v195, v195\n"
    "v_mfma_f32_16x16x32_bf16 v[12:15], a[96:99], v[172:175], v[12:15]\n"
    "v_cvt_pk_bf16_f32 v128, v192, v193\n"
    "v_add_f32 v224, v224, v195\n"
    "v_mfma_f32_16x16x32_bf16 v[200:203], a[64:67], a[32:35], v[216:219]\n"
    "v_exp_f32 v196, v196\n"
    "v_mfma_f32_16x16x32_bf16 v[200:203], a[68:71], a[36:39], v[200:203]\n"
    "ds_read_b64_tr_b16 a[96:97], v229 offset:16640\n"
    "v_cvt_pk_bf16_f32 v129, v194, v195\n"
    "v_mfma_f32_16x16x32_bf16 v[200:203], a[72:75], a[40:43], v[200:203]\n"
    "v_exp_f32 v197, v197\n"
    "v_mfma_f32_16x16x32_bf16 v[200:203], a[76:79], a[44:47], v[200:203]\n"
    "buffer_load_dwordx4 v232, s[16:19], s58 offen offset:3072 nt lds\n"
    "v_mfma_f32_16x16x32_bf16 v[204:207], a[76:79], a[60:63], v[220:223]\n"
    "v_exp_f32 v198, v198\n"
    "v_mfma_f32_16x16x32_bf16 v[204:207], a[72:75], a[56:59], v[204:207]\n"
    "s_add_u32 s58, s58, 16384\n"
    "ds_read_b64_tr_b16 a[98:99], v229 offset:20736\n"
    "v_cvt_pk_bf16_f32 v132, v196, v197\n"
    "v_mfma_f32_16x16x32_bf16 v[204:207], a[68:71], a[52:55], v[204:207]\n"
    "s_and_b32 s58, s58, 0x3ffff\n"
    "v_exp_f32 v199, v199\n"
    "v_mfma_f32_16x16x32_bf16 v[204:207], a[64:67], a[48:51], v[204:207]\n"
    "s_waitcnt lgkmcnt(6)\n"
    "ds_read_b128 a[92:95], v228 offset:37632\n"
    "v_cvt_pk_bf16_f32 v133, v198, v199\n"
    "v_mfma_f32_16x16x32_bf16 v[16:19], a[104:107], v[160:163], v[16:19]\n"
    "s_waitcnt lgkmcnt(4)\n"
    "v_exp_f32 v200, v200\n"
    "v_mfma_f32_16x16x32_bf16 v[16:19], a[108:111], v[176:179], v[16:19]\n"
    "ds_read_b64_tr_b16 a[100:101], v229 offset:24832\n"
    "v_add_f32 v226, v226, v200\n"
    "v_mfma_f32_16x16x32_bf16 v[20:23], a[108:111], v[180:183], v[20:23]\n"
    "v_exp_f32 v201, v201\n"
    "v_mfma_f32_16x16x32_bf16 v[20:23], a[104:107], v[164:167], v[20:23]\n"
    "s_add_u32 s56, s56, 16384\n"
    "ds_read_b64_tr_b16 a[102:103], v229 offset:28928\n"
    "v_add_f32 v226, v226, v201\n"
    "v_mfma_f32_16x16x32_bf16 v[24:27], a[104:107], v[168:171], v[24:27]\n"
    "s_and_b32 s56, s56, 0xffff\n"
    "v_exp_f32 v202, v202\n"
    "v_mfma_f32_16x16x32_bf16 v[24:27], a[108:111], v[184:187], v[24:27]\n"
    "ds_read_b128 a[64:67], v228 offset:40960\n"
    "v_add_f32 v226, v226, v202\n"
    "v_mfma_f32_16x16x32_bf16 v[28:31], a[108:111], v[188:191], v[28:31]\n"
    "v_exp_f32 v203, v203\n"
    "v_mfma_f32_16x16x32_bf16 v[28:31], a[104:107], v[172:175], v[28:31]\n"
    "v_cvt_pk_bf16_f32 v136, v200, v201\n"
    "v_add_f32 v226, v226, v203\n"
    "v_mfma_f32_16x16x32_bf16 v[192:195], a[80:83], a[0:3], v[208:211]\n"
    "s_mov_b32 m0, s57\n"
    "v_exp_f32 v204, v204\n"
    "v_mfma_f32_16x16x32_bf16 v[192:195], a[84:87], a[4:7], v[192:195]\n"
    "s_waitcnt lgkmcnt(6)\n"
    "ds_read_b64_tr_b16 a[104:105], v230 offset:16640\n"
    "v_cvt_pk_bf16_f32 v137, v202, v203\n"
    "v_mfma_f32_16x16x32_bf16 v[192:195], a[88:91], a[8:11], v[192:195]\n"
    "s_waitcnt lgkmcnt(4)\n"
    "v_exp_f32 v205, v205\n"
    "v_mfma_f32_16x16x32_bf16 v[192:195], a[92:95], a[12:15], v[192:195]\n"
    "buffer_load_dwordx4 v231, s[20:23], s59 offen offset:0 nt lds\n"
    "v_mfma_f32_16x16x32_bf16 v[196:199], a[92:95], a[28:31], v[212:215]\n"
    "v_exp_f32 v206, v206\n"
    "v_mfma_f32_16x16x32_bf16 v[196:199], a[88:91], a[24:27], v[196:199]\n"
    "ds_read_b64_tr_b16 a[106:107], v230 offset:20736\n"
    "v_cvt_pk_bf16_f32 v140, v204, v205\n"
    "v_mfma_f32_16x16x32_bf16 v[196:199], a[84:87], a[20:23], v[196:199]\n"
    "s_add_u32 m0, s51, 147456\n"
    "v_exp_f32 v207, v207\n"
    "v_mfma_f32_16x16x32_bf16 v[196:199], a[80:83], a[16:19], v[196:199]\n"
    "s_xor_b32 s53, s88, 147456\n"
    "ds_read_b128 a[68:71], v228 offset:41216\n"
    "v_cvt_pk_bf16_f32 v141, v206, v207\n"
    "v_mfma_f32_16x16x32_bf16 a[116:119], a[112:115], v[164:167], a[116:119]\n"
    "buffer_load_dwordx4 v234, s[24:27], s53 offen offset:0  lds\n"
    "v_mfma_f32_16x16x32_bf16 a[116:119], a[112:115], v[180:183], a[116:119]\n"
    "buffer_load_dwordx4 v235, s[24:27], s53 offen offset:1024  lds\n"
    "v_mfma_f32_16x16x32_bf16 v[32:35], a[96:99], v[160:163], v[32:35]\n"
    "s_waitcnt lgkmcnt(4)\n"
    "v_exp_f32 v192, v192\n"
    "v_mfma_f32_16x16x32_bf16 v[32:35], a[100:103], v[176:179], v[32:35]\n"
    "Lpg9%=:\n"
    "s_cbranch_execz Lwb1%=\n"
    "ds_read_b64_tr_b16 a[108:109], v230 offset:24832\n"
    "v_add_f32 v224, v224, v192\n"
    "v_mfma_f32_16x16x32_bf16 v[36:39], a[100:103], v[180:183], v[36:39]\n"
    "v_exp_f32 v193, v193\n"
    "v_mfma_f32_16x16x32_bf16 v[36:39], a[96:99], v[164:167], v[36:39]\n"
    "ds_read_b64_tr_b16 a[110:111], v230 offset:28928\n"
    "v_add_f32 v224, v224, v193\n"
    "v_mfma_f32_16x16x32_bf16 v[40:43], a[96:99], v[168:171], v[40:43]\n"
    "v_exp_f32 v194, v194\n"
    "v_mfma_f32_16x16x32_bf16 v[40:43], a[100:103], v[184:187], v[40:43]\n"
    "ds_read_b128 a[72:75], v228 offset:41472\n"
    "v_add_f32 v224, v224, v194\n"
    "v_mfma_f32_16x16x32_bf16 v[44:47], a[100:103], v[188:191], v[44:47]\n"
    "v_exp_f32 v195, v195\n"
    "v_mfma_f32_16x16x32_bf16 v[44:47], a[96:99], v[172:175], v[44:47]\n"
    "v_cvt_pk_bf16_f32 v130, v192, v193\n"
    "v_add_f32 v224, v224, v195\n"
    "v_mfma_f32_16x16x32_bf16 v[200:203], a[80:83], a[32:35], v[216:219]\n"
    "v_exp_f32 v196, v196\n"
    "v_mfma_f32_16x16x32_bf16 v[200:203], a[84:87], a[36:39], v[200:203]\n"
    "ds_read_b64_tr_b16 a[96:97], v229 offset:16896\n"
    "v_cvt_pk_bf16_f32 v131, v194, v195\n"
    "v_mfma_f32_16x16x32_bf16 v[200:203], a[88:91], a[40:43], v[200:203]\n"
    "s_mov_b32 m0, s57\n"
    "v_exp_f32 v197, v197\n"
    "v_mfma_f32_16x16x32_bf16 v[200:203], a[92:95], a[44:47], v[200:203]\n"
    "buffer_load_dwordx4 v232, s[20:23], s59 offen offset:1024 nt lds\n"
    "v_mfma_f32_16x16x32_bf16 v[204:207], a[92:95], a[60:63], v[220:223]\n"
    "v_exp_f32 v198, v198\n"
    "v_mfma_f32_16x16x32_bf16 v[204:207], a[88:91], a[56:59], v[204:207]\n"
    "ds_read_b64_tr_b16 a[98:99], v229 offset:20992\n"
    "v_cvt_pk_bf16_f32 v134, v196, v197\n"
    "v_mfma_f32_16x16x32_bf16 v[204:207], a[84:87], a[52:55], v[204:207]\n"
    "v_exp_f32 v199, v199\n"
    "v_mfma_f32_16x16x32_bf16 v[204:207], a[80:83], a[48:51], v[204:207]\n"
    "s_waitcnt lgkmcnt(6)\n"
    "ds_read_b128 a[76:79], v228 offset:41728\n"
    "v_cvt_pk_bf16_f32 v135, v198, v199\n"
    "v_mfma_f32_16x16x32_bf16 v[48:51], a[104:107], v[160:163], v[48:51]\n"
    "s_waitcnt lgkmcnt(4)\n"
    "v_exp_f32 v200, v200\n"
    "v_mfma_f32_16x16x32_bf16 v[48:51], a[108:111], v[176:179], v[48:51]\n"
    "s_sub_u32 s11, s60, s8\n"
    "ds_read_b64_tr_b16 a[100:101], v229 offset:25088\n"
    "v_add_f32 v226, v226, v200\n"
    "v_mfma_f32_16x16x32_bf16 v[52:55], a[108:111], v[180:183], v[52:55]\n"
    "s_add_u32 s60, s60, 142\n"
    "v_exp_f32 v201, v201\n"
    "v_mfma_f32_16x16x32_bf16 v[52:55], a[104:107], v[164:167], v[52:55]\n"
    "s_cmp_gt_i32 s11, 2\n"
    "ds_read_b64_tr_b16 a[102:103], v229 offset:29184\n"
    "v_add_f32 v226, v226, v201\n"
    "v_mfma_f32_16x16x32_bf16 v[56:59], a[104:107], v[168:171], v[56:59]\n"
    "s_cbranch_scc1 Lpa9x%=\n"
    "Lpb9x%=:\n"
    "v_exp_f32 v202, v202\n"
    "v_mfma_f32_16x16x32_bf16 v[56:59], a[108:111], v[184:187], v[56:59]\n"
    "ds_read_b128 a[80:83], v228 offset:45056\n"
    "v_add_f32 v226, v226, v202\n"
    "v_mfma_f32_16x16x32_bf16 v[60:63], a[108:111], v[188:191], v[60:63]\n"
    "v_exp_f32 v203, v203\n"
    "v_mfma_f32_16x16x32_bf16 v[60:63], a[104:107], v[172:175], v[60:63]\n"
    "v_cvt_pk_bf16_f32 v138, v200, v201\n"
    "v_add_f32 v226, v226, v203\n"
    "v_mfma_f32_16x16x32_bf16 v[192:195], a[64:67], a[0:3], v[208:211]\n"
    "v_exp_f32 v204, v204\n"
    "v_mfma_f32_16x16x32_bf16 v[192:195], a[68:71], a[4:7], v[192:195]\n"
    "s_waitcnt lgkmcnt(6)\n"
    "ds_read_b64_tr_b16 a[104:105], v230 offset:16896\n"
    "v_cvt_pk_bf16_f32 v139, v202, v203\n"
    "v_mfma_f32_16x16x32_bf16 v[192:195], a[72:75], a[8:11], v[192:195]\n"
    "s_waitcnt lgkmcnt(4)\n"
    "v_exp_f32 v205, v205\n"
    "v_mfma_f32_16x16x32_bf16 v[192:195], a[76:79], a[12:15], v[192:195]\n"
    "buffer_load_dwordx4 v231, s[20:23], s59 offen offset:2048 nt lds\n"
    "v_mfma_f32_16x16x32_bf16 v[196:199], a[76:79], a[28:31], v[212:215]\n"
    "v_exp_f32 v206, v206\n"
    "v_mfma_f32_16x16x32_bf16 v[196:199], a[72:75], a[24:27], v[196:199]\n"
    "ds_read_b64_tr_b16 a[106:107], v230 offset:20992\n"
    "v_cvt_pk_bf16_f32 v142, v204, v205\n"
    "v_mfma_f32_16x16x32_bf16 v[196:199], a[68:71], a[20:23], v[196:199]\n"
    "v_exp_f32 v207, v207\n"
    "v_mfma_f32_16x16x32_bf16 v[196:199], a[64:67], a[16:19], v[196:199]\n"
    "ds_read_b128 a[84:87], v228 offset:45312\n"
    "v_cvt_pk_bf16_f32 v143, v206, v207\n"
    "v_mfma_f32_16x16x32_bf16 v[64:67], a[96:99], v[160:163], v[64:67]\n"
    "s_waitcnt lgkmcnt(4)\n"
    "v_exp_f32 v192, v192\n"
    "v_mfma_f32_16x16x32_bf16 v[64:67], a[100:103], v[176:179], v[64:67]\n"
    "ds_read_b64_tr_b16 a[108:109], v230 offset:25088\n"
    "v_add_f32 v224, v224, v192\n"
    "v_mfma_f32_16x16x32_bf16 v[68:71], a[100:103], v[180:183], v[68:71]\n"
    "v_exp_f32 v193, v193\n"
    "v_mfma_f32_16x16x32_bf16 v[68:71], a[96:99], v[164:167], v[68:71]\n"
    "ds_read_b64_tr_b16 a[110:111], v230 offset:29184\n"
    "v_add_f32 v224, v224, v193\n"
    "v_mfma_f32_16x16x32_bf16 v[72:75], a[96:99], v[168:171], v[72:75]\n"
    "v_exp_f32 v194, v194\n"
    "v_mfma_f32_16x16x32_bf16 v[72:75], a[100:103], v[184:187], v[72:75]\n"
    "ds_read_b128 a[88:91], v228 offset:45568\n"
    "v_add_f32 v224, v224, v194\n"
    "v_mfma_f32_16x16x32_bf16 v[76:79], a[100:103], v[188:191], v[76:79]\n"
    "v_exp_f32 v195, v195\n"
    "v_mfma_f32_16x16x32_bf16 v[76:79], a[96:99], v[172:175], v[76:79]\n"
    "v_cvt_pk_bf16_f32 v144, v192, v193\n"
    "v_add_f32 v224, v224, v195\n"
    "v_mfma_f32_16x16x32_bf16 v[200:203], a[64:67], a[32:35], v[216:219]\n"
    "v_exp_f32 v196, v196\n"
    "v_mfma_f32_16x16x32_bf16 v[200:203], a[68:71], a[36:39], v[200:203]\n"
    "ds_read_b64_tr_b16 a[96:97], v229 offset:17152\n"
    "v_cvt_pk_bf16_f32 v145, v194, v195\n"
    "v_mfma_f32_16x16x32_bf16 v[200:203], a[72:75], a[40:43], v[200:203]\n"
    "v_exp_f32 v197, v197\n"
    "v_mfma_f32_16x16x32_bf16 v[200:203], a[76:79], a[44:47], v[200:203]\n"
    "buffer_load_dwordx4 v232, s[20:23], s59 offen offset:3072 nt lds\n"
    "v_mfma_f32_16x16x32_bf16 v[204:207], a[76:79], a[60:63], v[220:223]\n"
    "v_exp_f32 v198, v198\n"
    "v_mfma_f32_16x16x32_bf16 v[204:207], a[72:75], a[56:59], v[204:207]\n"
    "s_add_u32 s59, s59, 16384\n"
    "ds_read_b64_tr_b16 a[98:99], v229 offset:21248\n"
    "v_cvt_pk_bf16_f32 v148, v196, v197\n"
    "v_mfma_f32_16x16x32_bf16 v[204:207], a[68:71], a[52:55], v[204:207]\n"
    "s_and_b32 s59, s59, 0x3ffff\n"
    "v_exp_f32 v199, v199\n"
    "v_mfma_f32_16x16x32_bf16 v[204:207], a[64:67], a[48:51], v[204:207]\n"
    "s_add_u32 m0, s51, 147456\n"
    "ds_read_b128 a[92:95], v228 offset:45824\n"
    "v_cvt_pk_bf16_f32 v149, v198, v199\n"
    "v_mfma_f32_16x16x32_bf16 a[120:123], a[112:115], v[172:175], a[120:123]\n"
    "buffer_load_dwordx4 v236, s[24:27], s53 offen offset:2048  lds\n"
    "v_mfma_f32_16x16x32_bf16 a[120:123], a[112:115], v[188:191], a[120:123]\n"
    "s_waitcnt lgkmcnt(7)\n"
    "buffer_load_dwordx4 v237, s[24:27], s53 offen offset:3072  lds\n"
    "v_mfma_f32_16x16x32_bf16 v[80:83], a[104:107], v[160:163], v[80:83]\n"
    "s_waitcnt lgkmcnt(4)\n"
    "v_exp_f32 v200, v200\n"
    "v_mfma_f32_16x16x32_bf16 v[80:83], a[108:111], v[176:179], v[80:83]\n"
    "s_add_u32 s57, s57, 16384\n"
    "ds_read_b64_tr_b16 a[100:101], v229 offset:25344\n"
    "v_add_f32 v226, v226, v200\n"
    "v_mfma_f32_16x16x32_bf16 v[84:87], a[108:111], v[180:183], v[84:87]\n"
    "s_bitset0_b32 s57, 17\n"
    "v_exp_f32 v201, v201\n"
    "v_mfma_f32_16x16x32_bf16 v[84:87], a[104:107], v[164:167], v[84:87]\n"
    "ds_read_b64_tr_b16 a[102:103], v229 offset:29440\n"
    "v_add_f32 v226, v226, v201\n"
    "v_mfma_f32_16x16x32_bf16 v[88:91], a[104:107], v[168:171], v[88:91]\n"
    "s_waitcnt vmcnt(12) lgkmcnt(2)\n"
    "v_exp_f32 v202, v202\n"
    "v_mfma_f32_16x16x32_bf16 v[88:91], a[108:111], v[184:187], v[88:91]\n"
    "s_barrier\n"
    "ds_read_b128 a[64:67], v228 offset:49152\n"
    "v_add_f32 v226, v226, v202\n"
    "v_mfma_f32_16x16x32_bf16 v[92:95], a[108:111], v[188:191], v[92:95]\n"
    "s_bitset1_b32 s57, 16\n"
    "v_exp_f32 v203, v203\n"
    "v_mfma_f32_16x16x32_bf16 v[92:95], a[104:107], v[172:175], v[92:95]\n"
    "v_cvt_pk_bf16_f32 v152, v200, v201\n"
    "v_add_f32 v226, v226, v203\n"
    "v_mfma_f32_16x16x32_bf16 v[192:195], a[80:83], a[0:3], v[208:211]\n"
    "s_mov_b32 m0, s56\n"
    "v_exp_f32 v204, v204\n"
    "v_mfma_f32_16x16x32_bf16 v[192:195], a[84:87], a[4:7], v[192:195]\n"
    "ds_read_b64_tr_b16 a[104:105], v230 offset:17152\n"
    "v_cvt_pk_bf16_f32 v153, v202, v203\n"
    "v_mfma_f32_16x16x32_bf16 v[192:195], a[88:91], a[8:11], v[192:195]\n"
    "v_exp_f32 v205, v205\n"
    "v_mfma_f32_16x16x32_bf16 v[192:195], a[92:95], a[12:15], v[192:195]\n"
    "buffer_load_dwordx4 v231, s[16:19], s58 offen offset:0 nt lds\n"
    "v_mfma_f32_16x16x32_bf16 v[196:199], a[92:95], a[28:31], v[212:215]\n"
    "v_exp_f32 v206, v206\n"
    "v_mfma_f32_16x16x32_bf16 v[196:199], a[88:91], a[24:27], v[196:199]\n"
    "ds_read_b64_tr_b16 a[106:107], v230 offset:21248\n"
    "v_cvt_pk_bf16_f32 v156, v204, v205\n"
    "v_mfma_f32_16x16x32_bf16 v[196:199], a[84:87], a[20:23], v[196:199]\n"
    "v_exp_f32 v207, v207\n"
    "v_mfma_f32_16x16x32_bf16 v[196:199], a[80:83], a[16:19], v[196:199]\n"
    "ds_read_b128 a[68:71], v228 offset:49408\n"
    "v_cvt_pk_bf16_f32 v157, v206, v207\n"
    "v_mfma_f32_16x16x32_bf16 v[96:99], a[96:99], v[160:163], v[96:99]\n"
    "s_waitcnt lgkmcnt(4)\n"
    "v_exp_f32 v192, v192\n"
    "v_mfma_f32_16x16x32_bf16 v[96:99], a[100:103], v[176:179], v[96:99]\n"
    "ds_read_b64_tr_b16 a[108:109], v230 offset:25344\n"
    "v_add_f32 v224, v224, v192\n"
    "v_mfma_f32_16x16x32_bf16 v[100:103], a[100:103], v[180:183], v[100:103]\n"
    "v_exp_f32 v193, v193\n"
    "v_mfma_f32_16x16x32_bf16 v[100:103], a[96:99], v[164:167], v[100:103]\n"
    "ds_read_b64_tr_b16 a[110:111], v230 offset:29440\n"
    "v_add_f32 v224, v224, v193\n"
    "v_mfma_f32_16x16x32_bf16 v[104:107], a[96:99], v[168:171], v[104:107]\n"
    "v_exp_f32 v194, v194\n"
    "v_mfma_f32_16x16x32_bf16 v[104:107], a[100:103], v[184:187], v[104:107]\n"
    "ds_read_b128 a[72:75], v228 offset:49664\n"
    "v_add_f32 v224, v224, v194\n"
    "v_mfma_f32_16x16x32_bf16 v[108:111], a[100:103], v[188:191], v[108:111]\n"
    "v_exp_f32 v195, v195\n"
    "v_mfma_f32_16x16x32_bf16 v[108:111], a[96:99], v[172:175], v[108:111]\n"
    "v_cvt_pk_bf16_f32 v146, v192, v193\n"
    "v_add_f32 v224, v224, v195\n"
    "v_mfma_f32_16x16x32_bf16 v[200:203], a[80:83], a[32:35], v[216:219]\n"
    "v_exp_f32 v196, v196\n"
    "v_mfma_f32_16x16x32_bf16 v[200:203], a[84:87], a[36:39], v[200:203]\n"
    "ds_read_b64_tr_b16 a[96:97], v229 offset:32768\n"
    "v_cvt_pk_bf16_f32 v147, v194, v195\n"
    "v_mfma_f32_16x16x32_bf16 v[200:203], a[88:91], a[40:43], v[200:203]\n"
    "v_exp_f32 v197, v197\n"
    "v_mfma_f32_16x16x32_bf16 v[200:203], a[92:95], a[44:47], v[200:203]\n"
    "buffer_load_dwordx4 v232, s[16:19], s58 offen offset:1024 nt lds\n"
    "v_mfma_f32_16x16x32_bf16 v[204:207], a[92:95], a[60:63], v[220:223]\n"
    "v_exp_f32 v198, v198\n"
    "v_mfma_f32_16x16x32_bf16 v[204:207], a[88:91], a[56:59], v[204:207]\n"
    "ds_read_b64_tr_b16 a[98:99], v229 offset:36864\n"
    "v_cvt_pk_bf16_f32 v150, v196, v197\n"
    "v_mfma_f32_16x16x32_bf16 v[204:207], a[84:87], a[52:55], v[204:207]\n"
    "v_exp_f32 v199, v199\n"
    "v_mfma_f32_16x16x32_bf16 v[204:207], a[80:83], a[48:51], v[204:207]\n"
    "s_waitcnt lgkmcnt(6)\n"
    "ds_read_b128 a[76:79], v228 offset:49920\n"
    "v_cvt_pk_bf16_f32 v151, v198, v199\n"
    "v_mfma_f32_16x16x32_bf16 v[112:115], a[104:107], v[160:163], v[112:115]\n"
    "s_waitcnt lgkmcnt(4)\n"
    "v_exp_f32 v200, v200\n"
    "v_mfma_f32_16x16x32_bf16 v[112:115], a[108:111], v[176:179], v[112:115]\n"
    "ds_read_b64_tr_b16 a[100:101], v229 offset:40960\n"
    "v_add_f32 v226, v226, v200\n"
    "v_mfma_f32_16x16x32_bf16 v[116:119], a[108:111], v[180:183], v[116:119]\n"
    "v_exp_f32 v201, v201\n"
    "v_mfma_f32_16x16x32_bf16 v[116:119], a[104:107], v[164:167], v[116:119]\n"
    "ds_read_b64_tr_b16 a[102:103], v229 offset:45056\n"
    "v_add_f32 v226, v226, v201\n"
    "v_mfma_f32_16x16x32_bf16 v[120:123], a[104:107], v[168:171], v[120:123]\n"
    "v_exp_f32 v202, v202\n"
    "v_mfma_f32_16x16x32_bf16 v[120:123], a[108:111], v[184:187], v[120:123]\n"
    "ds_read_b128 a[80:83], v228 offset:53248\n"
    "v_add_f32 v226, v226, v202\n"
    "v_mfma_f32_16x16x32_bf16 v[124:127], a[108:111], v[188:191], v[124:127]\n"
    "v_exp_f32 v203, v203\n"
    "v_mfma_f32_16x16x32_bf16 v[124:127], a[104:107], v[172:175], v[124:127]\n"
    "v_cvt_pk_bf16_f32 v154, v200, v201\n"
    "v_add_f32 v226, v226, v203\n"
    "v_mfma_f32_16x16x32_bf16 v[192:195], a[64:67], a[0:3], v[208:211]\n"
    "s_mov_b32 m0, s56\n"
    "v_exp_f32 v204, v204\n"
    "v_mfma_f32_16x16x32_bf16 v[192:195], a[68:71], a[4:7], v[192:195]\n"
    "s_waitcnt lgkmcnt(6)\n"
    "ds_read_b64_tr_b16 a[104:105], v230 offset:32768\n"
    "v_cvt_pk_bf16_f32 v155, v202, v203\n"
    "v_mfma_f32_16x16x32_bf16 v[192:195], a[72:75], a[8:11], v[192:195]\n"
    "s_waitcnt lgkmcnt(4)\n"
    "v_exp_f32 v205, v205\n"
    "v_mfma_f32_16x16x32_bf16 v[192:195], a[76:79], a[12:15], v[192:195]\n"
    "buffer_load_dwordx4 v231, s[16:19], s58 offen offset:2048 nt lds\n"
    "v_mfma_f32_16x16x32_bf16 v[196:199], a[76:79], a[28:31], v[212:215]\n"
    "buffer_load_dword v246, v245, s[36:39], 0 offen sc1\n"
    "v_exp_f32 v206, v206\n"
    "v_mfma_f32_16x16x32_bf16 v[196:199], a[72:75], a[24:27], v[196:199]\n"
    "ds_read_b64_tr_b16 a[106:107], v230 offset:36864\n"
    "v_cvt_pk_bf16_f32 v158, v204, v205\n"
    "v_mfma_f32_16x16x32_bf16 v[196:199], a[68:71], a[20:23], v[196:199]\n"
    "v_exp_f32 v207, v207\n"
    "v_mfma_f32_16x16x32_bf16 v[196:199], a[64:67], a[16:19], v[196:199]\n"
    "ds_read_b128 a[84:87], v228 offset:53504\n"
    "v_cvt_pk_bf16_f32 v159, v206, v207\n"
    "v_mfma_f32_16x16x32_bf16 v[0:3], a[96:99], v[128:131], v[0:3]\n"
    "s_waitcnt lgkmcnt(4)\n"
    "v_exp_f32 v192, v192\n"
    "v_mfma_f32_16x16x32_bf16 v[0:3], a[100:103], v[144:147], v[0:3]\n"
    "s_memrealtime s[8:9]\n"
    "ds_read_b64_tr_b16 a[108:109], v230 offset:40960\n"
    "v_add_f32 v224, v224, v192\n"
    "v_mfma_f32_16x16x32_bf16 v[4:7], a[100:103], v[148:151], v[4:7]\n"
    "v_exp_f32 v193, v193\n"
    "v_mfma_f32_16x16x32_bf16 v[4:7], a[96:99], v[132:135], v[4:7]\n"
    "ds_read_b64_tr_b16 a[110:111], v230 offset:45056\n"
    "v_add_f32 v224, v224, v193\n"
    "v_mfma_f32_16x16x32_bf16 v[8:11], a[96:99], v[136:139], v[8:11]\n"
    "v_exp_f32 v194, v194\n"
    "v_mfma_f32_16x16x32_bf16 v[8:11], a[100:103], v[152:155], v[8:11]\n"
    "ds_read_b128 a[88:91], v228 offset:53760\n"
    "v_add_f32 v224, v224, v194\n"
    "v_mfma_f32_16x16x32_bf16 v[12:15], a[100:103], v[156:159], v[12:15]\n"
    "v_exp_f32 v195, v195\n"
    "v_mfma_f32_16x16x32_bf16 v[12:15], a[96:99], v[140:143], v[12:15]\n"
    "v_cvt_pk_bf16_f32 v160, v192, v193\n"
    "v_add_f32 v224, v224, v195\n"
    "v_mfma_f32_16x16x32_bf16 v[200:203], a[64:67], a[32:35], v[216:219]\n"
    "v_exp_f32 v196, v196\n"
    "v_mfma_f32_16x16x32_bf16 v[200:203], a[68:71], a[36:39], v[200:203]\n"
    "ds_read_b64_tr_b16 a[96:97], v229 offset:33024\n"
    "v_cvt_pk_bf16_f32 v161, v194, v195\n"
    "v_mfma_f32_16x16x32_bf16 v[200:203], a[72:75], a[40:43], v[200:203]\n"
    "v_exp_f32 v197, v197\n"
    "v_mfma_f32_16x16x32_bf16 v[200:203], a[76:79], a[44:47], v[200:203]\n"
    "buffer_load_dwordx4 v232, s[16:19], s58 offen offset:3072 nt lds\n"
    "v_mfma_f32_16x16x32_bf16 v[204:207], a[76:79], a[60:63], v[220:223]\n"
    "v_exp_f32 v198, v198\n"
    "v_mfma_f32_16x16x32_bf16 v[204:207], a[72:75], a[56:59], v[204:207]\n"
    "s_add_u32 s58, s58, 16384\n"
    "ds_read_b64_tr_b16 a[98:99], v229 offset:37120\n"
    "v_cvt_pk_bf16_f32 v164, v196, v197\n"
    "v_mfma_f32_16x16x32_bf16 v[204:207], a[68:71], a[52:55], v[204:207]\n"
    "s_and_b32 s58, s58, 0x3ffff\n"
    "v_exp_f32 v199, v199\n"
    "v_mfma_f32_16x16x32_bf16 v[204:207], a[64:67], a[48:51], v[204:207]\n"
    "s_waitcnt lgkmcnt(6)\n"
    "ds_read_b128 a[92:95], v228 offset:54016\n"
    "v_cvt_pk_bf16_f32 v165, v198, v199\n"
    "v_mfma_f32_16x16x32_bf16 v[16:19], a[104:107], v[128:131], v[16:19]\n"
    "s_waitcnt lgkmcnt(4)\n"
    "v_exp_f32 v200, v200\n"
    "v_mfma_f32_16x16x32_bf16 v[16:19], a[108:111], v[144:147], v[16:19]\n"
    "ds_read_b64_tr_b16 a[100:101], v229 offset:41216\n"
    "v_add_f32 v226, v226, v200\n"
    "v_mfma_f32_16x16x32_bf16 v[20:23], a[108:111], v[148:151], v[20:23]\n"
    "v_exp_f32 v201, v201\n"
    "v_mfma_f32_16x16x32_bf16 v[20:23], a[104:107], v[132:135], v[20:23]\n"
    "s_add_u32 s56, s56, 16384\n"
    "ds_read_b64_tr_b16 a[102:103], v229 offset:45312\n"
    "v_add_f32 v226, v226, v201\n"
    "v_mfma_f32_16x16x32_bf16 v[24:27], a[104:107], v[136:139], v[24:27]\n"
    "s_and_b32 s56, s56, 0xffff\n"
    "v_exp_f32 v202, v202\n"
    "v_mfma_f32_16x16x32_bf16 v[24:27], a[108:111], v[152:155], v[24:27]\n"
    "ds_read_b128 a[64:67], v228 offset:57344\n"
    "v_add_f32 v226, v226, v202\n"
    "v_mfma_f32_16x16x32_bf16 v[28:31], a[108:111], v[156:159], v[28:31]\n"
    "v_exp_f32 v203, v203\n"
    "v_mfma_f32_16x16x32_bf16 v[28:31], a[104:107], v[140:143], v[28:31]\n"
    "v_cvt_pk_bf16_f32 v168, v200, v201\n"
    "v_add_f32 v226, v226, v203\n"
    "v_mfma_f32_16x16x32_bf16 v[192:195], a[80:83], a[0:3], v[208:211]\n"
    "s_mov_b32 m0, s57\n"
    "v_exp_f32 v204, v204\n"
    "v_mfma_f32_16x16x32_bf16 v[192:195], a[84:87], a[4:7], v[192:195]\n"
    "s_waitcnt lgkmcnt(6)\n"
    "ds_read_b64_tr_b16 a[104:105], v230 offset:33024\n"
    "v_cvt_pk_bf16_f32 v169, v202, v203\n"
    "v_mfma_f32_16x16x32_bf16 v[192:195], a[88:91], a[8:11], v[192:195]\n"
    "s_waitcnt lgkmcnt(4)\n"
    "v_exp_f32 v205, v205\n"
    "v_mfma_f32_16x16x32_bf16 v[192:195], a[92:95], a[12:15], v[192:195]\n"
    "buffer_load_dwordx4 v231, s[20:23], s59 offen offset:0 nt lds\n"
    "v_mfma_f32_16x16x32_bf16 v[196:199], a[92:95], a[28:31], v[212:215]\n"
    "v_exp_f32 v206, v206\n"
    "v_mfma_f32_16x16x32_bf16 v[196:199], a[88:91], a[24:27], v[196:199]\n"
    "ds_read_b64_tr_b16 a[106:107], v230 offset:37120\n"
    "v_cvt_pk_bf16_f32 v172, v204, v205\n"
    "v_mfma_f32_16x16x32_bf16 v[196:199], a[84:87], a[20:23], v[196:199]\n"
    "v_exp_f32 v207, v207\n"
    "v_mfma_f32_16x16x32_bf16 v[196:199], a[80:83], a[16:19], v[196:199]\n"
    "ds_read_b128 a[68:71], v228 offset:57600\n"
    "v_cvt_pk_bf16_f32 v173, v206, v207\n"
    "v_mfma_f32_16x16x32_bf16 a[116:119], a[112:115], v[132:135], a[116:119]\n"
    "v_mfma_f32_16x16x32_bf16 a[116:119], a[112:115], v[148:151], a[116:119]\n"
    "v_mfma_f32_16x16x32_bf16 v[32:35], a[96:99], v[128:131], v[32:35]\n"
    "s_waitcnt lgkmcnt(4)\n"
    "v_exp_f32 v192, v192\n"
    "v_mfma_f32_16x16x32_bf16 v[32:35], a[100:103], v[144:147], v[32:35]\n"
    "ds_read_b64_tr_b16 a[108:109], v230 offset:41216\n"
    "v_add_f32 v224, v224, v192\n"
    "v_mfma_f32_16x16x32_bf16 v[36:39], a[100:103], v[148:151], v[36:39]\n"
    "v_exp_f32 v193, v193\n"
    "v_mfma_f32_16x16x32_bf16 v[36:39], a[96:99], v[132:135], v[36:39]\n"
    "ds_read_b64_tr_b16 a[110:111], v230 offset:45312\n"
    "v_add_f32 v224, v224, v193\n"
    "v_mfma_f32_16x16x32_bf16 v[40:43], a[96:99], v[136:139], v[40:43]\n"
    "v_exp_f32 v194, v194\n"
    "v_mfma_f32_16x16x32_bf16 v[40:43], a[100:103], v[152:155], v[40:43]\n"
    "ds_read_b128 a[72:75], v228 offset:57856\n"
    "v_add_f32 v224, v224, v194\n"
    "v_mfma_f32_16x16x32_bf16 v[44:47], a[100:103], v[156:159], v[44:47]\n"
    "v_exp_f32 v195, v195\n"
    "v_mfma_f32_16x16x32_bf16 v[44:47], a[96:99], v[140:143], v[44:47]\n"
    "v_cvt_pk_bf16_f32 v162, v192, v193\n"
    "v_add_f32 v224, v224, v195\n"
    "v_mfma_f32_16x16x32_bf16 v[200:203], a[80:83], a[32:35], v[216:219]\n"
    "v_exp_f32 v196, v196\n"
    "v_mfma_f32_16x16x32_bf16 v[200:203], a[84:87], a[36:39], v[200:203]\n"
    "ds_read_b64_tr_b16 a[96:97], v229 offset:33280\n"
    "v_cvt_pk_bf16_f32 v163, v194, v195\n"
    "v_mfma_f32_16x16x32_bf16 v[200:203], a[88:91], a[40:43], v[200:203]\n"
    "v_exp_f32 v197, v197\n"
    "v_mfma_f32_16x16x32_bf16 v[200:203], a[92:95], a[44:47], v[200:203]\n"
    "buffer_load_dwordx4 v232, s[20:23], s59 offen offset:1024 nt lds\n"
    "v_mfma_f32_16x16x32_bf16 v[204:207], a[92:95], a[60:63], v[220:223]\n"
    "v_exp_f32 v198, v198\n"
    "v_mfma_f32_16x16x32_bf16 v[204:207], a[88:91], a[56:59], v[204:207]\n"
    "ds_read_b64_tr_b16 a[98:99], v229 offset:37376\n"
    "v_cvt_pk_bf16_f32 v166, v196, v197\n"
    "v_mfma_f32_16x16x32_bf16 v[204:207], a[84:87], a[52:55], v[204:207]\n"
    "v_exp_f32 v199, v199\n"
    "v_mfma_f32_16x16x32_bf16 v[204:207], a[80:83], a[48:51], v[204:207]\n"
    "s_waitcnt lgkmcnt(6)\n"
    "ds_read_b128 a[76:79], v228 offset:58112\n"
    "v_cvt_pk_bf16_f32 v167, v198, v199\n"
    "v_mfma_f32_16x16x32_bf16 v[48:51], a[104:107], v[128:131], v[48:51]\n"
    "s_waitcnt lgkmcnt(4)\n"
    "v_exp_f32 v200, v200\n"
    "v_mfma_f32_16x16x32_bf16 v[48:51], a[108:111], v[144:147], v[48:51]\n"
    "s_sub_u32 s11, s60, s8\n"
    "ds_read_b64_tr_b16 a[100:101], v229 offset:41472\n"
    "v_add_f32 v226, v226, v200\n"
    "v_mfma_f32_16x16x32_bf16 v[52:55], a[108:111], v[148:151], v[52:55]\n"
    "s_add_u32 s60, s60, 142\n"
    "v_exp_f32 v201, v201\n"
    "v_mfma_f32_16x16x32_bf16 v[52:55], a[104:107], v[132:135], v[52:55]\n"
    "s_cmp_gt_i32 s11, 2\n"
    "ds_read_b64_tr_b16 a[102:103], v229 offset:45568\n"
    "v_add_f32 v226, v226, v201\n"
    "v_mfma_f32_16x16x32_bf16 v[56:59], a[104:107], v[136:139], v[56:59]\n"
    "s_cbranch_scc1 Lpa10x%=\n"
    "Lpb10x%=:\n"
    "v_exp_f32 v202, v202\n"
    "v_mfma_f32_16x16x32_bf16 v[56:59], a[108:111], v[152:155], v[56:59]\n"
    "ds_read_b128 a[80:83], v228 offset:61440\n"
    "v_add_f32 v226, v226, v202\n"
    "v_mfma_f32_16x16x32_bf16 v[60:63], a[108:111], v[156:159], v[60:63]\n"
    "v_exp_f32 v203, v203\n"
    "v_mfma_f32_16x16x32_bf16 v[60:63], a[104:107], v[140:143], v[60:63]\n"
    "v_cvt_pk_bf16_f32 v170, v200, v201\n"
    "v_add_f32 v226, v226, v203\n"
    "v_mfma_f32_16x16x32_bf16 v[192:195], a[64:67], a[0:3], v[208:211]\n"
    "v_exp_f32 v204, v204\n"
    "v_mfma_f32_16x16x32_bf16 v[192:195], a[68:71], a[4:7], v[192:195]\n"
    "s_waitcnt lgkmcnt(6)\n"
    "ds_read_b64_tr_b16 a[104:105], v230 offset:33280\n"
    "v_cvt_pk_bf16_f32 v171, v202, v203\n"
    "v_mfma_f32_16x16x32_bf16 v[192:195], a[72:75], a[8:11], v[192:195]\n"
    "s_waitcnt lgkmcnt(4)\n"
    "v_exp_f32 v205, v205\n"
    "v_mfma_f32_16x16x32_bf16 v[192:195], a[76:79], a[12:15], v[192:195]\n"
    "buffer_load_dwordx4 v231, s[20:23], s59 offen offset:2048 nt lds\n"
    "v_mfma_f32_16x16x32_bf16 v[196:199], a[76:79], a[28:31], v[212:215]\n"
    "v_exp_f32 v206, v206\n"
    "v_mfma_f32_16x16x32_bf16 v[196:199], a[72:75], a[24:27], v[196:199]\n"
    "ds_read_b64_tr_b16 a[106:107], v230 offset:37376\n"
    "v_cvt_pk_bf16_f32 v174, v204, v205\n"
    "v_mfma_f32_16x16x32_bf16 v[196:199], a[68:71], a[20:23], v[196:199]\n"
    "v_exp_f32 v207, v207\n"
    "v_mfma_f32_16x16x32_bf16 v[196:199], a[64:67], a[16:19], v[196:199]\n"
    "ds_read_b128 a[84:87], v228 offset:61696\n"
    "v_cvt_pk_bf16_f32 v175, v206, v207\n"
    "v_mfma_f32_16x16x32_bf16 v[64:67], a[96:99], v[128:131], v[64:67]\n"
    "s_waitcnt lgkmcnt(4)\n"
    "v_exp_f32 v192, v192\n"
    "v_mfma_f32_16x16x32_bf16 v[64:67], a[100:103], v[144:147], v[64:67]\n"
    "ds_read_b64_tr_b16 a[108:109], v230 offset:41472\n"
    "v_add_f32 v224, v224, v192\n"
    "v_mfma_f32_16x16x32_bf16 v[68:71], a[100:103], v[148:151], v[68:71]\n"
    "v_exp_f32 v193, v193\n"
    "v_mfma_f32_16x16x32_bf16 v[68:71], a[96:99], v[132:135], v[68:71]\n"
    "ds_read_b64_tr_b16 a[110:111], v230 offset:45568\n"
    "v_add_f32 v224, v224, v193\n"
    "v_mfma_f32_16x16x32_bf16 v[72:75], a[96:99], v[136:139], v[72:75]\n"
    "v_exp_f32 v194, v194\n"
    "v_mfma_f32_16x16x32_bf16 v[72:75], a[100:103], v[152:155], v[72:75]\n"
    "ds_read_b128 a[88:91], v228 offset:61952\n"
    "v_add_f32 v224, v224, v194\n"
    "v_mfma_f32_16x16x32_bf16 v[76:79], a[100:103], v[156:159], v[76:79]\n"
    "v_exp_f32 v195, v195\n"
    "v_mfma_f32_16x16x32_bf16 v[76:79], a[96:99], v[140:143], v[76:79]\n"
    "v_cvt_pk_bf16_f32 v176, v192, v193\n"
    "v_add_f32 v224, v224, v195\n"
    "v_mfma_f32_16x16x32_bf16 v[200:203], a[64:67], a[32:35], v[216:219]\n"
    "v_exp_f32 v196, v196\n"
    "v_mfma_f32_16x16x32_bf16 v[200:203], a[68:71], a[36:39], v[200:203]\n"
    "ds_read_b64_tr_b16 a[96:97], v229 offset:33536\n"
    "v_cvt_pk_bf16_f32 v177, v194, v195\n"
    "v_mfma_f32_16x16x32_bf16 v[200:203], a[72:75], a[40:43], v[200:203]\n"
    "v_exp_f32 v197, v197\n"
    "v_mfma_f32_16x16x32_bf16 v[200:203], a[76:79], a[44:47], v[200:203]\n"
    "buffer_load_dwordx4 v232, s[20:23], s59 offen offset:3072 nt lds\n"
    "v_mfma_f32_16x16x32_bf16 v[204:207], a[76:79], a[60:63], v[220:223]\n"
    "v_exp_f32 v198, v198\n"
    "v_mfma_f32_16x16x32_bf16 v[204:207], a[72:75], a[56:59], v[204:207]\n"
    "s_add_u32 s59, s59, 16384\n"
    "ds_read_b64_tr_b16 a[98:99], v229 offset:37632\n"
    "v_cvt_pk_bf16_f32 v180, v196, v197\n"
    "v_mfma_f32_16x16x32_bf16 v[204:207], a[68:71], a[52:55], v[204:207]\n"
    "s_and_b32 s59, s59, 0x3ffff\n"
    "v_exp_f32 v199, v199\n"
    "v_mfma_f32_16x16x32_bf16 v[204:207], a[64:67], a[48:51], v[204:207]\n"
    "ds_read_b128 a[92:95], v228 offset:62208\n"
    "v_cvt_pk_bf16_f32 v181, v198, v199\n"
    "v_mfma_f32_16x16x32_bf16 a[120:123], a[112:115], v[140:143], a[120:123]\n"
    "v_mfma_f32_16x16x32_bf16 a[120:123], a[112:115], v[156:159], a[120:123]\n"
    "s_waitcnt lgkmcnt(7)\n"
    "v_mfma_f32_16x16x32_bf16 v[80:83], a[104:107], v[128:131], v[80:83]\n"
    "s_waitcnt lgkmcnt(4)\n"
    "v_exp_f32 v200, v200\n"
    "v_mfma_f32_16x16x32_bf16 v[80:83], a[108:111], v[144:147], v[80:83]\n"
    "s_add_u32 s57, s57, 16384\n"
    "ds_read_b64_tr_b16 a[100:101], v229 offset:41728\n"
    "v_add_f32 v226, v226, v200\n"
    "v_mfma_f32_16x16x32_bf16 v[84:87], a[108:111], v[148:151], v[84:87]\n"
    "s_bitset0_b32 s57, 17\n"
    "v_exp_f32 v201, v201\n"
    "v_mfma_f32_16x16x32_bf16 v[84:87], a[104:107], v[132:135], v[84:87]\n"
    "ds_read_b64_tr_b16 a[102:103], v229 offset:45824\n"
    "v_add_f32 v226, v226, v201\n"
    "v_mfma_f32_16x16x32_bf16 v[88:91], a[104:107], v[136:139], v[88:91]\n"
    "s_waitcnt vmcnt(11) lgkmcnt(2)\n"
    "v_exp_f32 v202, v202\n"
    "v_mfma_f32_16x16x32_bf16 v[88:91], a[108:111], v[152:155], v[88:91]\n"
    "s_barrier\n"
    "ds_read_b128 a[64:67], v228 offset:0\n"
    "v_add_f32 v226, v226, v202\n"
    "v_mfma_f32_16x16x32_bf16 v[92:95], a[108:111], v[156:159], v[92:95]\n"
    "s_bitset1_b32 s57, 16\n"
    "v_exp_f32 v203, v203\n"
    "v_mfma_f32_16x16x32_bf16 v[92:95], a[104:107], v[140:143], v[92:95]\n"
    "v_cvt_pk_bf16_f32 v184, v200, v201\n"
    "v_add_f32 v226, v226, v203\n"
    "v_mfma_f32_16x16x32_bf16 v[192:195], a[80:83], a[0:3], v[208:211]\n"
    "s_mov_b32 m0, s56\n"
    "v_exp_f32 v204, v204\n"
    "v_mfma_f32_16x16x32_bf16 v[192:195], a[84:87], a[4:7], v[192:195]\n"
    "ds_read_b64_tr_b16 a[104:105], v230 offset:33536\n"
    "v_cvt_pk_bf16_f32 v185, v202, v203\n"
    "v_mfma_f32_16x16x32_bf16 v[192:195], a[88:91], a[8:11], v[192:195]\n"
    "v_exp_f32 v205, v205\n"
    "v_mfma_f32_16x16x32_bf16 v[192:195], a[92:95], a[12:15], v[192:195]\n"
    "buffer_load_dwordx4 v231, s[16:19], s58 offen offset:0 nt lds\n"
    "v_mfma_f32_16x16x32_bf16 v[196:199], a[92:95], a[28:31], v[212:215]\n"
    "v_exp_f32 v206, v206\n"
    "v_mfma_f32_16x16x32_bf16 v[196:199], a[88:91], a[24:27], v[196:199]\n"
    "ds_read_b64_tr_b16 a[106:107], v230 offset:37632\n"
    "v_cvt_pk_bf16_f32 v188, v204, v205\n"
    "v_mfma_f32_16x16x32_bf16 v[196:199], a[84:87], a[20:23], v[196:199]\n"
    "v_exp_f32 v207, v207\n"
    "v_mfma_f32_16x16x32_bf16 v[196:199], a[80:83], a[16:19], v[196:199]\n"
    "ds_read_b128 a[68:71], v228 offset:256\n"
    "v_cvt_pk_bf16_f32 v189, v206, v207\n"
    "v_mfma_f32_16x16x32_bf16 v[96:99], a[96:99], v[128:131], v[96:99]\n"
    "s_waitcnt lgkmcnt(4)\n"
    "v_exp_f32 v192, v192\n"
    "v_mfma_f32_16x16x32_bf16 v[96:99], a[100:103], v[144:147], v[96:99]\n"
    "ds_read_b64_tr_b16 a[108:109], v230 offset:41728\n"
    "v_add_f32 v224, v224, v192\n"
    "v_mfma_f32_16x16x32_bf16 v[100:103], a[100:103], v[148:151], v[100:103]\n"
    "v_exp_f32 v193, v193\n"
    "v_mfma_f32_16x16x32_bf16 v[100:103], a[96:99], v[132:135], v[100:103]\n"
    "ds_read_b64_tr_b16 a[110:111], v230 offset:45824\n"
    "v_add_f32 v224, v224, v193\n"
    "v_mfma_f32_16x16x32_bf16 v[104:107], a[96:99], v[136:139], v[104:107]\n"
    "v_exp_f32 v194, v194\n"
    "v_mfma_f32_16x16x32_bf16 v[104:107], a[100:103], v[152:155], v[104:107]\n"
    "ds_read_b128 a[72:75], v228 offset:512\n"
    "v_add_f32 v224, v224, v194\n"
    "v_mfma_f32_16x16x32_bf16 v[108:111], a[100:103], v[156:159], v[108:111]\n"
    "v_exp_f32 v195, v195\n"
    "v_mfma_f32_16x16x32_bf16 v[108:111], a[96:99], v[140:143], v[108:111]\n"
    "v_cvt_pk_bf16_f32 v178, v192, v193\n"
    "v_add_f32 v224, v224, v195\n"
    "v_mfma_f32_16x16x32_bf16 v[200:203], a[80:83], a[32:35], v[216:219]\n"
    "v_exp_f32 v196, v196\n"
    "v_mfma_f32_16x16x32_bf16 v[200:203], a[84:87], a[36:39], v[200:203]\n"
    "ds_read_b64_tr_b16 a[96:97], v229 offset:49152\n"
    "v_cvt_pk_bf16_f32 v179, v194, v195\n"
    "v_mfma_f32_16x16x32_bf16 v[200:203], a[88:91], a[40:43], v[200:203]\n"
    "v_exp_f32 v197, v197\n"
    "v_mfma_f32_16x16x32_bf16 v[200:203], a[92:95], a[44:47], v[200:203]\n"
    "buffer_load_dwordx4 v232, s[16:19], s58 offen offset:1024 nt lds\n"
    "v_mfma_f32_16x16x32_bf16 v[204:207], a[92:95], a[60:63], v[220:223]\n"
    "v_exp_f32 v198, v198\n"
    "v_mfma_f32_16x16x32_bf16 v[204:207], a[88:91], a[56:59], v[204:207]\n"
    "ds_read_b64_tr_b16 a[98:99], v229 offset:53248\n"
    "v_cvt_pk_bf16_f32 v182, v196, v197\n"
    "v_mfma_f32_16x16x32_bf16 v[204:207], a[84:87], a[52:55], v[204:207]\n"
    "v_exp_f32 v199, v199\n"
    "v_mfma_f32_16x16x32_bf16 v[204:207], a[80:83], a[48:51], v[204:207]\n"
    "s_waitcnt lgkmcnt(6)\n"
    "ds_read_b128 a[76:79], v228 offset:768\n"
    "v_cvt_pk_bf16_f32 v183, v198, v199\n"
    "v_mfma_f32_16x16x32_bf16 v[112:115], a[104:107], v[128:131], v[112:115]\n"
    "s_waitcnt lgkmcnt(4)\n"
    "v_exp_f32 v200, v200\n"
    "v_mfma_f32_16x16x32_bf16 v[112:115], a[108:111], v[144:147], v[112:115]\n"
    "ds_read_b64_tr_b16 a[100:101], v229 offset:57344\n"
    "v_add_f32 v226, v226, v200\n"
    "v_mfma_f32_16x16x32_bf16 v[116:119], a[108:111], v[148:151], v[116:119]\n"
    "v_exp_f32 v201, v201\n"
    "v_mfma_f32_16x16x32_bf16 v[116:119], a[104:107], v[132:135], v[116:119]\n"
    "ds_read_b64_tr_b16 a[102:103], v229 offset:61440\n"
    "v_add_f32 v226, v226, v201\n"
    "v_mfma_f32_16x16x32_bf16 v[120:123], a[104:107], v[136:139], v[120:123]\n"
    "v_exp_f32 v202, v202\n"
    "v_mfma_f32_16x16x32_bf16 v[120:123], a[108:111], v[152:155], v[120:123]\n"
    "ds_read_b128 a[80:83], v228 offset:4096\n"
    "v_add_f32 v226, v226, v202\n"
    "v_mfma_f32_16x16x32_bf16 v[124:127], a[108:111], v[156:159], v[124:127]\n"
    "v_exp_f32 v203, v203\n"
    "v_mfma_f32_16x16x32_bf16 v[124:127], a[104:107], v[140:143], v[124:127]\n"
    "v_cvt_pk_bf16_f32 v186, v200, v201\n"
    "v_add_f32 v226, v226, v203\n"
    "v_mfma_f32_16x16x32_bf16 v[192:195], a[64:67], a[0:3], v[208:211]\n"
    "s_mov_b32 m0, s56\n"
    "v_exp_f32 v204, v204\n"
    "v_mfma_f32_16x16x32_bf16 v[192:195], a[68:71], a[4:7], v[192:195]\n"
    "s_waitcnt lgkmcnt(6)\n"
    "ds_read_b64_tr_b16 a[104:105], v230 offset:49152\n"
    "v_cvt_pk_bf16_f32 v187, v202, v203\n"
    "v_mfma_f32_16x16x32_bf16 v[192:195], a[72:75], a[8:11], v[192:195]\n"
    "s_waitcnt lgkmcnt(4)\n"
    "v_exp_f32 v205, v205\n"
    "v_mfma_f32_16x16x32_bf16 v[192:195], a[76:79], a[12:15], v[192:195]\n"
    "buffer_load_dwordx4 v231, s[16:19], s58 offen offset:2048 nt lds\n"
    "v_mfma_f32_16x16x32_bf16 v[196:199], a[76:79], a[28:31], v[212:215]\n"
    "v_exp_f32 v206, v206\n"
    "v_mfma_f32_16x16x32_bf16 v[196:199], a[72:75], a[24:27], v[196:199]\n"
    "ds_read_b64_tr_b16 a[106:107], v230 offset:53248\n"
    "v_cvt_pk_bf16_f32 v190, v204, v205\n"
    "v_mfma_f32_16x16x32_bf16 v[196:199], a[68:71], a[20:23], v[196:199]\n"
    "v_exp_f32 v207, v207\n"
    "v_mfma_f32_16x16x32_bf16 v[196:199], a[64:67], a[16:19], v[196:199]\n"
    "ds_read_b128 a[84:87], v228 offset:4352\n"
    "v_cvt_pk_bf16_f32 v191, v206, v207\n"
    "v_mfma_f32_16x16x32_bf16 v[0:3], a[96:99], v[160:163], v[0:3]\n"
    "s_waitcnt lgkmcnt(4)\n"
    "v_exp_f32 v192, v192\n"
    "v_mfma_f32_16x16x32_bf16 v[0:3], a[100:103], v[176:179], v[0:3]\n"
    "s_memrealtime s[8:9]\n"
    "ds_read_b64_tr_b16 a[108:109], v230 offset:57344\n"
    "v_add_f32 v224, v224, v192\n"
    "v_mfma_f32_16x16x32_bf16 v[4:7], a[100:103], v[180:183], v[4:7]\n"
    "Lpg10%=:\n"
    "s_cbranch_execz Lwb1%=\n"
    "v_exp_f32 v193, v193\n"
    "v_mfma_f32_16x16x32_bf16 v[4:7], a[96:99], v[164:167], v[4:7]\n"
    "ds_read_b64_tr_b16 a[110:111], v230 offset:61440\n"
    "v_add_f32 v224, v224, v193\n"
    "v_mfma_f32_16x16x32_bf16 v[8:11], a[96:99], v[168:171], v[8:11]\n"
    "v_exp_f32 v194, v194\n"
    "v_mfma_f32_16x16x32_bf16 v[8:11], a[100:103], v[184:187], v[8:11]\n"
    "ds_read_b128 a[88:91], v228 offset:4608\n"
    "v_add_f32 v224, v224, v194\n"
    "v_mfma_f32_16x16x32_bf16 v[12:15], a[100:103], v[188:191], v[12:15]\n"
    "v_exp_f32 v195, v195\n"
    "v_mfma_f32_16x16x32_bf16 v[12:15], a[96:99], v[172:175], v[12:15]\n"
    "v_cvt_pk_bf16_f32 v128, v192, v193\n"
    "v_add_f32 v224, v224, v195\n"
    "v_mfma_f32_16x16x32_bf16 v[200:203], a[64:67], a[32:35], v[216:219]\n"
    "v_exp_f32 v196, v196\n"
    "v_mfma_f32_16x16x32_bf16 v[200:203], a[68:71], a[36:39], v[200:203]\n"
    "ds_read_b64_tr_b16 a[96:97], v229 offset:49408\n"
    "v_cvt_pk_bf16_f32 v129, v194, v195\n"
    "v_mfma_f32_16x16x32_bf16 v[200:203], a[72:75], a[40:43], v[200:203]\n"
    "v_exp_f32 v197, v197\n"
    "v_mfma_f32_16x16x32_bf16 v[200:203], a[76:79], a[44:47], v[200:203]\n"
    "buffer_load_dwordx4 v232, s[16:19], s58 offen offset:3072 nt lds\n"
    "v_mfma_f32_16x16x32_bf16 v[204:207], a[76:79], a[60:63], v[220:223]\n"
    "v_exp_f32 v198, v198\n"
    "v_mfma_f32_16x16x32_bf16 v[204:207], a[72:75], a[56:59], v[204:207]\n"
    "s_add_u32 s58, s58, 16384\n"
    "ds_read_b64_tr_b16 a[98:99], v229 offset:53504\n"
    "v_cvt_pk_bf16_f32 v132, v196, v197\n"
    "v_mfma_f32_16x16x32_bf16 v[204:207], a[68:71], a[52:55], v[204:207]\n"
    "s_and_b32 s58, s58, 0x3ffff\n"
    "v_exp_f32 v199, v199\n"
    "v_mfma_f32_16x16x32_bf16 v[204:207], a[64:67], a[48:51], v[204:207]\n"
    "s_waitcnt lgkmcnt(6)\n"
    "ds_read_b128 a[92:95], v228 offset:4864\n"
    "v_cvt_pk_bf16_f32 v133, v198, v199\n"
    "v_mfma_f32_16x16x32_bf16 v[16:19], a[104:107], v[160:163], v[16:19]\n"
    "s_waitcnt lgkmcnt(4)\n"
    "v_exp_f32 v200, v200\n"
    "v_mfma_f32_16x16x32_bf16 v[16:19], a[108:111], v[176:179], v[16:19]\n"
    "ds_read_b64_tr_b16 a[100:101], v229 offset:57600\n"
    "v_add_f32 v226, v226, v200\n"
    "v_mfma_f32_16x16x32_bf16 v[20:23], a[108:111], v[180:183], v[20:23]\n"
    "v_exp_f32 v201, v201\n"
    "v_mfma_f32_16x16x32_bf16 v[20:23], a[104:107], v[164:167], v[20:23]\n"
    "s_add_u32 s56, s56, 16384\n"
    "ds_read_b64_tr_b16 a[102:103], v229 offset:61696\n"
    "v_add_f32 v226, v226, v201\n"
    "v_mfma_f32_16x16x32_bf16 v[24:27], a[104:107], v[168:171], v[24:27]\n"
    "s_and_b32 s56, s56, 0xffff\n"
    "v_exp_f32 v202, v202\n"
    "v_mfma_f32_16x16x32_bf16 v[24:27], a[108:111], v[184:187], v[24:27]\n"
    "ds_read_b128 a[64:67], v228 offset:8192\n"
    "v_add_f32 v226, v226, v202\n"
    "v_mfma_f32_16x16x32_bf16 v[28:31], a[108:111], v[188:191], v[28:31]\n"
    "v_exp_f32 v203, v203\n"
    "v_mfma_f32_16x16x32_bf16 v[28:31], a[104:107], v[172:175], v[28:31]\n"
    "v_cvt_pk_bf16_f32 v136, v200, v201\n"
    "v_add_f32 v226, v226, v203\n"
    "v_mfma_f32_16x16x32_bf16 v[192:195], a[80:83], a[0:3], v[208:211]\n"
    "s_mov_b32 m0, s57\n"
    "v_exp_f32 v204, v204\n"
    "v_mfma_f32_16x16x32_bf16 v[192:195], a[84:87], a[4:7], v[192:195]\n"
    "s_waitcnt lgkmcnt(6)\n"
    "ds_read_b64_tr_b16 a[104:105], v230 offset:49408\n"
    "v_cvt_pk_bf16_f32 v137, v202, v203\n"
    "v_mfma_f32_16x16x32_bf16 v[192:195], a[88:91], a[8:11], v[192:195]\n"
    "s_waitcnt lgkmcnt(4)\n"
    "v_exp_f32 v205, v205\n"
    "v_mfma_f32_16x16x32_bf16 v[192:195], a[92:95], a[12:15], v[192:195]\n"
    "buffer_load_dwordx4 v231, s[20:23], s59 offen offset:0 nt lds\n"
    "v_mfma_f32_16x16x32_bf16 v[196:199], a[92:95], a[28:31], v[212:215]\n"
    "v_exp_f32 v206, v206\n"
    "v_mfma_f32_16x16x32_bf16 v[196:199], a[88:91], a[24:27], v[196:199]\n"
    "ds_read_b64_tr_b16 a[106:107], v230 offset:53504\n"
    "v_cvt_pk_bf16_f32 v140, v204, v205\n"
    "v_mfma_f32_16x16x32_bf16 v[196:199], a[84:87], a[20:23], v[196:199]\n"
    "v_exp_f32 v207, v207\n"
    "v_mfma_f32_16x16x32_bf16 v[196:199], a[80:83], a[16:19], v[196:199]\n"
    "ds_read_b128 a[68:71], v228 offset:8448\n"
    "v_cvt_pk_bf16_f32 v141, v206, v207\n"
    "v_mfma_f32_16x16x32_bf16 a[116:119], a[112:115], v[164:167], a[116:119]\n"
    "v_mfma_f32_16x16x32_bf16 a[116:119], a[112:115], v[180:183], a[116:119]\n"
    "v_mfma_f32_16x16x32_bf16 v[32:35], a[96:99], v[160:163], v[32:35]\n"
    "s_waitcnt lgkmcnt(4)\n"
    "v_exp_f32 v192, v192\n"
    "v_mfma_f32_16x16x32_bf16 v[32:35], a[100:103], v[176:179], v[32:35]\n"
    "ds_read_b64_tr_b16 a[108:109], v230 offset:57600\n"
    "v_add_f32 v224, v224, v192\n"
    "v_mfma_f32_16x16x32_bf16 v[36:39], a[100:103], v[180:183], v[36:39]\n"
    "v_exp_f32 v193, v193\n"
    "v_mfma_f32_16x16x32_bf16 v[36:39], a[96:99], v[164:167], v[36:39]\n"
    "ds_read_b64_tr_b16 a[110:111], v230 offset:61696\n"
    "v_add_f32 v224, v224, v193\n"
    "v_mfma_f32_16x16x32_bf16 v[40:43], a[96:99], v[168:171], v[40:43]\n"
    "v_exp_f32 v194, v194\n"
    "v_mfma_f32_16x16x32_bf16 v[40:43], a[100:103], v[184:187], v[40:43]\n"
    "ds_read_b128 a[72:75], v228 offset:8704\n"
    "v_add_f32 v224, v224, v194\n"
    "v_mfma_f32_16x16x32_bf16 v[44:47], a[100:103], v[188:191], v[44:47]\n"
    "v_exp_f32 v195, v195\n"
    "v_mfma_f32_16x16x32_bf16 v[44:47], a[96:99], v[172:175], v[44:47]\n"
    "v_cvt_pk_bf16_f32 v130, v192, v193\n"
    "v_add_f32 v224, v224, v195\n"
    "v_mfma_f32_16x16x32_bf16 v[200:203], a[80:83], a[32:35], v[216:219]\n"
    "v_exp_f32 v196, v196\n"
    "v_mfma_f32_16x16x32_bf16 v[200:203], a[84:87], a[36:39], v[200:203]\n"
    "ds_read_b64_tr_b16 a[96:97], v229 offset:49664\n"
    "v_cvt_pk_bf16_f32 v131, v194, v195\n"
    "v_mfma_f32_16x16x32_bf16 v[200:203], a[88:91], a[40:43], v[200:203]\n"
    "v_exp_f32 v197, v197\n"
    "v_mfma_f32_16x16x32_bf16 v[200:203], a[92:95], a[44:47], v[200:203]\n"
    "buffer_load_dwordx4 v232, s[20:23], s59 offen offset:1024 nt lds\n"
    "v_mfma_f32_16x16x32_bf16 v[204:207], a[92:95], a[60:63], v[220:223]\n"
    "v_exp_f32 v198, v198\n"
    "v_mfma_f32_16x16x32_bf16 v[204:207], a[88:91], a[56:59], v[204:207]\n"
    "ds_read_b64_tr_b16 a[98:99], v229 offset:53760\n"
    "v_cvt_pk_bf16_f32 v134, v196, v197\n"
    "v_mfma_f32_16x16x32_bf16 v[204:207], a[84:87], a[52:55], v[204:207]\n"
    "v_exp_f32 v199, v199\n"
    "v_mfma_f32_16x16x32_bf16 v[204:207], a[80:83], a[48:51], v[204:207]\n"
    "s_waitcnt lgkmcnt(6)\n"
    "ds_read_b128 a[76:79], v228 offset:8960\n"
    "v_cvt_pk_bf16_f32 v135, v198, v199\n"
    "v_mfma_f32_16x16x32_bf16 v[48:51], a[104:107], v[160:163], v[48:51]\n"
    "s_waitcnt lgkmcnt(4)\n"
    "v_exp_f32 v200, v200\n"
    "v_mfma_f32_16x16x32_bf16 v[48:51], a[108:111], v[176:179], v[48:51]\n"
    "s_sub_u32 s11, s60, s8\n"
    "ds_read_b64_tr_b16 a[100:101], v229 offset:57856\n"
    "v_add_f32 v226, v226, v200\n"
    "v_mfma_f32_16x16x32_bf16 v[52:55], a[108:111], v[180:183], v[52:55]\n"
    "s_add_u32 s60, s60, 142\n"
    "v_exp_f32 v201, v201\n"
    "v_mfma_f32_16x16x32_bf16 v[52:55], a[104:107], v[164:167], v[52:55]\n"
    "s_cmp_gt_i32 s11, 2\n"
    "ds_read_b64_tr_b16 a[102:103], v229 offset:61952\n"
    "v_add_f32 v226, v226, v201\n"
    "v_mfma_f32_16x16x32_bf16 v[56:59], a[104:107], v[168:171], v[56:59]\n"
    "s_cbranch_scc1 Lpa11x%=\n"
    "Lpb11x%=:\n"
    "v_exp_f32 v202, v202\n"
    "v_mfma_f32_16x16x32_bf16 v[56:59], a[108:111], v[184:187], v[56:59]\n"
    "ds_read_b128 a[80:83], v228 offset:12288\n"
    "v_add_f32 v226, v226, v202\n"
    "v_mfma_f32_16x16x32_bf16 v[60:63], a[108:111], v[188:191], v[60:63]\n"
    "v_exp_f32 v203, v203\n"
    "v_mfma_f32_16x16x32_bf16 v[60:63], a[104:107], v[172:175], v[60:63]\n"
    "v_cvt_pk_bf16_f32 v138, v200, v201\n"
    "v_add_f32 v226, v226, v203\n"
    "v_mfma_f32_16x16x32_bf16 v[192:195], a[64:67], a[0:3], v[208:211]\n"
    "v_exp_f32 v204, v204\n"
    "v_mfma_f32_16x16x32_bf16 v[192:195], a[68:71], a[4:7], v[192:195]\n"
    "s_waitcnt lgkmcnt(6)\n"
    "ds_read_b64_tr_b16 a[104:105], v230 offset:49664\n"
    "v_cvt_pk_bf16_f32 v139, v202, v203\n"
    "v_mfma_f32_16x16x32_bf16 v[192:195], a[72:75], a[8:11], v[192:195]\n"
    "s_waitcnt lgkmcnt(4)\n"
    "v_exp_f32 v205, v205\n"
    "v_mfma_f32_16x16x32_bf16 v[192:195], a[76:79], a[12:15], v[192:195]\n"
    "buffer_load_dwordx4 v231, s[20:23], s59 offen offset:2048 nt lds\n"
    "v_mfma_f32_16x16x32_bf16 v[196:199], a[76:79], a[28:31], v[212:215]\n"
    "v_exp_f32 v206, v206\n"
    "v_mfma_f32_16x16x32_bf16 v[196:199], a[72:75], a[24:27], v[196:199]\n"
    "ds_read_b64_tr_b16 a[106:107], v230 offset:53760\n"
    "v_cvt_pk_bf16_f32 v142, v204, v205\n"
    "v_mfma_f32_16x16x32_bf16 v[196:199], a[68:71], a[20:23], v[196:199]\n"
    "v_exp_f32 v207, v207\n"
    "v_mfma_f32_16x16x32_bf16 v[196:199], a[64:67], a[16:19], v[196:199]\n"
    "ds_read_b128 a[84:87], v228 offset:12544\n"
    "v_cvt_pk_bf16_f32 v143, v206, v207\n"
    "v_mfma_f32_16x16x32_bf16 v[64:67], a[96:99], v[160:163], v[64:67]\n"
    "s_waitcnt lgkmcnt(4)\n"
    "v_exp_f32 v192, v192\n"
    "v_mfma_f32_16x16x32_bf16 v[64:67], a[100:103], v[176:179], v[64:67]\n"
    "ds_read_b64_tr_b16 a[108:109], v230 offset:57856\n"
    "v_add_f32 v224, v224, v192\n"
    "v_mfma_f32_16x16x32_bf16 v[68:71], a[100:103], v[180:183], v[68:71]\n"
    "v_exp_f32 v193, v193\n"
    "v_mfma_f32_16x16x32_bf16 v[68:71], a[96:99], v[164:167], v[68:71]\n"
    "ds_read_b64_tr_b16 a[110:111], v230 offset:61952\n"
    "v_add_f32 v224, v224, v193\n"
    "v_mfma_f32_16x16x32_bf16 v[72:75], a[96:99], v[168:171], v[72:75]\n"
    "v_exp_f32 v194, v194\n"
    "v_mfma_f32_16x16x32_bf16 v[72:75], a[100:103], v[184:187], v[72:75]\n"
    "ds_read_b128 a[88:91], v228 offset:12800\n"
    "v_add_f32 v224, v224, v194\n"
    "v_mfma_f32_16x16x32_bf16 v[76:79], a[100:103], v[188:191], v[76:79]\n"
    "v_exp_f32 v195, v195\n"
    "v_mfma_f32_16x16x32_bf16 v[76:79], a[96:99], v[172:175], v[76:79]\n"
    "v_cvt_pk_bf16_f32 v144, v192, v193\n"
    "v_add_f32 v224, v224, v195\n"
    "v_mfma_f32_16x16x32_bf16 v[200:203], a[64:67], a[32:35], v[216:219]\n"
    "v_exp_f32 v196, v196\n"
    "v_mfma_f32_16x16x32_bf16 v[200:203], a[68:71], a[36:39], v[200:203]\n"
    "ds_read_b64_tr_b16 a[96:97], v229 offset:49920\n"
    "v_cvt_pk_bf16_f32 v145, v194, v195\n"
    "v_mfma_f32_16x16x32_bf16 v[200:203], a[72:75], a[40:43], v[200:203]\n"
    "v_exp_f32 v197, v197\n"
    "v_mfma_f32_16x16x32_bf16 v[200:203], a[76:79], a[44:47], v[200:203]\n"
    "buffer_load_dwordx4 v232, s[20:23], s59 offen offset:3072 nt lds\n"
    "v_mfma_f32_16x16x32_bf16 v[204:207], a[76:79], a[60:63], v[220:223]\n"
    "v_exp_f32 v198, v198\n"
    "v_mfma_f32_16x16x32_bf16 v[204:207], a[72:75], a[56:59], v[204:207]\n"
    "s_add_u32 s59, s59, 16384\n"
    "ds_read_b64_tr_b16 a[98:99], v229 offset:54016\n"
    "v_cvt_pk_bf16_f32 v148, v196, v197\n"
    "v_mfma_f32_16x16x32_bf16 v[204:207], a[68:71], a[52:55], v[204:207]\n"
    "s_and_b32 s59, s59, 0x3ffff\n"
    "v_exp_f32 v199, v199\n"
    "v_mfma_f32_16x16x32_bf16 v[204:207], a[64:67], a[48:51], v[204:207]\n"
    "ds_read_b128 a[92:95], v228 offset:13056\n"
    "v_cvt_pk_bf16_f32 v149, v198, v199\n"
    "v_mfma_f32_16x16x32_bf16 a[120:123], a[112:115], v[172:175], a[120:123]\n"
    "v_mfma_f32_16x16x32_bf16 a[120:123], a[112:115], v[188:191], a[120:123]\n"
    "s_waitcnt lgkmcnt(7)\n"
    "v_mfma_f32_16x16x32_bf16 v[80:83], a[104:107], v[160:163], v[80:83]\n"
    "s_waitcnt lgkmcnt(4)\n"
    "v_exp_f32 v200, v200\n"
    "v_mfma_f32_16x16x32_bf16 v[80:83], a[108:111], v[176:179], v[80:83]\n"
    "s_add_u32 s57, s57, 16384\n"
    "ds_read_b64_tr_b16 a[100:101], v229 offset:58112\n"
    "v_add_f32 v226, v226, v200\n"
    "v_mfma_f32_16x16x32_bf16 v[84:87], a[108:111], v[180:183], v[84:87]\n"
    "s_bitset0_b32 s57, 17\n"
    "v_exp_f32 v201, v201\n"
    "v_mfma_f32_16x16x32_bf16 v[84:87], a[104:107], v[164:167], v[84:87]\n"
    "ds_read_b64_tr_b16 a[102:103], v229 offset:62208\n"
    "v_add_f32 v226, v226, v201\n"
    "v_mfma_f32_16x16x32_bf16 v[88:91], a[104:107], v[168:171], v[88:91]\n"
    "s_waitcnt vmcnt(8) lgkmcnt(2)\n"
    "v_exp_f32 v202, v202\n"
    "v_mfma_f32_16x16x32_bf16 v[88:91], a[108:111], v[184:187], v[88:91]\n"
    "s_barrier\n"
    "ds_read_b128 a[64:67], v228 offset:16384\n"
    "v_add_f32 v226, v226, v202\n"
    "v_mfma_f32_16x16x32_bf16 v[92:95], a[108:111], v[188:191], v[92:95]\n"
    "s_bitset1_b32 s57, 16\n"
    "v_exp_f32 v203, v203\n"
    "v_mfma_f32_16x16x32_bf16 v[92:95], a[104:107], v[172:175], v[92:95]\n"
    "v_cvt_pk_bf16_f32 v152, v200, v201\n"
    "v_add_f32 v226, v226, v203\n"
    "v_mfma_f32_16x16x32_bf16 v[192:195], a[80:83], a[0:3], v[208:211]\n"
    "s_mov_b32 m0, s56\n"
    "v_exp_f32 v204, v204\n"
    "v_mfma_f32_16x16x32_bf16 v[192:195], a[84:87], a[4:7], v[192:195]\n"
    "ds_read_b64_tr_b16 a[104:105], v230 offset:49920\n"
    "v_cvt_pk_bf16_f32 v153, v202, v203\n"
    "v_mfma_f32_16x16x32_bf16 v[192:195], a[88:91], a[8:11], v[192:195]\n"
    "v_exp_f32 v205, v205\n"
    "v_mfma_f32_16x16x32_bf16 v[192:195], a[92:95], a[12:15], v[192:195]\n"
    "buffer_load_dwordx4 v231, s[16:19], s58 offen offset:0 nt lds\n"
    "v_mfma_f32_16x16x32_bf16 v[196:199], a[92:95], a[28:31], v[212:215]\n"
    "v_exp_f32 v206, v206\n"
    "v_mfma_f32_16x16x32_bf16 v[196:199], a[88:91], a[24:27], v[196:199]\n"
    "ds_read_b64_tr_b16 a[106:107], v230 offset:54016\n"
    "v_cvt_pk_bf16_f32 v156, v204, v205\n"
    "v_mfma_f32_16x16x32_bf16 v[196:199], a[84:87], a[20:23], v[196:199]\n"
    "v_exp_f32 v207, v207\n"
    "v_mfma_f32_16x16x32_bf16 v[196:199], a[80:83], a[16:19], v[196:199]\n"
    "ds_read_b128 a[68:71], v228 offset:16640\n"
    "v_cvt_pk_bf16_f32 v157, v206, v207\n"
    "v_mfma_f32_16x16x32_bf16 v[96:99], a[96:99], v[160:163], v[96:99]\n"
    "s_waitcnt lgkmcnt(4)\n"
    "v_exp_f32 v192, v192\n"
    "v_mfma_f32_16x16x32_bf16 v[96:99], a[100:103], v[176:179], v[96:99]\n"
    "ds_read_b64_tr_b16 a[108:109], v230 offset:58112\n"
    "v_add_f32 v224, v224, v192\n"
    "v_mfma_f32_16x16x32_bf16 v[100:103], a[100:103], v[180:183], v[100:103]\n"
    "v_exp_f32 v193, v193\n"
    "v_mfma_f32_16x16x32_bf16 v[100:103], a[96:99], v[164:167], v[100:103]\n"
    "ds_read_b64_tr_b16 a[110:111], v230 offset:62208\n"
    "v_add_f32 v224, v224, v193\n"
    "v_mfma_f32_16x16x32_bf16 v[104:107], a[96:99], v[168:171], v[104:107]\n"
    "v_exp_f32 v194, v194\n"
    "v_mfma_f32_16x16x32_bf16 v[104:107], a[100:103], v[184:187], v[104:107]\n"
    "ds_read_b128 a[72:75], v228 offset:16896\n"
    "v_add_f32 v224, v224, v194\n"
    "v_mfma_f32_16x16x32_bf16 v[108:111], a[100:103], v[188:191], v[108:111]\n"
    "v_exp_f32 v195, v195\n"
    "v_mfma_f32_16x16x32_bf16 v[108:111], a[96:99], v[172:175], v[108:111]\n"
    "v_cvt_pk_bf16_f32 v146, v192, v193\n"
    "v_add_f32 v224, v224, v195\n"
    "v_mfma_f32_16x16x32_bf16 v[200:203], a[80:83], a[32:35], v[216:219]\n"
    "v_exp_f32 v196, v196\n"
    "v_mfma_f32_16x16x32_bf16 v[200:203], a[84:87], a[36:39], v[200:203]\n"
    "ds_read_b64_tr_b16 a[96:97], v229 offset:0\n"
    "v_cvt_pk_bf16_f32 v147, v194, v195\n"
    "v_mfma_f32_16x16x32_bf16 v[200:203], a[88:91], a[40:43], v[200:203]\n"
    "v_exp_f32 v197, v197\n"
    "v_mfma_f32_16x16x32_bf16 v[200:203], a[92:95], a[44:47], v[200:203]\n"
    "buffer_load_dwordx4 v232, s[16:19], s58 offen offset:1024 nt lds\n"
    "v_mfma_f32_16x16x32_bf16 v[204:207], a[92:95], a[60:63], v[220:223]\n"
    "v_exp_f32 v198, v198\n"
    "v_mfma_f32_16x16x32_bf16 v[204:207], a[88:91], a[56:59], v[204:207]\n"
    "ds_read_b64_tr_b16 a[98:99], v229 offset:4096\n"
    "v_cvt_pk_bf16_f32 v150, v196, v197\n"
    "v_mfma_f32_16x16x32_bf16 v[204:207], a[84:87], a[52:55], v[204:207]\n"
    "v_exp_f32 v199, v199\n"
    "v_mfma_f32_16x16x32_bf16 v[204:207], a[80:83], a[48:51], v[204:207]\n"
    "s_waitcnt lgkmcnt(6)\n"
    "ds_read_b128 a[76:79], v228 offset:17152\n"
    "v_cvt_pk_bf16_f32 v151, v198, v199\n"
    "v_mfma_f32_16x16x32_bf16 v[112:115], a[104:107], v[160:163], v[112:115]\n"
    "s_waitcnt lgkmcnt(4)\n"
    "v_exp_f32 v200, v200\n"
    "v_mfma_f32_16x16x32_bf16 v[112:115], a[108:111], v[176:179], v[112:115]\n"
    "ds_read_b64_tr_b16 a[100:101], v229 offset:8192\n"
    "v_add_f32 v226, v226, v200\n"
    "v_mfma_f32_16x16x32_bf16 v[116:119], a[108:111], v[180:183], v[116:119]\n"
    "v_exp_f32 v201, v201\n"
    "v_mfma_f32_16x16x32_bf16 v[116:119], a[104:107], v[164:167], v[116:119]\n"
    "ds_read_b64_tr_b16 a[102:103], v229 offset:12288\n"
    "v_add_f32 v226, v226, v201\n"
    "v_mfma_f32_16x16x32_bf16 v[120:123], a[104:107], v[168:171], v[120:123]\n"
    "v_exp_f32 v202, v202\n"
    "v_mfma_f32_16x16x32_bf16 v[120:123], a[108:111], v[184:187], v[120:123]\n"
    "ds_read_b128 a[80:83], v228 offset:20480\n"
    "v_add_f32 v226, v226, v202\n"
    "v_mfma_f32_16x16x32_bf16 v[124:127], a[108:111], v[188:191], v[124:127]\n"
    "v_exp_f32 v203, v203\n"
    "v_mfma_f32_16x16x32_bf16 v[124:127], a[104:107], v[172:175], v[124:127]\n"
    "v_cvt_pk_bf16_f32 v154, v200, v201\n"
    "v_add_f32 v226, v226, v203\n"
    "v_mfma_f32_16x16x32_bf16 v[192:195], a[64:67], a[0:3], v[208:211]\n"
    "s_mov_b32 m0, s56\n"
    "v_exp_f32 v204, v204\n"
    "v_mfma_f32_16x16x32_bf16 v[192:195], a[68:71], a[4:7], v[192:195]\n"
    "s_waitcnt lgkmcnt(6)\n"
    "ds_read_b64_tr_b16 a[104:105], v230 offset:0\n"
    "v_cvt_pk_bf16_f32 v155, v202, v203\n"
    "v_mfma_f32_16x16x32_bf16 v[192:195], a[72:75], a[8:11], v[192:195]\n"
    "s_waitcnt lgkmcnt(4)\n"
    "v_exp_f32 v205, v205\n"
    "v_mfma_f32_16x16x32_bf16 v[192:195], a[76:79], a[12:15], v[192:195]\n"
    "buffer_load_dwordx4 v231, s[16:19], s58 offen offset:2048 nt lds\n"
    "v_mfma_f32_16x16x32_bf16 v[196:199], a[76:79], a[28:31], v[212:215]\n"
    "v_exp_f32 v206, v206\n"
    "v_mfma_f32_16x16x32_bf16 v[196:199], a[72:75], a[24:27], v[196:199]\n"
    "ds_read_b64_tr_b16 a[106:107], v230 offset:4096\n"
    "v_cvt_pk_bf16_f32 v158, v204, v205\n"
    "v_mfma_f32_16x16x32_bf16 v[196:199], a[68:71], a[20:23], v[196:199]\n"
    "v_exp_f32 v207, v207\n"
    "v_mfma_f32_16x16x32_bf16 v[196:199], a[64:67], a[16:19], v[196:199]\n"
    "ds_read_b128 a[84:87], v228 offset:20736\n"
    "v_cvt_pk_bf16_f32 v159, v206, v207\n"
    "v_mfma_f32_16x16x32_bf16 v[0:3], a[96:99], v[128:131], v[0:3]\n"
    "s_waitcnt lgkmcnt(4)\n"
    "v_exp_f32 v192, v192\n"
    "v_mfma_f32_16x16x32_bf16 v[0:3], a[100:103], v[144:147], v[0:3]\n"
    "s_memrealtime s[8:9]\n"
    "ds_read_b64_tr_b16 a[108:109], v230 offset:8192\n"
    "v_add_f32 v224, v224, v192\n"
    "v_mfma_f32_16x16x32_bf16 v[4:7], a[100:103], v[148:151], v[4:7]\n"
    "v_exp_f32 v193, v193\n"
    "v_mfma_f32_16x16x32_bf16 v[4:7], a[96:99], v[132:135], v[4:7]\n"
    "ds_read_b64_tr_b16 a[110:111], v230 offset:12288\n"
    "v_add_f32 v224, v224, v193\n"
    "v_mfma_f32_16x16x32_bf16 v[8:11], a[96:99], v[136:139], v[8:11]\n"
    "v_exp_f32 v194, v194\n"
    "v_mfma_f32_16x16x32_bf16 v[8:11], a[100:103], v[152:155], v[8:11]\n"
    "ds_read_b128 a[88:91], v228 offset:20992\n"
    "v_add_f32 v224, v224, v194\n"
    "v_mfma_f32_16x16x32_bf16 v[12:15], a[100:103], v[156:159], v[12:15]\n"
    "v_exp_f32 v195, v195\n"
    "v_mfma_f32_16x16x32_bf16 v[12:15], a[96:99], v[140:143], v[12:15]\n"
    "v_cvt_pk_bf16_f32 v160, v192, v193\n"
    "v_add_f32 v224, v224, v195\n"
    "v_mfma_f32_16x16x32_bf16 v[200:203], a[64:67], a[32:35], v[216:219]\n"
    "v_exp_f32 v196, v196\n"
    "v_mfma_f32_16x16x32_bf16 v[200:203], a[68:71], a[36:39], v[200:203]\n"
    "ds_read_b64_tr_b16 a[96:97], v229 offset:256\n"
    "v_cvt_pk_bf16_f32 v161, v194, v195\n"
    "v_mfma_f32_16x16x32_bf16 v[200:203], a[72:75], a[40:43], v[200:203]\n"
    "v_exp_f32 v197, v197\n"
    "v_mfma_f32_16x16x32_bf16 v[200:203], a[76:79], a[44:47], v[200:203]\n"
    "buffer_load_dwordx4 v232, s[16:19], s58 offen offset:3072 nt lds\n"
    "v_mfma_f32_16x16x32_bf16 v[204:207], a[76:79], a[60:63], v[220:223]\n"
    "v_exp_f32 v198, v198\n"
    "v_mfma_f32_16x16x32_bf16 v[204:207], a[72:75], a[56:59], v[204:207]\n"
    "s_add_u32 s58, s58, 16384\n"
    "ds_read_b64_tr_b16 a[98:99], v229 offset:4352\n"
    "v_cvt_pk_bf16_f32 v164, v196, v197\n"
    "v_mfma_f32_16x16x32_bf16 v[204:207], a[68:71], a[52:55], v[204:207]\n"
    "s_and_b32 s58, s58, 0x3ffff\n"
    "v_exp_f32 v199, v199\n"
    "v_mfma_f32_16x16x32_bf16 v[204:207], a[64:67], a[48:51], v[204:207]\n"
    "s_waitcnt lgkmcnt(6)\n"
    "ds_read_b128 a[92:95], v228 offset:21248\n"
    "v_cvt_pk_bf16_f32 v165, v198, v199\n"
    "v_mfma_f32_16x16x32_bf16 v[16:19], a[104:107], v[128:131], v[16:19]\n"
    "s_waitcnt lgkmcnt(4)\n"
    "v_exp_f32 v200, v200\n"
    "v_mfma_f32_16x16x32_bf16 v[16:19], a[108:111], v[144:147], v[16:19]\n"
    "ds_read_b64_tr_b16 a[100:101], v229 offset:8448\n"
    "v_add_f32 v226, v226, v200\n"
    "v_mfma_f32_16x16x32_bf16 v[20:23], a[108:111], v[148:151], v[20:23]\n"
    "v_exp_f32 v201, v201\n"
    "v_mfma_f32_16x16x32_bf16 v[20:23], a[104:107], v[132:135], v[20:23]\n"
    "s_add_u32 s56, s56, 16384\n"
    "ds_read_b64_tr_b16 a[102:103], v229 offset:12544\n"
    "v_add_f32 v226, v226, v201\n"
    "v_mfma_f32_16x16x32_bf16 v[24:27], a[104:107], v[136:139], v[24:27]\n"
    "s_and_b32 s56, s56, 0xffff\n"
    "v_exp_f32 v202, v202\n"
    "v_mfma_f32_16x16x32_bf16 v[24:27], a[108:111], v[152:155], v[24:27]\n"
    "ds_read_b128 a[64:67], v228 offset:24576\n"
    "v_add_f32 v226, v226, v202\n"
    "v_mfma_f32_16x16x32_bf16 v[28:31], a[108:111], v[156:159], v[28:31]\n"
    "v_exp_f32 v203, v203\n"
    "v_mfma_f32_16x16x32_bf16 v[28:31], a[104:107], v[140:143], v[28:31]\n"
    "v_cvt_pk_bf16_f32 v168, v200, v201\n"
    "v_add_f32 v226, v226, v203\n"
    "v_mfma_f32_16x16x32_bf16 v[192:195], a[80:83], a[0:3], v[208:211]\n"
    "s_mov_b32 m0, s57\n"
    "v_exp_f32 v204, v204\n"
    "v_mfma_f32_16x16x32_bf16 v[192:195], a[84:87], a[4:7], v[192:195]\n"
    "s_waitcnt lgkmcnt(6)\n"
    "ds_read_b64_tr_b16 a[104:105], v230 offset:256\n"
    "v_cvt_pk_bf16_f32 v169, v202, v203\n"
    "v_mfma_f32_16x16x32_bf16 v[192:195], a[88:91], a[8:11], v[192:195]\n"
    "s_waitcnt lgkmcnt(4)\n"
    "v_exp_f32 v205, v205\n"
    "v_mfma_f32_16x16x32_bf16 v[192:195], a[92:95], a[12:15], v[192:195]\n"
    "buffer_load_dwordx4 v231, s[20:23], s59 offen offset:0 nt lds\n"
    "v_mfma_f32_16x16x32_bf16 v[196:199], a[92:95], a[28:31], v[212:215]\n"
    "v_exp_f32 v206, v206\n"
    "v_mfma_f32_16x16x32_bf16 v[196:199], a[88:91], a[24:27], v[196:199]\n"
    "ds_read_b64_tr_b16 a[106:107], v230 offset:4352\n"
    "v_cvt_pk_bf16_f32 v172, v204, v205\n"
    "v_mfma_f32_16x16x32_bf16 v[196:199], a[84:87], a[20:23], v[196:199]\n"
    "v_exp_f32 v207, v207\n"
    "v_mfma_f32_16x16x32_bf16 v[196:199], a[80:83], a[16:19], v[196:199]\n"
    "ds_read_b128 a[68:71], v228 offset:24832\n"
    "v_cvt_pk_bf16_f32 v173, v206, v207\n"
    "v_mfma_f32_16x16x32_bf16 a[116:119], a[112:115], v[132:135], a[116:119]\n"
    "v_mfma_f32_16x16x32_bf16 a[116:119], a[112:115], v[148:151], a[116:119]\n"
    "v_mfma_f32_16x16x32_bf16 v[32:35], a[96:99], v[128:131], v[32:35]\n"
    "s_waitcnt lgkmcnt(4)\n"
    "v_exp_f32 v192, v192\n"
    "v_mfma_f32_16x16x32_bf16 v[32:35], a[100:103], v[144:147], v[32:35]\n"
    "ds_read_b64_tr_b16 a[108:109], v230 offset:8448\n"
    "v_add_f32 v224, v224, v192\n"
    "v_mfma_f32_16x16x32_bf16 v[36:39], a[100:103], v[148:151], v[36:39]\n"
    "v_exp_f32 v193, v193\n"
    "v_mfma_f32_16x16x32_bf16 v[36:39], a[96:99], v[132:135], v[36:39]\n"
    "ds_read_b64_tr_b16 a[110:111], v230 offset:12544\n"
    "v_add_f32 v224, v224, v193\n"
    "v_mfma_f32_16x16x32_bf16 v[40:43], a[96:99], v[136:139], v[40:43]\n"
    "v_exp_f32 v194, v194\n"
    "v_mfma_f32_16x16x32_bf16 v[40:43], a[100:103], v[152:155], v[40:43]\n"
    "ds_read_b128 a[72:75], v228 offset:25088\n"
    "v_add_f32 v224, v224, v194\n"
    "v_mfma_f32_16x16x32_bf16 v[44:47], a[100:103], v[156:159], v[44:47]\n"
    "v_exp_f32 v195, v195\n"
    "v_mfma_f32_16x16x32_bf16 v[44:47], a[96:99], v[140:143], v[44:47]\n"
    "v_cvt_pk_bf16_f32 v162, v192, v193\n"
    "v_add_f32 v224, v224, v195\n"
    "v_mfma_f32_16x16x32_bf16 v[200:203], a[80:83], a[32:35], v[216:219]\n"
    "v_exp_f32 v196, v196\n"
    "v_mfma_f32_16x16x32_bf16 v[200:203], a[84:87], a[36:39], v[200:203]\n"
    "ds_read_b64_tr_b16 a[96:97], v229 offset:512\n"
    "v_cvt_pk_bf16_f32 v163, v194, v195\n"
    "v_mfma_f32_16x16x32_bf16 v[200:203], a[88:91], a[40:43], v[200:203]\n"
    "v_exp_f32 v197, v197\n"
    "v_mfma_f32_16x16x32_bf16 v[200:203], a[92:95], a[44:47], v[200:203]\n"
    "buffer_load_dwordx4 v232, s[20:23], s59 offen offset:1024 nt lds\n"
    "v_mfma_f32_16x16x32_bf16 v[204:207], a[92:95], a[60:63], v[220:223]\n"
    "v_exp_f32 v198, v198\n"
    "v_mfma_f32_16x16x32_bf16 v[204:207], a[88:91], a[56:59], v[204:207]\n"
    "ds_read_b64_tr_b16 a[98:99], v229 offset:4608\n"
    "v_cvt_pk_bf16_f32 v166, v196, v197\n"
    "v_mfma_f32_16x16x32_bf16 v[204:207], a[84:87], a[52:55], v[204:207]\n"
    "v_exp_f32 v199, v199\n"
    "v_mfma_f32_16x16x32_bf16 v[204:207], a[80:83], a[48:51], v[204:207]\n"
    "s_waitcnt lgkmcnt(6)\n"
    "ds_read_b128 a[76:79], v228 offset:25344\n"
    "v_cvt_pk_bf16_f32 v167, v198, v199\n"
    "v_mfma_f32_16x16x32_bf16 v[48:51], a[104:107], v[128:131], v[48:51]\n"
    "s_waitcnt lgkmcnt(4)\n"
    "v_exp_f32 v200, v200\n"
    "v_mfma_f32_16x16x32_bf16 v[48:51], a[108:111], v[144:147], v[48:51]\n"
    "s_sub_u32 s11, s60, s8\n"
    "ds_read_b64_tr_b16 a[100:101], v229 offset:8704\n"
    "v_add_f32 v226, v226, v200\n"
    "v_mfma_f32_16x16x32_bf16 v[52:55], a[108:111], v[148:151], v[52:55]\n"
    "s_add_u32 s60, s60, 142\n"
    "v_exp_f32 v201, v201\n"
    "v_mfma_f32_16x16x32_bf16 v[52:55], a[104:107], v[132:135], v[52:55]\n"
    "s_cmp_gt_i32 s11, 2\n"
    "ds_read_b64_tr_b16 a[102:103], v229 offset:12800\n"
    "v_add_f32 v226, v226, v201\n"
    "v_mfma_f32_16x16x32_bf16 v[56:59], a[104:107], v[136:139], v[56:59]\n"
    "s_cbranch_scc1 Lpa12x%=\n"
    "Lpb12x%=:\n"
    "v_exp_f32 v202, v202\n"
    "v_mfma_f32_16x16x32_bf16 v[56:59], a[108:111], v[152:155], v[56:59]\n"
    "ds_read_b128 a[80:83], v228 offset:28672\n"
    "v_add_f32 v226, v226, v202\n"
    "v_mfma_f32_16x16x32_bf16 v[60:63], a[108:111], v[156:159], v[60:63]\n"
    "v_exp_f32 v203, v203\n"
    "v_mfma_f32_16x16x32_bf16 v[60:63], a[104:107], v[140:143], v[60:63]\n"
    "v_cvt_pk_bf16_f32 v170, v200, v201\n"
    "v_add_f32 v226, v226, v203\n"
    "v_mfma_f32_16x16x32_bf16 v[192:195], a[64:67], a[0:3], v[208:211]\n"
    "v_exp_f32 v204, v204\n"
    "v_mfma_f32_16x16x32_bf16 v[192:195], a[68:71], a[4:7], v[192:195]\n"
    "s_waitcnt lgkmcnt(6)\n"
    "ds_read_b64_tr_b16 a[104:105], v230 offset:512\n"
    "v_cvt_pk_bf16_f32 v171, v202, v203\n"
    "v_mfma_f32_16x16x32_bf16 v[192:195], a[72:75], a[8:11], v[192:195]\n"
    "s_waitcnt lgkmcnt(4)\n"
    "v_exp_f32 v205, v205\n"
    "v_mfma_f32_16x16x32_bf16 v[192:195], a[76:79], a[12:15], v[192:195]\n"
    "buffer_load_dwordx4 v231, s[20:23], s59 offen offset:2048 nt lds\n"
    "v_mfma_f32_16x16x32_bf16 v[196:199], a[76:79], a[28:31], v[212:215]\n"
    "v_exp_f32 v206, v206\n"
    "v_mfma_f32_16x16x32_bf16 v[196:199], a[72:75], a[24:27], v[196:199]\n"
    "ds_read_b64_tr_b16 a[106:107], v230 offset:4608\n"
    "v_cvt_pk_bf16_f32 v174, v204, v205\n"
    "v_mfma_f32_16x16x32_bf16 v[196:199], a[68:71], a[20:23], v[196:199]\n"
    "v_exp_f32 v207, v207\n"
    "v_mfma_f32_16x16x32_bf16 v[196:199], a[64:67], a[16:19], v[196:199]\n"
    "ds_read_b128 a[84:87], v228 offset:28928\n"
    "v_cvt_pk_bf16_f32 v175, v206, v207\n"
    "v_mfma_f32_16x16x32_bf16 v[64:67], a[96:99], v[128:131], v[64:67]\n"
    "s_waitcnt lgkmcnt(4)\n"
    "v_exp_f32 v192, v192\n"
    "v_mfma_f32_16x16x32_bf16 v[64:67], a[100:103], v[144:147], v[64:67]\n"
    "ds_read_b64_tr_b16 a[108:109], v230 offset:8704\n"
    "v_add_f32 v224, v224, v192\n"
    "v_mfma_f32_16x16x32_bf16 v[68:71], a[100:103], v[148:151], v[68:71]\n"
    "v_exp_f32 v193, v193\n"
    "v_mfma_f32_16x16x32_bf16 v[68:71], a[96:99], v[132:135], v[68:71]\n"
    "ds_read_b64_tr_b16 a[110:111], v230 offset:12800\n"
    "v_add_f32 v224, v224, v193\n"
    "v_mfma_f32_16x16x32_bf16 v[72:75], a[96:99], v[136:139], v[72:75]\n"
    "v_exp_f32 v194, v194\n"
    "v_mfma_f32_16x16x32_bf16 v[72:75], a[100:103], v[152:155], v[72:75]\n"
    "ds_read_b128 a[88:91], v228 offset:29184\n"
    "v_add_f32 v224, v224, v194\n"
    "v_mfma_f32_16x16x32_bf16 v[76:79], a[100:103], v[156:159], v[76:79]\n"
    "v_exp_f32 v195, v195\n"
    "v_mfma_f32_16x16x32_bf16 v[76:79], a[96:99], v[140:143], v[76:79]\n"
    "v_cvt_pk_bf16_f32 v176, v192, v193\n"
    "v_add_f32 v224, v224, v195\n"
    "v_mfma_f32_16x16x32_bf16 v[200:203], a[64:67], a[32:35], v[216:219]\n"
    "v_exp_f32 v196, v196\n"
    "v_mfma_f32_16x16x32_bf16 v[200:203], a[68:71], a[36:39], v[200:203]\n"
    "ds_read_b64_tr_b16 a[96:97], v229 offset:768\n"
    "v_cvt_pk_bf16_f32 v177, v194, v195\n"
    "v_mfma_f32_16x16x32_bf16 v[200:203], a[72:75], a[40:43], v[200:203]\n"
    "v_exp_f32 v197, v197\n"
    "v_mfma_f32_16x16x32_bf16 v[200:203], a[76:79], a[44:47], v[200:203]\n"
    "buffer_load_dwordx4 v232, s[20:23], s59 offen offset:3072 nt lds\n"
    "v_mfma_f32_16x16x32_bf16 v[204:207], a[76:79], a[60:63], v[220:223]\n"
    "v_exp_f32 v198, v198\n"
    "v_mfma_f32_16x16x32_bf16 v[204:207], a[72:75], a[56:59], v[204:207]\n"
    "s_add_u32 s59, s59, 16384\n"
    "ds_read_b64_tr_b16 a[98:99], v229 offset:4864\n"
    "v_cvt_pk_bf16_f32 v180, v196, v197\n"
    "v_mfma_f32_16x16x32_bf16 v[204:207], a[68:71], a[52:55], v[204:207]\n"
    "s_and_b32 s59, s59, 0x3ffff\n"
    "v_exp_f32 v199, v199\n"
    "v_mfma_f32_16x16x32_bf16 v[204:207], a[64:67], a[48:51], v[204:207]\n"
    "ds_read_b128 a[92:95], v228 offset:29440\n"
    "v_cvt_pk_bf16_f32 v181, v198, v199\n"
    "v_mfma_f32_16x16x32_bf16 a[120:123], a[112:115], v[140:143], a[120:123]\n"
    "v_mfma_f32_16x16x32_bf16 a[120:123], a[112:115], v[156:159], a[120:123]\n"
    "s_waitcnt lgkmcnt(7)\n"
    "v_mfma_f32_16x16x32_bf16 v[80:83], a[104:107], v[128:131], v[80:83]\n"
    "s_waitcnt lgkmcnt(4)\n"
    "v_exp_f32 v200, v200\n"
    "v_mfma_f32_16x16x32_bf16 v[80:83], a[108:111], v[144:147], v[80:83]\n"
    "s_add_u32 s57, s57, 16384\n"
    "ds_read_b64_tr_b16 a[100:101], v229 offset:8960\n"
    "v_add_f32 v226, v226, v200\n"
    "v_mfma_f32_16x16x32_bf16 v[84:87], a[108:111], v[148:151], v[84:87]\n"
    "s_bitset0_b32 s57, 17\n"
    "v_exp_f32 v201, v201\n"
    "v_mfma_f32_16x16x32_bf16 v[84:87], a[104:107], v[132:135], v[84:87]\n"
    "ds_read_b64_tr_b16 a[102:103], v229 offset:13056\n"
    "v_add_f32 v226, v226, v201\n"
    "v_mfma_f32_16x16x32_bf16 v[88:91], a[104:107], v[136:139], v[88:91]\n"
    "s_waitcnt vmcnt(8) lgkmcnt(2)\n"
    "v_exp_f32 v202, v202\n"
    "v_mfma_f32_16x16x32_bf16 v[88:91], a[108:111], v[152:155], v[88:91]\n"
    "s_barrier\n"
    "ds_read_b128 a[64:67], v228 offset:32768\n"
    "v_add_f32 v226, v226, v202\n"
    "v_mfma_f32_16x16x32_bf16 v[92:95], a[108:111], v[156:159], v[92:95]\n"
    "s_bitset1_b32 s57, 16\n"
    "v_exp_f32 v203, v203\n"
    "v_mfma_f32_16x16x32_bf16 v[92:95], a[104:107], v[140:143], v[92:95]\n"
    "v_cvt_pk_bf16_f32 v184, v200, v201\n"
    "v_add_f32 v226, v226, v203\n"
    "v_mfma_f32_16x16x32_bf16 v[192:195], a[80:83], a[0:3], v[208:211]\n"
    "s_mov_b32 m0, s57\n"
    "v_exp_f32 v204, v204\n"
    "v_mfma_f32_16x16x32_bf16 v[192:195], a[84:87], a[4:7], v[192:195]\n"
    "ds_read_b64_tr_b16 a[104:105], v230 offset:768\n"
    "v_cvt_pk_bf16_f32 v185, v202, v203\n"
    "v_mfma_f32_16x16x32_bf16 v[192:195], a[88:91], a[8:11], v[192:195]\n"
    "v_exp_f32 v205, v205\n"
    "v_mfma_f32_16x16x32_bf16 v[192:195], a[92:95], a[12:15], v[192:195]\n"
    "buffer_load_dwordx4 v231, s[20:23], s59 offen offset:0 nt lds\n"
    "v_mfma_f32_16x16x32_bf16 v[196:199], a[92:95], a[28:31], v[212:215]\n"
    "v_exp_f32 v206, v206\n"
    "v_mfma_f32_16x16x32_bf16 v[196:199], a[88:91], a[24:27], v[196:199]\n"
    "ds_read_b64_tr_b16 a[106:107], v230 offset:4864\n"
    "v_cvt_pk_bf16_f32 v188, v204, v205\n"
    "v_mfma_f32_16x16x32_bf16 v[196:199], a[84:87], a[20:23], v[196:199]\n"
    "v_exp_f32 v207, v207\n"
    "v_mfma_f32_16x16x32_bf16 v[196:199], a[80:83], a[16:19], v[196:199]\n"
    "ds_read_b128 a[68:71], v228 offset:33024\n"
    "v_cvt_pk_bf16_f32 v189, v206, v207\n"
    "v_mfma_f32_16x16x32_bf16 v[96:99], a[96:99], v[128:131], v[96:99]\n"
    "s_waitcnt lgkmcnt(4)\n"
    "v_exp_f32 v192, v192\n"
    "v_mfma_f32_16x16x32_bf16 v[96:99], a[100:103], v[144:147], v[96:99]\n"
    "Lpg11%=:\n"
    "s_cbranch_execz Lwb1%=\n"
    "ds_read_b64_tr_b16 a[108:109], v230 offset:8960\n"
    "v_add_f32 v224, v224, v192\n"
    "v_mfma_f32_16x16x32_bf16 v[100:103], a[100:103], v[148:151], v[100:103]\n"
    "v_exp_f32 v193, v193\n"
    "v_mfma_f32_16x16x32_bf16 v[100:103], a[96:99], v[132:135], v[100:103]\n"
    "ds_read_b64_tr_b16 a[110:111], v230 offset:13056\n"
    "v_add_f32 v224, v224, v193\n"
    "v_mfma_f32_16x16x32_bf16 v[104:107], a[96:99], v[136:139], v[104:107]\n"
    "v_exp_f32 v194, v194\n"
    "v_mfma_f32_16x16x32_bf16 v[104:107], a[100:103], v[152:155], v[104:107]\n"
    "ds_read_b128 a[72:75], v228 offset:33280\n"
    "v_add_f32 v224, v224, v194\n"
    "v_mfma_f32_16x16x32_bf16 v[108:111], a[100:103], v[156:159], v[108:111]\n"
    "v_exp_f32 v195, v195\n"
    "v_mfma_f32_16x16x32_bf16 v[108:111], a[96:99], v[140:143], v[108:111]\n"
    "v_cvt_pk_bf16_f32 v178, v192, v193\n"
    "v_add_f32 v224, v224, v195\n"
    "v_mfma_f32_16x16x32_bf16 v[200:203], a[80:83], a[32:35], v[216:219]\n"
    "v_exp_f32 v196, v196\n"
    "v_mfma_f32_16x16x32_bf16 v[200:203], a[84:87], a[36:39], v[200:203]\n"
    "ds_read_b64_tr_b16 a[96:97], v229 offset:16384\n"
    "v_cvt_pk_bf16_f32 v179, v194, v195\n"
    "v_mfma_f32_16x16x32_bf16 v[200:203], a[88:91], a[40:43], v[200:203]\n"
    "v_exp_f32 v197, v197\n"
    "v_mfma_f32_16x16x32_bf16 v[200:203], a[92:95], a[44:47], v[200:203]\n"
    "buffer_load_dwordx4 v232, s[20:23], s59 offen offset:1024 nt lds\n"
    "v_mfma_f32_16x16x32_bf16 v[204:207], a[92:95], a[60:63], v[220:223]\n"
    "v_exp_f32 v198, v198\n"
    "v_mfma_f32_16x16x32_bf16 v[204:207], a[88:91], a[56:59], v[204:207]\n"
    "ds_read_b64_tr_b16 a[98:99], v229 offset:20480\n"
    "v_cvt_pk_bf16_f32 v182, v196, v197\n"
    "v_mfma_f32_16x16x32_bf16 v[204:207], a[84:87], a[52:55], v[204:207]\n"
    "v_exp_f32 v199, v199\n"
    "v_mfma_f32_16x16x32_bf16 v[204:207], a[80:83], a[48:51], v[204:207]\n"
    "s_waitcnt lgkmcnt(6)\n"
    "ds_read_b128 a[76:79], v228 offset:33536\n"
    "v_cvt_pk_bf16_f32 v183, v198, v199\n"
    "v_mfma_f32_16x16x32_bf16 v[112:115], a[104:107], v[128:131], v[112:115]\n"
    "s_waitcnt lgkmcnt(4)\n"
    "v_exp_f32 v200, v200\n"
    "v_mfma_f32_16x16x32_bf16 v[112:115], a[108:111], v[144:147], v[112:115]\n"
    "ds_read_b64_tr_b16 a[100:101], v229 offset:24576\n"
    "v_add_f32 v226, v226, v200\n"
    "v_mfma_f32_16x16x32_bf16 v[116:119], a[108:111], v[148:151], v[116:119]\n"
    "v_exp_f32 v201, v201\n"
    "v_mfma_f32_16x16x32_bf16 v[116:119], a[104:107], v[132:135], v[116:119]\n"
    "ds_read_b64_tr_b16 a[102:103], v229 offset:28672\n"
    "v_add_f32 v226, v226, v201\n"
    "v_mfma_f32_16x16x32_bf16 v[120:123], a[104:107], v[136:139], v[120:123]\n"
    "v_exp_f32 v202, v202\n"
    "v_mfma_f32_16x16x32_bf16 v[120:123], a[108:111], v[152:155], v[120:123]\n"
    "ds_read_b128 a[80:83], v228 offset:36864\n"
    "v_add_f32 v226, v226, v202\n"
    "v_mfma_f32_16x16x32_bf16 v[124:127], a[108:111], v[156:159], v[124:127]\n"
    "v_exp_f32 v203, v203\n"
    "v_mfma_f32_16x16x32_bf16 v[124:127], a[104:107], v[140:143], v[124:127]\n"
    "v_cvt_pk_bf16_f32 v186, v200, v201\n"
    "v_add_f32 v226, v226, v203\n"
    "v_mfma_f32_16x16x32_bf16 v[192:195], a[64:67], a[0:3], v[208:211]\n"
    "s_lshl_b32 s54, s44, 2\n"
    "ds_read_b128 a[84:87], v228 offset:37120\n"
    "ds_read_b128 a[88:91], v228 offset:37376\n"
    "v_mfma_f32_16x16x32_bf16 v[192:195], a[68:71], a[4:7], v[192:195]\n"
    "s_add_u32 s54, s54, 65536\n"
    "ds_read_b128 a[92:95], v228 offset:37632\n"
    "ds_read_b64_tr_b16 a[104:105], v230 offset:16384\n"
    "s_waitcnt lgkmcnt(10)\n"
    "v_mfma_f32_16x16x32_bf16 v[192:195], a[72:75], a[8:11], v[192:195]\n"
    "s_waitcnt lgkmcnt(7)\n"
    "ds_read_b64_tr_b16 a[106:107], v230 offset:20480\n"
    "ds_read_b64_tr_b16 a[108:109], v230 offset:24576\n"
    "v_mfma_f32_16x16x32_bf16 v[192:195], a[76:79], a[12:15], v[192:195]\n"
    "s_mov_b32 m0, s57\n"
    "ds_read_b64_tr_b16 a[110:111], v230 offset:28672\n"
    "buffer_load_dwordx4 v231, s[20:23], s59 offen offset:2048 nt lds\n"
    "v_mfma_f32_16x16x32_bf16 v[0:3], a[96:99], v[160:163], v[0:3]\n"
    "s_waitcnt lgkmcnt(8)\n"
    "v_cvt_pk_bf16_f32 v187, v202, v203\n"
    "v_exp_f32 v204, v204\n"
    "v_mfma_f32_16x16x32_bf16 v[0:3], a[100:103], v[176:179], v[0:3]\n"
    "v_exp_f32 v205, v205\n"
    "v_mfma_f32_16x16x32_bf16 v[4:7], a[96:99], v[164:167], v[4:7]\n"
    "ds_read_b64_tr_b16 a[96:97], v229 offset:16640\n"
    "ds_read_b64_tr_b16 a[98:99], v229 offset:20736\n"
    "v_mfma_f32_16x16x32_bf16 v[4:7], a[100:103], v[180:183], v[4:7]\n"
    "ds_read_b64_tr_b16 a[100:101], v229 offset:24832\n"
    "ds_read_b64_tr_b16 a[102:103], v229 offset:28928\n"
    "v_mfma_f32_16x16x32_bf16 v[196:199], a[64:67], a[16:19], v[212:215]\n"
    "ds_read_b128 a[64:67], v228 offset:40960\n"
    "v_exp_f32 v206, v206\n"
    "v_mfma_f32_16x16x32_bf16 v[196:199], a[68:71], a[20:23], v[196:199]\n"
    "ds_read_b128 a[68:71], v228 offset:41216\n"
    "v_exp_f32 v207, v207\n"
    "v_mfma_f32_16x16x32_bf16 v[196:199], a[72:75], a[24:27], v[196:199]\n"
    "ds_read_b128 a[72:75], v228 offset:41472\n"
    "v_cvt_pk_bf16_f32 v190, v204, v205\n"
    "v_mfma_f32_16x16x32_bf16 v[196:199], a[76:79], a[28:31], v[196:199]\n"
    "s_waitcnt lgkmcnt(9)\n"
    "ds_read_b128 a[76:79], v228 offset:41728\n"
    "v_cvt_pk_bf16_f32 v191, v206, v207\n"
    "v_mfma_f32_16x16x32_bf16 v[16:19], a[104:107], v[160:163], v[16:19]\n"
    "s_waitcnt lgkmcnt(8)\n"
    "v_add_u32 v251, s54, v233\n"
    "v_add_u32 v252, 65536, v233\n"
    "v_mfma_f32_16x16x32_bf16 v[16:19], a[108:111], v[176:179], v[16:19]\n"
    "v_exp_f32 v192, v192\n"
    "v_mfma_f32_16x16x32_bf16 v[20:23], a[104:107], v[164:167], v[20:23]\n"
    "ds_read_b64_tr_b16 a[104:105], v230 offset:16640\n"
    "ds_read_b64_tr_b16 a[106:107], v230 offset:20736\n"
    "v_mfma_f32_16x16x32_bf16 v[20:23], a[108:111], v[180:183], v[20:23]\n"
    "ds_read_b64_tr_b16 a[108:109], v230 offset:24832\n"
    "ds_read_b64_tr_b16 a[110:111], v230 offset:28928\n"
    "v_mfma_f32_16x16x32_bf16 v[200:203], a[80:83], a[0:3], v[208:211]\n"
    "v_exp_f32 v193, v193\n"
    "v_mfma_f32_16x16x32_bf16 v[200:203], a[84:87], a[4:7], v[200:203]\n"
    "v_exp_f32 v194, v194\n"
    "v_mfma_f32_16x16x32_bf16 v[200:203], a[88:91], a[8:11], v[200:203]\n"
    "v_cvt_pk_bf16_f32 v128, v192, v193\n"
    "v_exp_f32 v195, v195\n"
    "v_mfma_f32_16x16x32_bf16 v[200:203], a[92:95], a[12:15], v[200:203]\n"
    "s_waitcnt lgkmcnt(10)\n"
    "buffer_load_dwordx4 v232, s[20:23], s59 offen offset:3072 nt lds\n"
    "v_mfma_f32_16x16x32_bf16 v[32:35], a[96:99], v[160:163], v[32:35]\n"
    "s_waitcnt lgkmcnt(8)\n"
    "v_cvt_pk_bf16_f32 v129, v194, v195\n"
    "v_exp_f32 v196, v196\n"
    "v_mfma_f32_16x16x32_bf16 v[32:35], a[100:103], v[176:179], v[32:35]\n"
    "v_exp_f32 v197, v197\n"
    "v_mfma_f32_16x16x32_bf16 v[36:39], a[96:99], v[164:167], v[36:39]\n"
    "ds_read_b64_tr_b16 a[96:97], v229 offset:16896\n"
    "ds_read_b64_tr_b16 a[98:99], v229 offset:20992\n"
    "v_mfma_f32_16x16x32_bf16 v[36:39], a[100:103], v[180:183], v[36:39]\n"
    "ds_read_b64_tr_b16 a[100:101], v229 offset:25088\n"
    "ds_read_b64_tr_b16 a[102:103], v229 offset:29184\n"
    "v_mfma_f32_16x16x32_bf16 v[204:207], a[80:83], a[16:19], v[212:215]\n"
    "ds_read_b128 a[80:83], v228 offset:45056\n"
    "v_exp_f32 v198, v198\n"
    "v_mfma_f32_16x16x32_bf16 v[204:207], a[84:87], a[20:23], v[204:207]\n"
    "ds_read_b128 a[84:87], v228 offset:45312\n"
    "v_cvt_pk_bf16_f32 v132, v196, v197\n"
    "v_mfma_f32_16x16x32_bf16 v[204:207], a[88:91], a[24:27], v[204:207]\n"
    "s_waitcnt lgkmcnt(8)\n"
    "ds_read_b128 a[88:91], v228 offset:45568\n"
    "v_exp_f32 v199, v199\n"
    "v_mfma_f32_16x16x32_bf16 v[204:207], a[92:95], a[28:31], v[204:207]\n"
    "s_add_u32 m0, s51, 0\n"
    "ds_read_b128 a[92:95], v228 offset:45824\n"
    "v_cvt_pk_bf16_f32 v133, v198, v199\n"
    "v_mfma_f32_16x16x32_bf16 v[48:51], a[104:107], v[160:163], v[48:51]\n"
    "s_xor_b32 s53, s88, 163840\n"
    "buffer_load_dwordx4 v234, s[24:27], s53 offen offset:0  lds\n"
    "s_waitcnt lgkmcnt(8)\n"
    "v_mfma_f32_16x16x32_bf16 v[48:51], a[108:111], v[176:179], v[48:51]\n"
    "v_exp_f32 v200, v200\n"
    "v_mfma_f32_16x16x32_bf16 v[52:55], a[104:107], v[164:167], v[52:55]\n"
    "ds_read_b64_tr_b16 a[104:105], v230 offset:16896\n"
    "ds_read_b64_tr_b16 a[106:107], v230 offset:20992\n"
    "v_mfma_f32_16x16x32_bf16 v[52:55], a[108:111], v[180:183], v[52:55]\n"
    "ds_read_b64_tr_b16 a[108:109], v230 offset:25088\n"
    "ds_read_b64_tr_b16 a[110:111], v230 offset:29184\n"
    "v_mfma_f32_16x16x32_bf16 a[116:119], a[112:115], v[164:167], a[116:119]\n"
    "v_exp_f32 v201, v201\n"
    "v_mfma_f32_16x16x32_bf16 a[116:119], a[112:115], v[180:183], a[116:119]\n"
    "v_exp_f32 v202, v202\n"
    "v_mfma_f32_16x16x32_bf16 v[192:195], a[64:67], a[0:3], v[208:211]\n"
    "v_cvt_pk_bf16_f32 v130, v200, v201\n"
    "v_exp_f32 v203, v203\n"
    "v_mfma_f32_16x16x32_bf16 v[192:195], a[68:71], a[4:7], v[192:195]\n"
    "v_cvt_pk_bf16_f32 v131, v202, v203\n"
    "v_exp_f32 v204, v204\n"
    "v_mfma_f32_16x16x32_bf16 v[192:195], a[72:75], a[8:11], v[192:195]\n"
    "v_exp_f32 v205, v205\n"
    "v_mfma_f32_16x16x32_bf16 v[192:195], a[76:79], a[12:15], v[192:195]\n"
    "s_waitcnt lgkmcnt(10)\n"
    "buffer_load_dwordx4 v235, s[24:27], s53 offen offset:1024  lds\n"
    "v_mfma_f32_16x16x32_bf16 v[64:67], a[96:99], v[160:163], v[64:67]\n"
    "s_waitcnt lgkmcnt(8)\n"
    "v_exp_f32 v206, v206\n"
    "v_mfma_f32_16x16x32_bf16 v[64:67], a[100:103], v[176:179], v[64:67]\n"
    "v_cvt_pk_bf16_f32 v134, v204, v205\n"
    "v_exp_f32 v207, v207\n"
    "v_mfma_f32_16x16x32_bf16 v[68:71], a[96:99], v[164:167], v[68:71]\n"
    "ds_read_b64_tr_b16 a[96:97], v229 offset:17152\n"
    "ds_read_b64_tr_b16 a[98:99], v229 offset:21248\n"
    "v_mfma_f32_16x16x32_bf16 v[68:71], a[100:103], v[180:183], v[68:71]\n"
    "ds_read_b64_tr_b16 a[100:101], v229 offset:25344\n"
    "ds_read_b64_tr_b16 a[102:103], v229 offset:29440\n"
    "v_mfma_f32_16x16x32_bf16 v[196:199], a[64:67], a[16:19], v[212:215]\n"
    "v_cvt_pk_bf16_f32 v135, v206, v207\n"
    "v_exp_f32 v192, v192\n"
    "v_mfma_f32_16x16x32_bf16 v[196:199], a[68:71], a[20:23], v[196:199]\n"
    "v_exp_f32 v193, v193\n"
    "v_mfma_f32_16x16x32_bf16 v[196:199], a[72:75], a[24:27], v[196:199]\n"
    "v_exp_f32 v194, v194\n"
    "v_mfma_f32_16x16x32_bf16 v[196:199], a[76:79], a[28:31], v[196:199]\n"
    "s_waitcnt vmcnt(6) lgkmcnt(6)\n"
    "buffer_load_dwordx4 v236, s[24:27], s53 offen offset:2048  lds\n"
    "v_mfma_f32_16x16x32_bf16 v[80:83], a[104:107], v[160:163], v[80:83]\n"
    "s_barrier\n"
    "ds_read_b128 a[64:67], v228 offset:49152\n"
    "ds_read_b128 a[68:71], v228 offset:49408\n"
    "s_waitcnt lgkmcnt(6)\n"
    "v_mfma_f32_16x16x32_bf16 v[80:83], a[108:111], v[176:179], v[80:83]\n"
    "ds_read_b128 a[72:75], v228 offset:49664\n"
    "ds_read_b128 a[76:79], v228 offset:49920\n"
    "v_mfma_f32_16x16x32_bf16 v[84:87], a[104:107], v[164:167], v[84:87]\n"
    "ds_read_b64_tr_b16 a[104:105], v230 offset:17152\n"
    "ds_read_b64_tr_b16 a[106:107], v230 offset:21248\n"
    "v_mfma_f32_16x16x32_bf16 v[84:87], a[108:111], v[180:183], v[84:87]\n"
    "ds_read_b64_tr_b16 a[108:109], v230 offset:25344\n"
    "ds_read_b64_tr_b16 a[110:111], v230 offset:29440\n"
    "v_mfma_f32_16x16x32_bf16 v[200:203], a[80:83], a[0:3], v[208:211]\n"
    "v_cvt_pk_bf16_f32 v144, v192, v193\n"
    "v_exp_f32 v195, v195\n"
    "v_mfma_f32_16x16x32_bf16 v[200:203], a[84:87], a[4:7], v[200:203]\n"
    "v_cvt_pk_bf16_f32 v145, v194, v195\n"
    "v_exp_f32 v196, v196\n"
    "v_mfma_f32_16x16x32_bf16 v[200:203], a[88:91], a[8:11], v[200:203]\n"
    "v_exp_f32 v197, v197\n"
    "v_mfma_f32_16x16x32_bf16 v[200:203], a[92:95], a[12:15], v[200:203]\n"
    "s_waitcnt lgkmcnt(10)\n"
    "v_exp_f32 v198, v198\n"
    "v_mfma_f32_16x16x32_bf16 v[96:99], a[96:99], v[160:163], v[96:99]\n"
    "s_waitcnt lgkmcnt(8)\n"
    "v_cvt_pk_bf16_f32 v148, v196, v197\n"
    "v_exp_f32 v199, v199\n"
    "v_mfma_f32_16x16x32_bf16 v[96:99], a[100:103], v[176:179], v[96:99]\n"
    "v_cvt_pk_bf16_f32 v149, v198, v199\n"
    "v_mfma_f32_16x16x32_bf16 v[100:103], a[96:99], v[164:167], v[100:103]\n"
    "ds_read_b64_tr_b16 a[96:97], v229 offset:32768\n"
    "ds_read_b64_tr_b16 a[98:99], v229 offset:36864\n"
    "v_mfma_f32_16x16x32_bf16 v[100:103], a[100:103], v[180:183], v[100:103]\n"
    "buffer_load_dwordx4 v237, s[24:27], s53 offen offset:3072  lds\n"
    "v_mfma_f32_16x16x32_bf16 v[204:207], a[80:83], a[16:19], v[212:215]\n"
    "ds_read_b64_tr_b16 a[100:101], v229 offset:40960\n"
    "ds_read_b64_tr_b16 a[102:103], v229 offset:45056\n"
    "v_mfma_f32_16x16x32_bf16 v[204:207], a[84:87], a[20:23], v[204:207]\n"
    "ds_read_b128 a[80:83], v228 offset:53248\n"
    "ds_read_b128 a[84:87], v228 offset:53504\n"
    "v_mfma_f32_16x16x32_bf16 v[204:207], a[88:91], a[24:27], v[204:207]\n"
    "ds_read_b128 a[88:91], v228 offset:53760\n"
    "v_exp_f32 v200, v200\n"
    "v_mfma_f32_16x16x32_bf16 v[204:207], a[92:95], a[28:31], v[204:207]\n"
    "s_waitcnt lgkmcnt(9)\n"
    "ds_read_b128 a[92:95], v228 offset:54016\n"
    "v_exp_f32 v201, v201\n"
    "v_mfma_f32_16x16x32_bf16 v[112:115], a[104:107], v[160:163], v[112:115]\n"
    "s_waitcnt lgkmcnt(8)\n"
    "v_exp_f32 v202, v202\n"
    "v_mfma_f32_16x16x32_bf16 v[112:115], a[108:111], v[176:179], v[112:115]\n"
    "v_cvt_pk_bf16_f32 v146, v200, v201\n"
    "v_exp_f32 v203, v203\n"
    "v_mfma_f32_16x16x32_bf16 v[116:119], a[104:107], v[164:167], v[116:119]\n"
    "ds_read_b64_tr_b16 a[104:105], v230 offset:32768\n"
    "ds_read_b64_tr_b16 a[106:107], v230 offset:36864\n"
    "v_mfma_f32_16x16x32_bf16 v[116:119], a[108:111], v[180:183], v[116:119]\n"
    "buffer_load_dword v254, v234, s[24:27], s47 offen offset:0\n"
    "v_mfma_f32_16x16x32_bf16 v[192:195], a[64:67], a[0:3], v[208:211]\n"
    "s_sub_u32 s85, s51, 4096\n"
    "ds_read_b64_tr_b16 a[108:109], v230 offset:40960\n"
    "ds_read_b64_tr_b16 a[110:111], v230 offset:45056\n"
    "v_mfma_f32_16x16x32_bf16 v[192:195], a[68:71], a[4:7], v[192:195]\n"
    "s_and_b32 s85, s85, 0x3000\n"
    "v_cvt_pk_bf16_f32 v147, v202, v203\n"
    "v_exp_f32 v204, v204\n"
    "v_mfma_f32_16x16x32_bf16 v[192:195], a[72:75], a[8:11], v[192:195]\n"
    "s_sub_u32 s86, s51, 8192\n"
    "v_exp_f32 v205, v205\n"
    "v_mfma_f32_16x16x32_bf16 v[192:195], a[76:79], a[12:15], v[192:195]\n"
    "s_and_b32 s86, s86, 0x3000\n"
    "v_exp_f32 v206, v206\n"
    "v_mfma_f32_16x16x32_bf16 v[196:199], a[64:67], a[16:19], v[212:215]\n"
    "s_sub_u32 s87, s51, 12288\n"
    "ds_read_b128 a[64:67], v228 offset:57344\n"
    "v_cvt_pk_bf16_f32 v150, v204, v205\n"
    "v_mfma_f32_16x16x32_bf16 v[196:199], a[68:71], a[20:23], v[196:199]\n"
    "s_and_b32 s87, s87, 0x3000\n"
    "ds_read_b128 a[68:71], v228 offset:57600\n"
    "v_exp_f32 v207, v207\n"
    "v_mfma_f32_16x16x32_bf16 v[196:199], a[72:75], a[24:27], v[196:199]\n"
    "s_lshl_b32 s84, s90, 9\n"
    "ds_read_b128 a[72:75], v228 offset:57856\n"
    "v_cvt_pk_bf16_f32 v151, v206, v207\n"
    "v_mfma_f32_16x16x32_bf16 v[196:199], a[76:79], a[28:31], v[196:199]\n"
    "s_add_u32 s84, s84, 65536\n"
    "ds_read_b128 a[76:79], v228 offset:58112\n"
    "v_exp_f32 v192, v192\n"
    "s_waitcnt lgkmcnt(14)\n"
    "v_mfma_f32_16x16x32_bf16 v[0:3], a[96:99], v[128:131], v[0:3]\n"
    "s_waitcnt lgkmcnt(12)\n"
    "v_exp_f32 v193, v193\n"
    "v_mfma_f32_16x16x32_bf16 v[0:3], a[100:103], v[144:147], v[0:3]\n"
    "v_exp_f32 v194, v194\n"
    "v_mfma_f32_16x16x32_bf16 v[4:7], a[96:99], v[132:135], v[4:7]\n"
    "s_waitcnt lgkmcnt(11)\n"
    "ds_read_b64_tr_b16 a[96:97], v229 offset:33024\n"
    "ds_read_b64_tr_b16 a[98:99], v229 offset:37120\n"
    "v_mfma_f32_16x16x32_bf16 v[4:7], a[100:103], v[148:151], v[4:7]\n"
    "s_add_u32 m0, s51, 16384\n"
    "ds_read_b64_tr_b16 a[100:101], v229 offset:41216\n"
    "ds_read_b64_tr_b16 a[102:103], v229 offset:45312\n"
    "v_mfma_f32_16x16x32_bf16 v[200:203], a[80:83], a[0:3], v[208:211]\n"
    "s_xor_b32 s53, s88, 180224\n"
    "buffer_load_dwordx4 v234, s[24:27], s53 offen offset:0  lds\n"
    "s_waitcnt lgkmcnt(14)\n"
    "v_mfma_f32_16x16x32_bf16 v[200:203], a[84:87], a[4:7], v[200:203]\n"
    "s_waitcnt lgkmcnt(13)\n"
    "v_cvt_pk_bf16_f32 v160, v192, v193\n"
    "v_exp_f32 v195, v195\n"
    "v_mfma_f32_16x16x32_bf16 v[200:203], a[88:91], a[8:11], v[200:203]\n"
    "s_waitcnt lgkmcnt(12)\n"
    "v_cvt_pk_bf16_f32 v161, v194, v195\n"
    "v_exp_f32 v196, v196\n"
    "v_mfma_f32_16x16x32_bf16 v[200:203], a[92:95], a[12:15], v[200:203]\n"
    "s_waitcnt lgkmcnt(10)\n"
    "v_mov_b32 v250, v224\n"
    "v_exp_f32 v197, v197\n"
    "v_mfma_f32_16x16x32_bf16 v[16:19], a[104:107], v[128:131], v[16:19]\n"
    "s_waitcnt lgkmcnt(8)\n"
    "v_permlane16_swap_b32 v224, v250\n"
    "v_add_f32 v224, v224, v250\n"
    "v_mfma_f32_16x16x32_bf16 v[16:19], a[108:111], v[144:147], v[16:19]\n"
    "v_mov_b32 v250, v224\n"
    "v_exp_f32 v198, v198\n"
    "v_mfma_f32_16x16x32_bf16 v[20:23], a[104:107], v[132:135], v[20:23]\n"
    "ds_read_b64_tr_b16 a[104:105], v230 offset:33024\n"
    "ds_read_b64_tr_b16 a[106:107], v230 offset:37120\n"
    "v_mfma_f32_16x16x32_bf16 v[20:23], a[108:111], v[148:151], v[20:23]\n"
    "buffer_load_dword v249, v235, s[24:27], s47 offen offset:1024\n"
    "v_mfma_f32_16x16x32_bf16 v[204:207], a[80:83], a[16:19], v[212:215]\n"
    "s_add_u32 s54, s92, 1572864\n"
    "ds_read_b64_tr_b16 a[108:109], v230 offset:41216\n"
    "ds_read_b64_tr_b16 a[110:111], v230 offset:45312\n"
    "v_mfma_f32_16x16x32_bf16 v[204:207], a[84:87], a[20:23], v[204:207]\n"
    "s_xor_b32 s68, s93, 98304\n"
    "ds_read_b128 a[80:83], v228 offset:61440\n"
    "ds_read_b128 a[84:87], v228 offset:61696\n"
    "v_mfma_f32_16x16x32_bf16 v[204:207], a[88:91], a[24:27], v[204:207]\n"
    "s_xor_b32 s69, s93, 229376\n"
    "ds_read_b128 a[88:91], v228 offset:61952\n"
    "v_permlane32_swap_b32 v224, v250\n"
    "v_mfma_f32_16x16x32_bf16 v[204:207], a[92:95], a[28:31], v[204:207]\n"
    "s_xor_b32 s70, s93, 327680\n"
    "ds_read_b128 a[92:95], v228 offset:62208\n"
    "v_add_f32 v224, v224, v250\n"
    "s_waitcnt lgkmcnt(10)\n"
    "v_mfma_f32_16x16x32_bf16 v[32:35], a[96:99], v[128:131], v[32:35]\n"
    "s_xor_b32 s71, s93, 458752\n"
    "v_cvt_pk_bf16_f32 v164, v196, v197\n"
    "v_exp_f32 v199, v199\n"
    "s_waitcnt lgkmcnt(8)\n"
    "v_mfma_f32_16x16x32_bf16 v[32:35], a[100:103], v[144:147], v[32:35]\n"
    "s_xor_b32 s72, s93, 557056\n"
    "v_cvt_pk_bf16_f32 v165, v198, v199\n"
    "v_exp_f32 v200, v200\n"
    "v_mfma_f32_16x16x32_bf16 v[36:39], a[96:99], v[132:135], v[36:39]\n"
    "s_xor_b32 s73, s93, 688128\n"
    "ds_read_b64_tr_b16 a[96:97], v229 offset:33280\n"
    "ds_read_b64_tr_b16 a[98:99], v229 offset:37376\n"
    "v_mfma_f32_16x16x32_bf16 v[36:39], a[100:103], v[148:151], v[36:39]\n"
    "buffer_load_dwordx4 v235, s[24:27], s53 offen offset:1024  lds\n"
    "v_mfma_f32_16x16x32_bf16 a[116:119], a[112:115], v[132:135], a[116:119]\n"
    "ds_read_b64_tr_b16 a[100:101], v229 offset:41472\n"
    "ds_read_b64_tr_b16 a[102:103], v229 offset:45568\n"
    "v_mfma_f32_16x16x32_bf16 a[116:119], a[112:115], v[148:151], a[116:119]\n"
    "v_exp_f32 v201, v201\n"
    "v_mfma_f32_16x16x32_bf16 v[192:195], a[64:67], a[0:3], v[208:211]\n"
    "v_exp_f32 v202, v202\n"
    "v_mfma_f32_16x16x32_bf16 v[192:195], a[68:71], a[4:7], v[192:195]\n"
    "v_cvt_pk_bf16_f32 v162, v200, v201\n"
    "v_exp_f32 v203, v203\n"
    "v_mfma_f32_16x16x32_bf16 v[192:195], a[72:75], a[8:11], v[192:195]\n"
    "v_cvt_pk_bf16_f32 v163, v202, v203\n"
    "v_exp_f32 v204, v204\n"
    "v_mfma_f32_16x16x32_bf16 v[192:195], a[76:79], a[12:15], v[192:195]\n"
    "s_waitcnt lgkmcnt(10)\n"
    "v_exp_f32 v205, v205\n"
    "v_mfma_f32_16x16x32_bf16 v[48:51], a[104:107], v[128:131], v[48:51]\n"
    "s_waitcnt lgkmcnt(8)\n"
    "v_exp_f32 v206, v206\n"
    "v_mfma_f32_16x16x32_bf16 v[48:51], a[108:111], v[144:147], v[48:51]\n"
    "v_cvt_pk_bf16_f32 v166, v204, v205\n"
    "v_exp_f32 v207, v207\n"
    "v_mfma_f32_16x16x32_bf16 v[52:55], a[104:107], v[132:135], v[52:55]\n"
    "ds_read_b64_tr_b16 a[104:105], v230 offset:33280\n"
    "ds_read_b64_tr_b16 a[106:107], v230 offset:37376\n"
    "v_mfma_f32_16x16x32_bf16 v[52:55], a[108:111], v[148:151], v[52:55]\n"
    "ds_read_b64_tr_b16 a[108:109], v230 offset:41472\n"
    "ds_read_b64_tr_b16 a[110:111], v230 offset:45568\n"
    "v_mfma_f32_16x16x32_bf16 v[196:199], a[64:67], a[16:19], v[212:215]\n"
    "v_cvt_pk_bf16_f32 v167, v206, v207\n"
    "v_exp_f32 v192, v192\n"
    "v_mfma_f32_16x16x32_bf16 v[196:199], a[68:71], a[20:23], v[196:199]\n"
    "v_exp_f32 v193, v193\n"
    "v_mfma_f32_16x16x32_bf16 v[196:199], a[72:75], a[24:27], v[196:199]\n"
    "v_exp_f32 v194, v194\n"
    "v_mfma_f32_16x16x32_bf16 v[196:199], a[76:79], a[28:31], v[196:199]\n"
    "s_waitcnt lgkmcnt(6)\n"
    "buffer_load_dwordx4 v236, s[24:27], s53 offen offset:2048  lds\n"
    "v_mfma_f32_16x16x32_bf16 v[64:67], a[96:99], v[128:131], v[64:67]\n"
    "s_waitcnt lgkmcnt(4)\n"
    "v_cvt_pk_bf16_f32 v176, v192, v193\n"
    "v_exp_f32 v195, v195\n"
    "v_mfma_f32_16x16x32_bf16 v[64:67], a[100:103], v[144:147], v[64:67]\n"
    "v_cvt_pk_bf16_f32 v177, v194, v195\n"
    "v_mfma_f32_16x16x32_bf16 v[68:71], a[96:99], v[132:135], v[68:71]\n"
    "v_mfma_f32_16x16x32_bf16 v[68:71], a[100:103], v[148:151], v[68:71]\n"
    "s_waitcnt vmcnt(9)\n"
    "v_exp_f32 v196, v196\n"
    "v_mfma_f32_16x16x32_bf16 a[124:127], a[112:115], v[128:131], 0\n"
    "s_barrier\n"
    "ds_read_b64_tr_b16 a[96:97], v229 offset:33536\n"
    "ds_read_b64_tr_b16 a[98:99], v229 offset:37632\n"
    "v_mfma_f32_16x16x32_bf16 a[124:127], a[112:115], v[144:147], a[124:127]\n"
    "ds_read_b64_tr_b16 a[100:101], v229 offset:41728\n"
    "ds_read_b64_tr_b16 a[102:103], v229 offset:45824\n"
    "v_mfma_f32_16x16x32_bf16 v[200:203], a[80:83], a[0:3], v[208:211]\n"
    "ds_read_b64_tr_b16 a[64:65], v229 offset:49408\n"
    "ds_read_b64_tr_b16 a[66:67], v229 offset:53504\n"
    "v_mfma_f32_16x16x32_bf16 v[200:203], a[84:87], a[4:7], v[200:203]\n"
    "ds_read_b64_tr_b16 a[68:69], v229 offset:57600\n"
    "ds_read_b64_tr_b16 a[70:71], v229 offset:61696\n"
    "v_mfma_f32_16x16x32_bf16 v[200:203], a[88:91], a[8:11], v[200:203]\n"
    "ds_read_b64_tr_b16 a[72:73], v230 offset:49408\n"
    "ds_read_b64_tr_b16 a[74:75], v230 offset:53504\n"
    "v_mfma_f32_16x16x32_bf16 v[200:203], a[92:95], a[12:15], v[200:203]\n"
    "s_waitcnt lgkmcnt(12)\n"
    "buffer_load_dwordx4 v237, s[24:27], s53 offen offset:3072  lds\n"
    "v_mfma_f32_16x16x32_bf16 v[80:83], a[104:107], v[128:131], v[80:83]\n"
    "s_add_u32 s52, s92, 524288\n"
    "ds_read_b64_tr_b16 a[76:77], v230 offset:57600\n"
    "ds_read_b64_tr_b16 a[78:79], v230 offset:61696\n"
    "s_waitcnt lgkmcnt(12)\n"
    "v_mfma_f32_16x16x32_bf16 v[80:83], a[108:111], v[144:147], v[80:83]\n"
    "v_exp_f32 v197, v197\n"
    "v_mfma_f32_16x16x32_bf16 v[84:87], a[104:107], v[132:135], v[84:87]\n"
    "ds_read_b64_tr_b16 a[104:105], v230 offset:33536\n"
    "ds_read_b64_tr_b16 a[106:107], v230 offset:37632\n"
    "v_mfma_f32_16x16x32_bf16 v[84:87], a[108:111], v[148:151], v[84:87]\n"
    "ds_read_b64_tr_b16 a[108:109], v230 offset:41728\n"
    "ds_read_b64_tr_b16 a[110:111], v230 offset:45824\n"
    "v_mfma_f32_16x16x32_bf16 v[204:207], a[80:83], a[16:19], v[212:215]\n"
    "v_exp_f32 v198, v198\n"
    "v_mfma_f32_16x16x32_bf16 v[204:207], a[84:87], a[20:23], v[204:207]\n"
    "v_cvt_pk_bf16_f32 v180, v196, v197\n"
    "v_exp_f32 v199, v199\n"
    "v_mfma_f32_16x16x32_bf16 v[204:207], a[88:91], a[24:27], v[204:207]\n"
    "v_cvt_pk_bf16_f32 v181, v198, v199\n"
    "v_exp_f32 v200, v200\n"
    "v_mfma_f32_16x16x32_bf16 v[204:207], a[92:95], a[28:31], v[204:207]\n"
    "s_waitcnt lgkmcnt(14)\n"
    "v_exp_f32 v201, v201\n"
    "v_mfma_f32_16x16x32_bf16 v[96:99], a[96:99], v[128:131], v[96:99]\n"
    "s_waitcnt lgkmcnt(12)\n"
    "v_exp_f32 v202, v202\n"
    "v_mfma_f32_16x16x32_bf16 v[96:99], a[100:103], v[144:147], v[96:99]\n"
    "v_cvt_pk_bf16_f32 v178, v200, v201\n"
    "v_exp_f32 v203, v203\n"
    "v_mfma_f32_16x16x32_bf16 v[100:103], a[96:99], v[132:135], v[100:103]\n"
    "ds_read_b64_tr_b16 a[96:97], v229 offset:49152\n"
    "ds_read_b64_tr_b16 a[98:99], v229 offset:53248\n"
    "v_mfma_f32_16x16x32_bf16 v[100:103], a[100:103], v[148:151], v[100:103]\n"
    "s_waitcnt lgkmcnt(4)\n"
    "ds_read_b64_tr_b16 a[100:101], v229 offset:57344\n"
    "ds_read_b64_tr_b16 a[102:103], v229 offset:61440\n"
    "v_mfma_f32_16x16x32_bf16 v[112:115], a[104:107], v[128:131], v[112:115]\n"
    "s_waitcnt lgkmcnt(4)\n"
    "ds_read_b64_tr_b16 a[80:81], v229 offset:49664\n"
    "ds_read_b64_tr_b16 a[82:83], v229 offset:53760\n"
    "v_mfma_f32_16x16x32_bf16 v[112:115], a[108:111], v[144:147], v[112:115]\n"
    "ds_read_b64_tr_b16 a[84:85], v229 offset:57856\n"
    "ds_read_b64_tr_b16 a[86:87], v229 offset:61952\n"
    "v_mfma_f32_16x16x32_bf16 v[116:119], a[104:107], v[132:135], v[116:119]\n"
    "ds_read_b64_tr_b16 a[88:89], v230 offset:49664\n"
    "ds_read_b64_tr_b16 a[90:91], v230 offset:53760\n"
    "v_mfma_f32_16x16x32_bf16 v[116:119], a[108:111], v[148:151], v[116:119]\n"
    "ds_read_b64_tr_b16 a[92:93], v230 offset:57856\n"
    "ds_read_b64_tr_b16 a[94:95], v230 offset:61952\n"
    "v_mfma_f32_16x16x32_bf16 a[124:127], a[112:115], v[160:163], a[124:127]\n"
    "ds_read_b64_tr_b16 a[104:105], v230 offset:49152\n"
    "ds_read_b64_tr_b16 a[106:107], v230 offset:53248\n"
    "ds_read_b64_tr_b16 a[108:109], v230 offset:57344\n"
    "ds_read_b64_tr_b16 a[110:111], v230 offset:61440\n"
    "ds_read_b64_tr_b16 v[128:129], v229 offset:49920\n"
    "ds_read_b64_tr_b16 v[130:131], v229 offset:54016\n"
    "ds_read_b64_tr_b16 v[132:133], v229 offset:58112\n"
    "ds_read_b64_tr_b16 v[134:135], v229 offset:62208\n"
    "ds_read_b64_tr_b16 v[144:145], v230 offset:49920\n"
    "ds_read_b64_tr_b16 v[146:147], v230 offset:54016\n"
    "ds_read_b64_tr_b16 v[148:149], v230 offset:58112\n"
    "ds_read_b64_tr_b16 v[150:151], v230 offset:62208\n"
    "v_cvt_pk_bf16_f32 v179, v202, v203\n"
    "v_exp_f32 v204, v204\n"
    "v_exp_f32 v205, v205\n"
    "v_mfma_f32_16x16x32_bf16 a[124:127], a[112:115], v[176:179], a[124:127]\n"
    "s_waitcnt lgkmcnt(15)\n"
    "v_exp_f32 v206, v206\n"
    "v_mfma_f32_16x16x32_bf16 v[0:3], a[96:99], v[160:163], v[0:3]\n"
    "s_waitcnt lgkmcnt(15)\n"
    "v_cvt_pk_bf16_f32 v182, v204, v205\n"
    "v_exp_f32 v207, v207\n"
    "v_mfma_f32_16x16x32_bf16 v[0:3], a[100:103], v[176:179], v[0:3]\n"
    "s_waitcnt lgkmcnt(10)\n"
    "v_cvt_pk_bf16_f32 v183, v206, v207\n"
    "v_mfma_f32_16x16x32_bf16 v[16:19], a[104:107], v[160:163], v[16:19]\n"
    "s_waitcnt lgkmcnt(8)\n"
    "v_mfma_f32_16x16x32_bf16 v[16:19], a[108:111], v[176:179], v[16:19]\n"
    "v_accvgpr_read_b32 v250, a124\n"
    "v_add_f32 v250, v224, v250\n"
    "v_mfma_f32_16x16x32_bf16 v[32:35], a[64:67], v[160:163], v[32:35]\n"
    "v_rcp_f32 v224, v250\n"
    "v_mfma_f32_16x16x32_bf16 v[32:35], a[68:71], v[176:179], v[32:35]\n"
    "v_mul_f32 v0, v224, v0\n"
    "v_mul_f32 v1, v224, v1\n"
    "v_mfma_f32_16x16x32_bf16 v[48:51], a[72:75], v[160:163], v[48:51]\n"
    "v_mul_f32 v2, v224, v2\n"
    "v_mul_f32 v3, v224, v3\n"
    "v_mfma_f32_16x16x32_bf16 v[48:51], a[76:79], v[176:179], v[48:51]\n"
    "v_cvt_pk_bf16_f32 v0, v0, v1\n"
    "v_cvt_pk_bf16_f32 v1, v2, v3\n"
    "v_mfma_f32_16x16x32_bf16 v[64:67], a[80:83], v[160:163], v[64:67]\n"
    "v_mul_f32 v16, v224, v16\n"
    "v_mul_f32 v17, v224, v17\n"
    "v_mfma_f32_16x16x32_bf16 v[64:67], a[84:87], v[176:179], v[64:67]\n"
    "v_mul_f32 v18, v224, v18\n"
    "v_mul_f32 v19, v224, v19\n"
    "v_mfma_f32_16x16x32_bf16 v[80:83], a[88:91], v[160:163], v[80:83]\n"
    "v_cvt_pk_bf16_f32 v2, v16, v17\n"
    "v_cvt_pk_bf16_f32 v3, v18, v19\n"
    "v_mfma_f32_16x16x32_bf16 v[80:83], a[92:95], v[176:179], v[80:83]\n"
    "buffer_store_dwordx4 v[0:3], v233, s[4:7], s92 offen offset:0 sc1\n"
    "v_mfma_f32_16x16x32_bf16 a[116:119], a[112:115], v[164:167], a[116:119]\n"
    "v_mul_f32 v32, v224, v32\n"
    "v_mul_f32 v33, v224, v33\n"
    "v_mfma_f32_16x16x32_bf16 a[116:119], a[112:115], v[180:183], a[116:119]\n"
    "s_waitcnt lgkmcnt(6)\n"
    "v_mul_f32 v34, v224, v34\n"
    "v_mul_f32 v35, v224, v35\n"
    "v_mfma_f32_16x16x32_bf16 v[96:99], v[128:131], v[160:163], v[96:99]\n"
    "s_waitcnt lgkmcnt(4)\n"
    "v_cvt_pk_bf16_f32 v32, v32, v33\n"
    "v_cvt_pk_bf16_f32 v33, v34, v35\n"
    "v_mfma_f32_16x16x32_bf16 v[96:99], v[132:135], v[176:179], v[96:99]\n"
    "s_waitcnt lgkmcnt(2)\n"
    "v_mul_f32 v48, v224, v48\n"
    "v_mul_f32 v49, v224, v49\n"
    "v_mfma_f32_16x16x32_bf16 v[112:115], v[144:147], v[160:163], v[112:115]\n"
    "s_waitcnt lgkmcnt(0)\n"
    "v_accvgpr_read_b32 v225, a116\n"
    "v_add_f32 v250, v250, v225\n"
    "v_mfma_f32_16x16x32_bf16 v[112:115], v[148:151], v[176:179], v[112:115]\n"
    "ds_write_b32 v251, v250 offset:0\n"
    "v_rcp_f32 v225, v225\n"
    "v_mul_f32 v50, v224, v50\n"
    "v_mul_f32 v51, v224, v51\n"
    "v_cvt_pk_bf16_f32 v34, v48, v49\n"
    "v_cvt_pk_bf16_f32 v35, v50, v51\n"
    "v_mul_f32 v64, v224, v64\n"
    "v_mul_f32 v65, v224, v65\n"
    "v_mul_f32 v66, v224, v66\n"
    "v_mul_f32 v67, v224, v67\n"
    "v_cvt_pk_bf16_f32 v64, v64, v65\n"
    "v_cvt_pk_bf16_f32 v65, v66, v67\n"
    "v_mul_f32 v80, v224, v80\n"
    "v_mul_f32 v81, v224, v81\n"
    "v_mul_f32 v82, v224, v82\n"
    "v_mul_f32 v83, v224, v83\n"
    "v_cvt_pk_bf16_f32 v66, v80, v81\n"
    "v_cvt_pk_bf16_f32 v67, v82, v83\n"
    "v_mfma_f32_16x16x32_bf16 v[36:39], a[64:67], v[164:167], v[36:39]\n"
    "buffer_store_dwordx4 v[32:35], v233, s[4:7], s92 offen offset:1024 sc1\n"
    "v_mfma_f32_16x16x32_bf16 v[36:39], a[68:71], v[180:183], v[36:39]\n"
    "buffer_store_dwordx4 v[64:67], v233, s[4:7], s92 offen offset:2048 sc1\n"
    "v_mfma_f32_16x16x32_bf16 v[52:55], a[72:75], v[164:167], v[52:55]\n"
    "ds_read_b128 a[64:67], v228 offset:32768\n"
    "ds_read_b128 a[68:71], v228 offset:33024\n"
    "v_mfma_f32_16x16x32_bf16 v[52:55], a[76:79], v[180:183], v[52:55]\n"
    "ds_read_b128 a[72:75], v228 offset:33280\n"
    "ds_read_b128 a[76:79], v228 offset:33536\n"
    "v_mul_f32 v96, v224, v96\n"
    "v_mul_f32 v97, v224, v97\n"
    "v_mul_f32 v98, v224, v98\n"
    "v_mul_f32 v99, v224, v99\n"
    "v_cvt_pk_bf16_f32 v96, v96, v97\n"
    "v_cvt_pk_bf16_f32 v97, v98, v99\n"
    "v_mul_f32 v112, v224, v112\n"
    "v_mul_f32 v113, v224, v113\n"
    "v_mul_f32 v114, v224, v114\n"
    "v_mul_f32 v115, v224, v115\n"
    "v_cvt_pk_bf16_f32 v98, v112, v113\n"
    "v_cvt_pk_bf16_f32 v99, v114, v115\n"
    "v_mfma_f32_16x16x32_bf16 v[4:7], a[96:99], v[164:167], v[4:7]\n"
    "buffer_store_dwordx4 v[96:99], v233, s[4:7], s92 offen offset:3072 sc1\n"
    "v_mfma_f32_16x16x32_bf16 v[4:7], a[100:103], v[180:183], v[4:7]\n"
    "ds_read_b64_tr_b16 a[96:97], v229 offset:16384\n"
    "ds_read_b64_tr_b16 a[98:99], v229 offset:20480\n"
    "v_mfma_f32_16x16x32_bf16 v[20:23], a[104:107], v[164:167], v[20:23]\n"
    "ds_read_b64_tr_b16 a[100:101], v229 offset:24576\n"
    "ds_read_b64_tr_b16 a[102:103], v229 offset:28672\n"
    "v_mfma_f32_16x16x32_bf16 v[20:23], a[108:111], v[180:183], v[20:23]\n"
    "ds_read_b64_tr_b16 a[104:105], v230 offset:16384\n"
    "ds_read_b64_tr_b16 a[106:107], v230 offset:20480\n"
    "v_mfma_f32_16x16x32_bf16 v[68:71], a[80:83], v[164:167], v[68:71]\n"
    "Lpg12%=:\n"
    "s_cbranch_execz Lwb1%=\n"
    "ds_read_b64_tr_b16 a[108:109], v230 offset:24576\n"
    "ds_read_b64_tr_b16 a[110:111], v230 offset:28672\n"
    "v_mfma_f32_16x16x32_bf16 v[68:71], a[84:87], v[180:183], v[68:71]\n"
    "v_mul_f32 v36, v225, v36\n"
    "v_mul_f32 v37, v225, v37\n"
    "v_mfma_f32_16x16x32_bf16 v[84:87], a[88:91], v[164:167], v[84:87]\n"
    "v_mul_f32 v38, v225, v38\n"
    "v_mul_f32 v39, v225, v39\n"
    "v_mfma_f32_16x16x32_bf16 v[84:87], a[92:95], v[180:183], v[84:87]\n"
    "s_waitcnt lgkmcnt(12)\n"
    "s_barrier\n"
    "ds_read_b128 v[208:211], v252 offset:0\n"
    "ds_read_b128 a[80:83], v228 offset:36864\n"
    "v_mfma_f32_16x16x32_bf16 v[100:103], v[128:131], v[164:167], v[100:103]\n"
    "ds_read_b128 a[84:87], v228 offset:37120\n"
    "ds_read_b128 a[88:91], v228 offset:37376\n"
    "v_mfma_f32_16x16x32_bf16 v[100:103], v[132:135], v[180:183], v[100:103]\n"
    "ds_read_b128 a[92:95], v228 offset:37632\n"
    "v_cvt_pk_bf16_f32 v36, v36, v37\n"
    "v_mfma_f32_16x16x32_bf16 v[116:119], v[144:147], v[164:167], v[116:119]\n"
    "v_cvt_pk_bf16_f32 v37, v38, v39\n"
    "v_mul_f32 v52, v225, v52\n"
    "v_mfma_f32_16x16x32_bf16 v[116:119], v[148:151], v[180:183], v[116:119]\n"
    "v_mul_f32 v53, v225, v53\n"
    "v_mul_f32 v54, v225, v54\n"
    "v_mul_f32 v55, v225, v55\n"
    "v_cvt_pk_bf16_f32 v38, v52, v53\n"
    "v_cvt_pk_bf16_f32 v39, v54, v55\n"
    "v_mul_f32 v4, v225, v4\n"
    "v_mul_f32 v5, v225, v5\n"
    "v_mul_f32 v6, v225, v6\n"
    "v_mul_f32 v7, v225, v7\n"
    "v_cvt_pk_bf16_f32 v4, v4, v5\n"
    "v_cvt_pk_bf16_f32 v5, v6, v7\n"
    "v_mul_f32 v20, v225, v20\n"
    "s_waitcnt lgkmcnt(4)\n"
    "v_add_f32 v208, v208, v209\n"
    "v_add_f32 v210, v210, v211\n"
    "v_add_f32 v208, v208, v210\n"
    "v_cmp_ngt_f32 vcc, 0x53800000, v208\n"
    "v_mul_f32 v21, v225, v21\n"
    "v_mul_f32 v22, v225, v22\n"
    "s_and_b64 vcc, vcc, s[64:65]\n"
    "s_cbranch_scc1 Lfall%=\n"
    "v_mul_f32 v23, v225, v23\n"
    "v_cvt_pk_bf16_f32 v6, v20, v21\n"
    "v_cvt_pk_bf16_f32 v7, v22, v23\n"
    "v_mfma_f32_16x16x32_bf16 v[192:195], a[64:67], a[32:35], v[216:219]\n"
    "v_mul_f32 v68, v225, v68\n"
    "v_mul_f32 v69, v225, v69\n"
    "v_mfma_f32_16x16x32_bf16 v[192:195], a[68:71], a[36:39], v[192:195]\n"
    "v_mul_f32 v70, v225, v70\n"
    "v_mul_f32 v71, v225, v71\n"
    "v_mfma_f32_16x16x32_bf16 v[192:195], a[72:75], a[40:43], v[192:195]\n"
    "v_cvt_pk_bf16_f32 v68, v68, v69\n"
    "v_cvt_pk_bf16_f32 v69, v70, v71\n"
    "v_mfma_f32_16x16x32_bf16 v[192:195], a[76:79], a[44:47], v[192:195]\n"
    "v_mul_f32 v84, v225, v84\n"
    "v_mul_f32 v85, v225, v85\n"
    "v_mfma_f32_16x16x32_bf16 v[8:11], a[96:99], v[168:171], v[8:11]\n"
    "v_mul_f32 v86, v225, v86\n"
    "v_mul_f32 v87, v225, v87\n"
    "v_mfma_f32_16x16x32_bf16 v[8:11], a[100:103], v[184:187], v[8:11]\n"
    "v_cvt_pk_bf16_f32 v70, v84, v85\n"
    "v_cvt_pk_bf16_f32 v71, v86, v87\n"
    "v_mfma_f32_16x16x32_bf16 v[12:15], a[96:99], v[172:175], v[12:15]\n"
    "ds_read_b64_tr_b16 a[96:97], v229 offset:16640\n"
    "ds_read_b64_tr_b16 a[98:99], v229 offset:20736\n"
    "v_mfma_f32_16x16x32_bf16 v[12:15], a[100:103], v[188:191], v[12:15]\n"
    "ds_read_b64_tr_b16 a[100:101], v229 offset:24832\n"
    "ds_read_b64_tr_b16 a[102:103], v229 offset:28928\n"
    "v_mfma_f32_16x16x32_bf16 v[196:199], a[64:67], a[48:51], v[220:223]\n"
    "ds_read_b128 a[64:67], v228 offset:40960\n"
    "v_mul_f32 v100, v225, v100\n"
    "v_mfma_f32_16x16x32_bf16 v[196:199], a[68:71], a[52:55], v[196:199]\n"
    "ds_read_b128 a[68:71], v228 offset:41216\n"
    "v_mul_f32 v101, v225, v101\n"
    "v_mfma_f32_16x16x32_bf16 v[196:199], a[72:75], a[56:59], v[196:199]\n"
    "ds_read_b128 a[72:75], v228 offset:41472\n"
    "v_mul_f32 v102, v225, v102\n"
    "v_mfma_f32_16x16x32_bf16 v[196:199], a[76:79], a[60:63], v[196:199]\n"
    "ds_read_b128 a[76:79], v228 offset:41728\n"
    "v_mul_f32 v103, v225, v103\n"
    "v_mfma_f32_16x16x32_bf16 v[24:27], a[104:107], v[168:171], v[24:27]\n"
    "v_cvt_pk_bf16_f32 v100, v100, v101\n"
    "v_cvt_pk_bf16_f32 v101, v102, v103\n"
    "v_mfma_f32_16x16x32_bf16 v[24:27], a[108:111], v[184:187], v[24:27]\n"
    "v_mul_f32 v116, v225, v116\n"
    "v_mul_f32 v117, v225, v117\n"
    "v_mfma_f32_16x16x32_bf16 v[28:31], a[104:107], v[172:175], v[28:31]\n"
    "ds_read_b64_tr_b16 a[104:105], v230 offset:16640\n"
    "ds_read_b64_tr_b16 a[106:107], v230 offset:20736\n"
    "v_mfma_f32_16x16x32_bf16 v[28:31], a[108:111], v[188:191], v[28:31]\n"
    "s_waitcnt lgkmcnt(13)\n"
    "ds_read_b64_tr_b16 a[108:109], v230 offset:24832\n"
    "ds_read_b64_tr_b16 a[110:111], v230 offset:28928\n"
    "v_mfma_f32_16x16x32_bf16 v[200:203], a[80:83], a[32:35], v[216:219]\n"
    "s_waitcnt lgkmcnt(14)\n"
    "v_mul_f32 v118, v225, v118\n"
    "v_mul_f32 v119, v225, v119\n"
    "v_mfma_f32_16x16x32_bf16 v[200:203], a[84:87], a[36:39], v[200:203]\n"
    "s_waitcnt lgkmcnt(13)\n"
    "v_cvt_pk_bf16_f32 v102, v116, v117\n"
    "v_cvt_pk_bf16_f32 v103, v118, v119\n"
    "v_mfma_f32_16x16x32_bf16 v[200:203], a[88:91], a[40:43], v[200:203]\n"
    "s_waitcnt lgkmcnt(12)\n"
    "v_exp_f32 v192, v192\n"
    "v_mfma_f32_16x16x32_bf16 v[200:203], a[92:95], a[44:47], v[200:203]\n"
    "s_waitcnt lgkmcnt(10)\n"
    "v_exp_f32 v193, v193\n"
    "v_mfma_f32_16x16x32_bf16 v[40:43], a[96:99], v[168:171], v[40:43]\n"
    "s_waitcnt lgkmcnt(8)\n"
    "v_exp_f32 v194, v194\n"
    "v_mfma_f32_16x16x32_bf16 v[40:43], a[100:103], v[184:187], v[40:43]\n"
    "v_cvt_pk_bf16_f32 v136, v192, v193\n"
    "v_exp_f32 v195, v195\n"
    "v_mfma_f32_16x16x32_bf16 v[44:47], a[96:99], v[172:175], v[44:47]\n"
    "ds_read_b64_tr_b16 a[96:97], v229 offset:16896\n"
    "ds_read_b64_tr_b16 a[98:99], v229 offset:20992\n"
    "v_mfma_f32_16x16x32_bf16 v[44:47], a[100:103], v[188:191], v[44:47]\n"
    "ds_read_b64_tr_b16 a[100:101], v229 offset:25088\n"
    "ds_read_b64_tr_b16 a[102:103], v229 offset:29184\n"
    "v_mfma_f32_16x16x32_bf16 v[204:207], a[80:83], a[48:51], v[220:223]\n"
    "s_waitcnt vmcnt(57)\n"
    "ds_read_b64_tr_b16 a[0:1], v242 offset:0\n"
    "ds_read_b64_tr_b16 a[4:5], v243 offset:0\n"
    "v_mfma_f32_16x16x32_bf16 v[204:207], a[84:87], a[52:55], v[204:207]\n"
    "ds_read_b64_tr_b16 a[8:9], v242 offset:256\n"
    "ds_read_b64_tr_b16 a[12:13], v243 offset:256\n"
    "v_mfma_f32_16x16x32_bf16 v[204:207], a[88:91], a[56:59], v[204:207]\n"
    "ds_read_b64_tr_b16 a[16:17], v242 offset:512\n"
    "ds_read_b64_tr_b16 a[20:21], v243 offset:512\n"
    "v_mfma_f32_16x16x32_bf16 v[204:207], a[92:95], a[60:63], v[204:207]\n"
    "s_waitcnt vmcnt(0) lgkmcnt(12)\n"
    "buffer_store_dword v246, v247, s[36:39], 0 offen offset:0 sc1\n"
    "v_mfma_f32_16x16x32_bf16 v[56:59], a[104:107], v[168:171], v[56:59]\n"
    "s_waitcnt lgkmcnt(10)\n"
    "buffer_store_dwordx4 v[4:7], v233, s[4:7], s52 offen offset:0 sc1\n"
    "v_mfma_f32_16x16x32_bf16 v[56:59], a[108:111], v[184:187], v[56:59]\n"
    "ds_read_b64_tr_b16 a[24:25], v242 offset:768\n"
    "ds_read_b64_tr_b16 a[28:29], v243 offset:768\n"
    "v_mfma_f32_16x16x32_bf16 v[60:63], a[104:107], v[172:175], v[60:63]\n"
    "ds_read_b128 a[80:83], v228 offset:45056\n"
    "ds_read_b128 a[84:87], v228 offset:45312\n"
    "v_mfma_f32_16x16x32_bf16 v[60:63], a[108:111], v[188:191], v[60:63]\n"
    "buffer_store_dwordx4 v[36:39], v233, s[4:7], s52 offen offset:1024 sc1\n"
    "v_mfma_f32_16x16x32_bf16 a[120:123], a[112:115], v[172:175], a[120:123]\n"
    "ds_read_b128 a[88:91], v228 offset:45568\n"
    "ds_read_b128 a[92:95], v228 offset:45824\n"
    "v_mfma_f32_16x16x32_bf16 a[120:123], a[112:115], v[188:191], a[120:123]\n"
    "s_waitcnt vmcnt(46)\n"
    "ds_read_b64_tr_b16 a[2:3], v242 offset:16384\n"
    "ds_read_b64_tr_b16 a[6:7], v243 offset:16384\n"
    "ds_read_b64_tr_b16 a[10:11], v242 offset:16640\n"
    "ds_read_b64_tr_b16 a[14:15], v243 offset:16640\n"
    "ds_read_b64_tr_b16 a[18:19], v242 offset:16896\n"
    "ds_read_b64_tr_b16 a[22:23], v243 offset:16896\n"
    "ds_read_b64_tr_b16 a[26:27], v242 offset:17152\n"
    "ds_read_b64_tr_b16 a[30:31], v243 offset:17152\n"
    "ds_read_b64_tr_b16 a[104:105], v230 offset:16896\n"
    "ds_read_b64_tr_b16 a[106:107], v230 offset:20992\n"
    "ds_read_b64_tr_b16 a[108:109], v230 offset:25088\n"
    "ds_read_b64_tr_b16 a[110:111], v230 offset:29184\n"
    "v_cvt_pk_bf16_f32 v137, v194, v195\n"
    "v_mfma_f32_16x16x32_bf16 v[192:195], a[64:67], a[32:35], v[216:219]\n"
    "v_exp_f32 v196, v196\n"
    "v_mfma_f32_16x16x32_bf16 v[192:195], a[68:71], a[36:39], v[192:195]\n"
    "v_exp_f32 v197, v197\n"
    "v_mfma_f32_16x16x32_bf16 v[192:195], a[72:75], a[40:43], v[192:195]\n"
    "v_exp_f32 v198, v198\n"
    "v_mfma_f32_16x16x32_bf16 v[192:195], a[76:79], a[44:47], v[192:195]\n"
    "s_waitcnt lgkmcnt(15)\n"
    "buffer_store_dwordx4 v[68:71], v233, s[4:7], s52 offen offset:2048 sc1\n"
    "v_mfma_f32_16x16x32_bf16 v[72:75], a[96:99], v[168:171], v[72:75]\n"
    "s_waitcnt lgkmcnt(15)\n"
    "v_cvt_pk_bf16_f32 v140, v196, v197\n"
    "v_exp_f32 v199, v199\n"
    "v_mfma_f32_16x16x32_bf16 v[72:75], a[100:103], v[184:187], v[72:75]\n"
    "v_cvt_pk_bf16_f32 v141, v198, v199\n"
    "v_exp_f32 v200, v200\n"
    "v_mfma_f32_16x16x32_bf16 v[76:79], a[96:99], v[172:175], v[76:79]\n"
    "ds_read_b64_tr_b16 a[96:97], v229 offset:17152\n"
    "ds_read_b64_tr_b16 a[98:99], v229 offset:21248\n"
    "v_mfma_f32_16x16x32_bf16 v[76:79], a[100:103], v[188:191], v[76:79]\n"
    "buffer_store_dwordx4 v[100:103], v233, s[4:7], s52 offen offset:3072 sc1\n"
    "v_mfma_f32_16x16x32_bf16 v[196:199], a[64:67], a[48:51], v[220:223]\n"
    "ds_read_b64_tr_b16 a[100:101], v229 offset:25344\n"
    "ds_read_b64_tr_b16 a[102:103], v229 offset:29440\n"
    "v_mfma_f32_16x16x32_bf16 v[196:199], a[68:71], a[52:55], v[196:199]\n"
    "ds_read_b128 a[64:67], v228 offset:49152\n"
    "ds_read_b128 a[68:71], v228 offset:49408\n"
    "v_mfma_f32_16x16x32_bf16 v[196:199], a[72:75], a[56:59], v[196:199]\n"
    "ds_read_b128 a[72:75], v228 offset:49664\n"
    "v_exp_f32 v201, v201\n"
    "v_mfma_f32_16x16x32_bf16 v[196:199], a[76:79], a[60:63], v[196:199]\n"
    "s_waitcnt lgkmcnt(9)\n"
    "ds_read_b128 a[76:79], v228 offset:49920\n"
    "v_exp_f32 v202, v202\n"
    "v_mfma_f32_16x16x32_bf16 v[88:91], a[104:107], v[168:171], v[88:91]\n"
    "s_waitcnt lgkmcnt(8)\n"
    "v_cvt_pk_bf16_f32 v138, v200, v201\n"
    "v_exp_f32 v203, v203\n"
    "v_mfma_f32_16x16x32_bf16 v[88:91], a[108:111], v[184:187], v[88:91]\n"
    "v_cvt_pk_bf16_f32 v139, v202, v203\n"
    "v_exp_f32 v204, v204\n"
    "v_mfma_f32_16x16x32_bf16 v[92:95], a[104:107], v[172:175], v[92:95]\n"
    "ds_read_b64_tr_b16 a[104:105], v230 offset:17152\n"
    "ds_read_b64_tr_b16 a[106:107], v230 offset:21248\n"
    "v_mfma_f32_16x16x32_bf16 v[92:95], a[108:111], v[188:191], v[92:95]\n"
    "s_mov_b32 s6, 0\n"
    "ds_read_b64_tr_b16 a[108:109], v230 offset:25344\n"
    "ds_read_b64_tr_b16 a[110:111], v230 offset:29440\n"
    "v_mfma_f32_16x16x32_bf16 v[200:203], a[80:83], a[32:35], v[216:219]\n"
    "s_nop 0\n"
    "v_exp_f32 v205, v205\n"
    "v_mfma_f32_16x16x32_bf16 v[200:203], a[84:87], a[36:39], v[200:203]\n"
    "s_add_u32 m0, s51, 131072\n"
    "v_exp_f32 v206, v206\n"
    "v_mfma_f32_16x16x32_bf16 v[200:203], a[88:91], a[40:43], v[200:203]\n"
    "s_xor_b32 s53, s88, 212992\n"
    "buffer_load_dwordx4 v234, s[24:27], s53 offen offset:0  lds\n"
    "v_mfma_f32_16x16x32_bf16 v[200:203], a[92:95], a[44:47], v[200:203]\n"
    "s_waitcnt lgkmcnt(10)\n"
    "buffer_load_dwordx4 v235, s[24:27], s53 offen offset:1024  lds\n"
    "v_mfma_f32_16x16x32_bf16 v[104:107], a[96:99], v[168:171], v[104:107]\n"
    "s_waitcnt lgkmcnt(8)\n"
    "v_cvt_pk_bf16_f32 v142, v204, v205\n"
    "v_exp_f32 v207, v207\n"
    "v_mfma_f32_16x16x32_bf16 v[104:107], a[100:103], v[184:187], v[104:107]\n"
    "v_cvt_pk_bf16_f32 v143, v206, v207\n"
    "v_exp_f32 v192, v192\n"
    "v_mfma_f32_16x16x32_bf16 v[108:111], a[96:99], v[172:175], v[108:111]\n"
    "ds_read_b64_tr_b16 a[96:97], v229 offset:32768\n"
    "ds_read_b64_tr_b16 a[98:99], v229 offset:36864\n"
    "v_mfma_f32_16x16x32_bf16 v[108:111], a[100:103], v[188:191], v[108:111]\n"
    "buffer_load_dwordx4 v236, s[24:27], s53 offen offset:2048  lds\n"
    "v_mfma_f32_16x16x32_bf16 v[204:207], a[80:83], a[48:51], v[220:223]\n"
    "ds_read_b64_tr_b16 a[100:101], v229 offset:40960\n"
    "ds_read_b64_tr_b16 a[102:103], v229 offset:45056\n"
    "v_mfma_f32_16x16x32_bf16 v[204:207], a[84:87], a[52:55], v[204:207]\n"
    "ds_read_b128 a[80:83], v228 offset:53248\n"
    "ds_read_b128 a[84:87], v228 offset:53504\n"
    "v_mfma_f32_16x16x32_bf16 v[204:207], a[88:91], a[56:59], v[204:207]\n"
    "ds_read_b128 a[88:91], v228 offset:53760\n"
    "v_exp_f32 v193, v193\n"
    "v_mfma_f32_16x16x32_bf16 v[204:207], a[92:95], a[60:63], v[204:207]\n"
    "s_waitcnt lgkmcnt(9)\n"
    "buffer_load_dwordx4 v237, s[24:27], s53 offen offset:3072  lds\n"
    "v_mfma_f32_16x16x32_bf16 v[120:123], a[104:107], v[168:171], v[120:123]\n"
    "s_waitcnt lgkmcnt(7)\n"
    "ds_read_b128 a[92:95], v228 offset:54016\n"
    "v_exp_f32 v194, v194\n"
    "v_mfma_f32_16x16x32_bf16 v[120:123], a[108:111], v[184:187], v[120:123]\n"
    "v_cvt_pk_bf16_f32 v152, v192, v193\n"
    "v_exp_f32 v195, v195\n"
    "v_mfma_f32_16x16x32_bf16 v[124:127], a[104:107], v[172:175], v[124:127]\n"
    "v_cvt_pk_bf16_f32 v153, v194, v195\n"
    "v_exp_f32 v196, v196\n"
    "v_mfma_f32_16x16x32_bf16 v[124:127], a[108:111], v[188:191], v[124:127]\n"
    "s_nop 0\n"
    "v_exp_f32 v197, v197\n"
    "v_mfma_f32_16x16x32_bf16 v[192:195], a[64:67], a[32:35], v[216:219]\n"
    "s_add_u32 m0, s51, 147456\n"
    "v_exp_f32 v198, v198\n"
    "v_mfma_f32_16x16x32_bf16 v[192:195], a[68:71], a[36:39], v[192:195]\n"
    "s_xor_b32 s53, s88, 229376\n"
    "buffer_load_dwordx4 v234, s[24:27], s53 offen offset:0  lds\n"
    "v_mfma_f32_16x16x32_bf16 v[192:195], a[72:75], a[40:43], v[192:195]\n"
    "s_barrier\n"
    "ds_read_b64_tr_b16 a[104:105], v230 offset:32768\n"
    "ds_read_b64_tr_b16 a[106:107], v230 offset:36864\n"
    "v_mfma_f32_16x16x32_bf16 v[192:195], a[76:79], a[44:47], v[192:195]\n"
    "buffer_load_dwordx4 v235, s[24:27], s53 offen offset:1024  lds\n"
    "ds_read_b64_tr_b16 a[108:109], v230 offset:40960\n"
    "ds_read_b64_tr_b16 a[110:111], v230 offset:45056\n"
    "v_cvt_pk_bf16_f32 v156, v196, v197\n"
    "v_exp_f32 v199, v199\n"
    "s_nop 0\n"
    "v_cvt_pk_bf16_f32 v157, v198, v199\n"
    "v_mfma_f32_16x16x32_bf16 v[196:199], a[64:67], a[48:51], v[220:223]\n"
    "ds_read_b128 a[64:67], v228 offset:57344\n"
    "v_exp_f32 v200, v200\n"
    "v_mfma_f32_16x16x32_bf16 v[196:199], a[68:71], a[52:55], v[196:199]\n"
    "ds_read_b128 a[68:71], v228 offset:57600\n"
    "v_exp_f32 v201, v201\n"
    "v_mfma_f32_16x16x32_bf16 v[196:199], a[72:75], a[56:59], v[196:199]\n"
    "ds_read_b128 a[72:75], v228 offset:57856\n"
    "v_exp_f32 v202, v202\n"
    "v_mfma_f32_16x16x32_bf16 v[196:199], a[76:79], a[60:63], v[196:199]\n"
    "s_waitcnt lgkmcnt(13)\n"
    "buffer_load_dwordx4 v236, s[24:27], s53 offen offset:2048  lds\n"
    "v_mfma_f32_16x16x32_bf16 v[8:11], a[96:99], v[136:139], v[8:11]\n"
    "s_waitcnt lgkmcnt(11)\n"
    "ds_read_b128 a[76:79], v228 offset:58112\n"
    "v_cvt_pk_bf16_f32 v154, v200, v201\n"
    "v_exp_f32 v203, v203\n"
    "s_nop 0\n"
    "v_cvt_pk_bf16_f32 v155, v202, v203\n"
    "v_exp_f32 v204, v204\n"
    "v_exp_f32 v205, v205\n"
    "v_mfma_f32_16x16x32_bf16 v[8:11], a[100:103], v[152:155], v[8:11]\n"
    "v_exp_f32 v206, v206\n"
    "v_mfma_f32_16x16x32_bf16 v[12:15], a[96:99], v[140:143], v[12:15]\n"
    "ds_read_b64_tr_b16 a[96:97], v229 offset:33024\n"
    "ds_read_b64_tr_b16 a[98:99], v229 offset:37120\n"
    "v_cvt_pk_bf16_f32 v158, v204, v205\n"
    "v_exp_f32 v207, v207\n"
    "s_nop 0\n"
    "v_cvt_pk_bf16_f32 v159, v206, v207\n"
    "v_exp_f32 v192, v192\n"
    "v_exp_f32 v193, v193\n"
    "v_mfma_f32_16x16x32_bf16 v[12:15], a[100:103], v[156:159], v[12:15]\n"
    "s_waitcnt lgkmcnt(13)\n"
    "buffer_load_dwordx4 v237, s[24:27], s53 offen offset:3072  lds\n"
    "v_mfma_f32_16x16x32_bf16 v[200:203], a[80:83], a[32:35], v[216:219]\n"
    "s_waitcnt lgkmcnt(12)\n"
    "buffer_load_dword v249, v244, s[36:39], 0 offen offset:0 sc1\n"
    "v_mfma_f32_16x16x32_bf16 v[200:203], a[84:87], a[36:39], v[200:203]\n"
    "s_waitcnt lgkmcnt(11)\n"
    "ds_read_b64_tr_b16 a[100:101], v229 offset:41216\n"
    "ds_read_b64_tr_b16 a[102:103], v229 offset:45312\n"
    "v_mfma_f32_16x16x32_bf16 v[200:203], a[88:91], a[40:43], v[200:203]\n"
    "s_waitcnt lgkmcnt(12)\n"
    "v_exp_f32 v194, v194\n"
    "v_mfma_f32_16x16x32_bf16 v[200:203], a[92:95], a[44:47], v[200:203]\n"
    "s_add_u32 m0, s51, 81920\n"
    "v_mov_b32 v250, v226\n"
    "v_cvt_pk_bf16_f32 v168, v192, v193\n"
    "s_waitcnt lgkmcnt(10)\n"
    "v_mfma_f32_16x16x32_bf16 v[24:27], a[104:107], v[136:139], v[24:27]\n"
    "s_xor_b32 s53, s88, 196608\n"
    "buffer_load_dwordx4 v234, s[24:27], s53 offen offset:0  lds\n"
    "s_waitcnt lgkmcnt(8)\n"
    "v_mfma_f32_16x16x32_bf16 v[24:27], a[108:111], v[152:155], v[24:27]\n"
    "v_permlane16_swap_b32 v226, v250\n"
    "v_add_f32 v226, v226, v250\n"
    "v_mfma_f32_16x16x32_bf16 v[28:31], a[104:107], v[140:143], v[28:31]\n"
    "ds_read_b64_tr_b16 a[104:105], v230 offset:33024\n"
    "ds_read_b64_tr_b16 a[106:107], v230 offset:37120\n"
    "v_mfma_f32_16x16x32_bf16 v[28:31], a[108:111], v[156:159], v[28:31]\n"
    "buffer_load_dwordx4 v235, s[24:27], s53 offen offset:1024  lds\n"
    "v_mfma_f32_16x16x32_bf16 v[204:207], a[80:83], a[48:51], v[220:223]\n"
    "ds_read_b64_tr_b16 a[108:109], v230 offset:41216\n"
    "ds_read_b64_tr_b16 a[110:111], v230 offset:45312\n"
    "v_mfma_f32_16x16x32_bf16 v[204:207], a[84:87], a[52:55], v[204:207]\n"
    "ds_read_b128 a[80:83], v228 offset:61440\n"
    "ds_read_b128 a[84:87], v228 offset:61696\n"
    "v_mfma_f32_16x16x32_bf16 v[204:207], a[88:91], a[56:59], v[204:207]\n"
    "ds_read_b128 a[88:91], v228 offset:61952\n"
    "v_mov_b32 v250, v226\n"
    "v_mfma_f32_16x16x32_bf16 v[204:207], a[92:95], a[60:63], v[204:207]\n"
    "s_waitcnt lgkmcnt(9)\n"
    "buffer_load_dwordx4 v236, s[24:27], s53 offen offset:2048  lds\n"
    "v_mfma_f32_16x16x32_bf16 v[40:43], a[96:99], v[136:139], v[40:43]\n"
    "s_waitcnt lgkmcnt(7)\n"
    "ds_read_b128 a[92:95], v228 offset:62208\n"
    "v_permlane32_swap_b32 v226, v250\n"
    "v_mfma_f32_16x16x32_bf16 v[40:43], a[100:103], v[152:155], v[40:43]\n"
    "v_add_f32 v226, v226, v250\n"
    "v_exp_f32 v195, v195\n"
    "v_mfma_f32_16x16x32_bf16 v[44:47], a[96:99], v[140:143], v[44:47]\n"
    "ds_read_b64_tr_b16 a[96:97], v229 offset:33280\n"
    "ds_read_b64_tr_b16 a[98:99], v229 offset:37376\n"
    "v_mfma_f32_16x16x32_bf16 v[44:47], a[100:103], v[156:159], v[44:47]\n"
    "buffer_load_dwordx4 v237, s[24:27], s53 offen offset:3072  lds\n"
    "v_mfma_f32_16x16x32_bf16 a[120:123], a[112:115], v[140:143], a[120:123]\n"
    "ds_read_b64_tr_b16 a[100:101], v229 offset:41472\n"
    "ds_read_b64_tr_b16 a[102:103], v229 offset:45568\n"
    "v_mfma_f32_16x16x32_bf16 a[120:123], a[112:115], v[156:159], a[120:123]\n"
    "s_add_u32 m0, s51, 32768\n"
    "s_xor_b32 s53, s88, 245760\n"
    "buffer_load_dwordx4 v234, s[24:27], s53 offen offset:0  lds\n"
    "v_cvt_pk_bf16_f32 v169, v194, v195\n"
    "v_mfma_f32_16x16x32_bf16 v[192:195], a[64:67], a[32:35], v[216:219]\n"
    "v_exp_f32 v196, v196\n"
    "v_mfma_f32_16x16x32_bf16 v[192:195], a[68:71], a[36:39], v[192:195]\n"
    "v_exp_f32 v197, v197\n"
    "v_mfma_f32_16x16x32_bf16 v[192:195], a[72:75], a[40:43], v[192:195]\n"
    "v_exp_f32 v198, v198\n"
    "v_mfma_f32_16x16x32_bf16 v[192:195], a[76:79], a[44:47], v[192:195]\n"
    "s_waitcnt lgkmcnt(10)\n"
    "buffer_load_dwordx4 v235, s[24:27], s53 offen offset:1024  lds\n"
    "v_mfma_f32_16x16x32_bf16 v[56:59], a[104:107], v[136:139], v[56:59]\n"
    "s_waitcnt lgkmcnt(8)\n"
    "v_cvt_pk_bf16_f32 v172, v196, v197\n"
    "v_exp_f32 v199, v199\n"
    "v_mfma_f32_16x16x32_bf16 v[56:59], a[108:111], v[152:155], v[56:59]\n"
    "v_cvt_pk_bf16_f32 v173, v198, v199\n"
    "v_exp_f32 v200, v200\n"
    "v_mfma_f32_16x16x32_bf16 v[60:63], a[104:107], v[140:143], v[60:63]\n"
    "ds_read_b64_tr_b16 a[104:105], v230 offset:33280\n"
    "ds_read_b64_tr_b16 a[106:107], v230 offset:37376\n"
    "v_mfma_f32_16x16x32_bf16 v[60:63], a[108:111], v[156:159], v[60:63]\n"
    "buffer_load_dwordx4 v236, s[24:27], s53 offen offset:2048  lds\n"
    "v_mfma_f32_16x16x32_bf16 v[196:199], a[64:67], a[48:51], v[220:223]\n"
    "ds_read_b64_tr_b16 a[108:109], v230 offset:41472\n"
    "ds_read_b64_tr_b16 a[110:111], v230 offset:45568\n"
    "v_mfma_f32_16x16x32_bf16 v[196:199], a[68:71], a[52:55], v[196:199]\n"
    "ds_read_b64_tr_b16 a[64:65], v229 offset:49408\n"
    "ds_read_b64_tr_b16 a[66:67], v229 offset:53504\n"
    "v_mfma_f32_16x16x32_bf16 v[196:199], a[72:75], a[56:59], v[196:199]\n"
    "ds_read_b64_tr_b16 a[68:69], v229 offset:57600\n"
    "ds_read_b64_tr_b16 a[70:71], v229 offset:61696\n"
    "v_mfma_f32_16x16x32_bf16 v[196:199], a[76:79], a[60:63], v[196:199]\n"
    "s_waitcnt lgkmcnt(10)\n"
    "buffer_load_dwordx4 v237, s[24:27], s53 offen offset:3072  lds\n"
    "v_mfma_f32_16x16x32_bf16 v[72:75], a[96:99], v[136:139], v[72:75]\n"
    "s_waitcnt lgkmcnt(8)\n"
    "ds_read_b64_tr_b16 a[72:73], v230 offset:49408\n"
    "ds_read_b64_tr_b16 a[74:75], v230 offset:53504\n"
    "v_mfma_f32_16x16x32_bf16 v[72:75], a[100:103], v[152:155], v[72:75]\n"
    "ds_read_b64_tr_b16 a[76:77], v230 offset:57600\n"
    "ds_read_b64_tr_b16 a[78:79], v230 offset:61696\n"
    "v_mfma_f32_16x16x32_bf16 v[76:79], a[96:99], v[140:143], v[76:79]\n"
    "ds_read_b64_tr_b16 a[96:97], v229 offset:33536\n"
    "ds_read_b64_tr_b16 a[98:99], v229 offset:37632\n"
    "v_mfma_f32_16x16x32_bf16 v[76:79], a[100:103], v[156:159], v[76:79]\n"
    "s_waitcnt vmcnt(8)\n"
    "buffer_store_dword v246, v247, s[36:39], 0 offen offset:128 sc1\n"
    "v_mfma_f32_16x16x32_bf16 a[124:127], a[112:115], v[136:139], 0\n"
    "ds_read_b64_tr_b16 a[100:101], v229 offset:41728\n"
    "ds_read_b64_tr_b16 a[102:103], v229 offset:45824\n"
    "v_mfma_f32_16x16x32_bf16 a[124:127], a[112:115], v[152:155], a[124:127]\n"
    "v_cmp_eq_u32 vcc, s66, v249\n"
    "v_exp_f32 v201, v201\n"
    "v_exp_f32 v202, v202\n"
    "s_and_b32 s55, vcc_lo, 4369\n"
    "s_cmp_eq_u32 s55, 4369\n"
    "s_cbranch_scc0 Lsp0%=\n"
    "Lok0%=:\n"
    "v_cvt_pk_bf16_f32 v170, v200, v201\n"
    "v_exp_f32 v203, v203\n"
    "s_nop 0\n"
    "v_cvt_pk_bf16_f32 v171, v202, v203\n"
    "v_mfma_f32_16x16x32_bf16 v[200:203], a[80:83], a[32:35], v[216:219]\n"
    "v_exp_f32 v204, v204\n"
    "v_mfma_f32_16x16x32_bf16 v[200:203], a[84:87], a[36:39], v[200:203]\n"
    "v_exp_f32 v205, v205\n"
    "v_mfma_f32_16x16x32_bf16 v[200:203], a[88:91], a[40:43], v[200:203]\n"
    "s_waitcnt lgkmcnt(14)\n"
    "v_exp_f32 v206, v206\n"
    "v_mfma_f32_16x16x32_bf16 v[200:203], a[92:95], a[44:47], v[200:203]\n"
    "s_xor_b32 s91, s89, 0\n"
    "buffer_load_dwordx4 v[0:3], v233, s[28:31], s91 offen offset:0 sc1\n"
    "v_mfma_f32_16x16x32_bf16 v[88:91], a[104:107], v[136:139], v[88:91]\n"
    "s_waitcnt lgkmcnt(12)\n"
    "v_cvt_pk_bf16_f32 v174, v204, v205\n"
    "v_exp_f32 v207, v207\n"
    "v_mfma_f32_16x16x32_bf16 v[88:91], a[108:111], v[152:155], v[88:91]\n"
    "v_cvt_pk_bf16_f32 v175, v206, v207\n"
    "v_exp_f32 v192, v192\n"
    "v_mfma_f32_16x16x32_bf16 v[92:95], a[104:107], v[140:143], v[92:95]\n"
    "ds_read_b64_tr_b16 a[104:105], v230 offset:33536\n"
    "ds_read_b64_tr_b16 a[106:107], v230 offset:37632\n"
    "v_mfma_f32_16x16x32_bf16 v[92:95], a[108:111], v[156:159], v[92:95]\n"
    "buffer_load_dwordx4 v[16:19], v233, s[28:31], s91 offen offset:1024 sc1\n"
    "v_mfma_f32_16x16x32_bf16 v[204:207], a[80:83], a[48:51], v[220:223]\n"
    "ds_read_b64_tr_b16 a[108:109], v230 offset:41728\n"
    "ds_read_b64_tr_b16 a[110:111], v230 offset:45824\n"
    "v_mfma_f32_16x16x32_bf16 v[204:207], a[84:87], a[52:55], v[204:207]\n"
    "v_exp_f32 v193, v193\n"
    "v_mfma_f32_16x16x32_bf16 v[204:207], a[88:91], a[56:59], v[204:207]\n"
    "v_exp_f32 v194, v194\n"
    "v_mfma_f32_16x16x32_bf16 v[204:207], a[92:95], a[60:63], v[204:207]\n"
    "s_waitcnt lgkmcnt(6)\n"
    "buffer_load_dwordx4 v[32:35], v233, s[28:31], s91 offen offset:2048 sc1\n"
    "v_mfma_f32_16x16x32_bf16 v[104:107], a[96:99], v[136:139], v[104:107]\n"
    "s_waitcnt lgkmcnt(4)\n"
    "v_cvt_pk_bf16_f32 v184, v192, v193\n"
    "v_exp_f32 v195, v195\n"
    "v_mfma_f32_16x16x32_bf16 v[104:107], a[100:103], v[152:155], v[104:107]\n"
    "v_cvt_pk_bf16_f32 v185, v194, v195\n"
    "v_exp_f32 v196, v196\n"
    "v_mfma_f32_16x16x32_bf16 v[108:111], a[96:99], v[140:143], v[108:111]\n"
    "ds_read_b64_tr_b16 a[96:97], v229 offset:49152\n"
    "ds_read_b64_tr_b16 a[98:99], v229 offset:53248\n"
    "v_mfma_f32_16x16x32_bf16 v[108:111], a[100:103], v[156:159], v[108:111]\n"
    "s_waitcnt lgkmcnt(4)\n"
    "buffer_load_dwordx4 v[48:51], v233, s[28:31], s91 offen offset:3072 sc1\n"
    "v_mfma_f32_16x16x32_bf16 v[120:123], a[104:107], v[136:139], v[120:123]\n"
    "s_waitcnt lgkmcnt(2)\n"
    "ds_read_b64_tr_b16 a[100:101], v229 offset:57344\n"
    "ds_read_b64_tr_b16 a[102:103], v229 offset:61440\n"
    "v_mfma_f32_16x16x32_bf16 v[120:123], a[108:111], v[152:155], v[120:123]\n"
    "ds_read_b64_tr_b16 a[80:81], v229 offset:49664\n"
    "ds_read_b64_tr_b16 a[82:83], v229 offset:53760\n"
    "v_mfma_f32_16x16x32_bf16 v[124:127], a[104:107], v[140:143], v[124:127]\n"
    "ds_read_b64_tr_b16 a[84:85], v229 offset:57856\n"
    "ds_read_b64_tr_b16 a[86:87], v229 offset:61952\n"
    "v_mfma_f32_16x16x32_bf16 v[124:127], a[108:111], v[156:159], v[124:127]\n"
    "s_xor_b32 s91, s89, 16384\n"
    "buffer_load_dwordx4 v[64:67], v233, s[28:31], s91 offen offset:0 sc1\n"
    "v_mfma_f32_16x16x32_bf16 a[124:127], a[112:115], v[168:171], a[124:127]\n"
    "s_add_u32 s52, s92, 1048576\n"
    "ds_read_b64_tr_b16 a[88:89], v230 offset:49664\n"
    "ds_read_b64_tr_b16 a[90:91], v230 offset:53760\n"
    "ds_read_b64_tr_b16 a[92:93], v230 offset:57856\n"
    "ds_read_b64_tr_b16 a[94:95], v230 offset:61952\n"
    "ds_read_b64_tr_b16 a[104:105], v230 offset:49152\n"
    "ds_read_b64_tr_b16 a[106:107], v230 offset:53248\n"
    "ds_read_b64_tr_b16 a[108:109], v230 offset:57344\n"
    "ds_read_b64_tr_b16 a[110:111], v230 offset:61440\n"
    "ds_read_b64_tr_b16 v[128:129], v229 offset:49920\n"
    "ds_read_b64_tr_b16 v[130:131], v229 offset:54016\n"
    "ds_read_b64_tr_b16 v[132:133], v229 offset:58112\n"
    "ds_read_b64_tr_b16 v[134:135], v229 offset:62208\n"
    "ds_read_b64_tr_b16 v[144:145], v230 offset:49920\n"
    "ds_read_b64_tr_b16 v[146:147], v230 offset:54016\n"
    "ds_read_b64_tr_b16 v[148:149], v230 offset:58112\n"
    "ds_read_b64_tr_b16 v[150:151], v230 offset:62208\n"
    "v_exp_f32 v197, v197\n"
    "v_exp_f32 v198, v198\n"
    "v_cvt_pk_bf16_f32 v188, v196, v197\n"
    "v_exp_f32 v199, v199\n"
    "s_nop 0\n"
    "v_cvt_pk_bf16_f32 v189, v198, v199\n"
    "v_exp_f32 v200, v200\n"
    "v_exp_f32 v201, v201\n"
    "v_exp_f32 v202, v202\n"
    "v_cvt_pk_bf16_f32 v186, v200, v201\n"
    "v_exp_f32 v203, v203\n"
    "s_nop 0\n"
    "v_cvt_pk_bf16_f32 v187, v202, v203\n"
    "v_exp_f32 v204, v204\n"
    "v_exp_f32 v205, v205\n"
    "v_mfma_f32_16x16x32_bf16 a[124:127], a[112:115], v[184:187], a[124:127]\n"
    "s_waitcnt lgkmcnt(15)\n"
    "v_exp_f32 v206, v206\n"
    "v_mfma_f32_16x16x32_bf16 v[8:11], a[96:99], v[168:171], v[8:11]\n"
    "s_waitcnt lgkmcnt(15)\n"
    "v_cvt_pk_bf16_f32 v190, v204, v205\n"
    "v_exp_f32 v207, v207\n"
    "v_mfma_f32_16x16x32_bf16 v[8:11], a[100:103], v[184:187], v[8:11]\n"
    "s_waitcnt lgkmcnt(10)\n"
    "v_cvt_pk_bf16_f32 v191, v206, v207\n"
    "v_mfma_f32_16x16x32_bf16 v[24:27], a[104:107], v[168:171], v[24:27]\n"
    "s_waitcnt lgkmcnt(8)\n"
    "v_mfma_f32_16x16x32_bf16 v[24:27], a[108:111], v[184:187], v[24:27]\n"
    "buffer_load_dwordx4 v[80:83], v233, s[28:31], s91 offen offset:1024 sc1\n"
    "v_mfma_f32_16x16x32_bf16 v[40:43], a[64:67], v[168:171], v[40:43]\n"
    "v_accvgpr_read_b32 v250, a124\n"
    "v_add_f32 v250, v226, v250\n"
    "v_mfma_f32_16x16x32_bf16 v[40:43], a[68:71], v[184:187], v[40:43]\n"
    "v_rcp_f32 v226, v250\n"
    "v_mfma_f32_16x16x32_bf16 v[56:59], a[72:75], v[168:171], v[56:59]\n"
    "v_mul_f32 v8, v226, v8\n"
    "v_mul_f32 v9, v226, v9\n"
    "v_mfma_f32_16x16x32_bf16 v[56:59], a[76:79], v[184:187], v[56:59]\n"
    "buffer_load_dwordx4 v[96:99], v233, s[28:31], s91 offen offset:2048 sc1\n"
    "v_mfma_f32_16x16x32_bf16 v[72:75], a[80:83], v[168:171], v[72:75]\n"
    "v_mul_f32 v10, v226, v10\n"
    "v_mul_f32 v11, v226, v11\n"
    "v_mfma_f32_16x16x32_bf16 v[72:75], a[84:87], v[184:187], v[72:75]\n"
    "v_cvt_pk_bf16_f32 v8, v8, v9\n"
    "v_cvt_pk_bf16_f32 v9, v10, v11\n"
    "v_mfma_f32_16x16x32_bf16 v[88:91], a[88:91], v[168:171], v[88:91]\n"
    "v_mul_f32 v24, v226, v24\n"
    "v_mul_f32 v25, v226, v25\n"
    "v_mfma_f32_16x16x32_bf16 v[88:91], a[92:95], v[184:187], v[88:91]\n"
    "buffer_load_dwordx4 v[112:115], v233, s[28:31], s91 offen offset:3072 sc1\n"
    "v_mfma_f32_16x16x32_bf16 a[120:123], a[112:115], v[172:175], a[120:123]\n"
    "s_waitcnt lgkmcnt(6)\n"
    "v_mul_f32 v26, v226, v26\n"
    "v_mul_f32 v27, v226, v27\n"
    "v_mfma_f32_16x16x32_bf16 a[120:123], a[112:115], v[188:191], a[120:123]\n"
    "s_xor_b32 s91, s89, 4096\n"
    "buffer_load_dwordx4 v[4:7], v233, s[28:31], s91 offen offset:0 sc1\n"
    "v_mfma_f32_16x16x32_bf16 v[104:107], v[128:131], v[168:171], v[104:107]\n"
    "s_waitcnt lgkmcnt(4)\n"
    "v_cvt_pk_bf16_f32 v10, v24, v25\n"
    "v_cvt_pk_bf16_f32 v11, v26, v27\n"
    "v_mfma_f32_16x16x32_bf16 v[104:107], v[132:135], v[184:187], v[104:107]\n"
    "s_waitcnt lgkmcnt(2)\n"
    "v_mul_f32 v40, v226, v40\n"
    "v_mul_f32 v41, v226, v41\n"
    "v_mfma_f32_16x16x32_bf16 v[120:123], v[144:147], v[168:171], v[120:123]\n"
    "s_waitcnt lgkmcnt(0)\n"
    "v_accvgpr_read_b32 v227, a120\n"
    "v_add_f32 v250, v250, v227\n"
    "v_mfma_f32_16x16x32_bf16 v[120:123], v[148:151], v[184:187], v[120:123]\n"
    "buffer_load_dwordx4 v[20:23], v233, s[28:31], s91 offen offset:1024 sc1\n"
    "v_mfma_f32_16x16x32_bf16 v[44:47], a[64:67], v[172:175], v[44:47]\n"
    "ds_write_b32 v251, v250 offset:1024\n"
    "v_rcp_f32 v227, v227\n"
    "v_mfma_f32_16x16x32_bf16 v[44:47], a[68:71], v[188:191], v[44:47]\n"
    "v_mul_f32 v42, v226, v42\n"
    "v_mul_f32 v43, v226, v43\n"
    "v_mfma_f32_16x16x32_bf16 v[60:63], a[72:75], v[172:175], v[60:63]\n"
    "v_cvt_pk_bf16_f32 v40, v40, v41\n"
    "v_cvt_pk_bf16_f32 v41, v42, v43\n"
    "v_mfma_f32_16x16x32_bf16 v[60:63], a[76:79], v[188:191], v[60:63]\n"
    "buffer_load_dwordx4 v[36:39], v233, s[28:31], s91 offen offset:2048 sc1\n"
    "v_mfma_f32_16x16x32_bf16 v[12:15], a[96:99], v[172:175], v[12:15]\n"
    "v_mul_f32 v56, v226, v56\n"
    "v_mul_f32 v57, v226, v57\n"
    "v_mfma_f32_16x16x32_bf16 v[12:15], a[100:103], v[188:191], v[12:15]\n"
    "v_mul_f32 v58, v226, v58\n"
    "v_mul_f32 v59, v226, v59\n"
    "v_mfma_f32_16x16x32_bf16 v[28:31], a[104:107], v[172:175], v[28:31]\n"
    "v_cvt_pk_bf16_f32 v42, v56, v57\n"
    "v_cvt_pk_bf16_f32 v43, v58, v59\n"
    "v_mfma_f32_16x16x32_bf16 v[28:31], a[108:111], v[188:191], v[28:31]\n"
    "s_waitcnt lgkmcnt(0)\n"
    "s_barrier\n"
    "buffer_load_dwordx4 v[52:55], v233, s[28:31], s91 offen offset:3072 sc1\n"
    "v_mfma_f32_16x16x32_bf16 v[76:79], a[80:83], v[172:175], v[76:79]\n"
    "ds_read_b128 v[208:211], v252 offset:1024\n"
    "v_mul_f32 v72, v226, v72\n"
    "v_mfma_f32_16x16x32_bf16 v[76:79], a[84:87], v[188:191], v[76:79]\n"
    "Lpg13%=:\n"
    "s_cbranch_execz Lwb2%=\n"
    "v_mul_f32 v73, v226, v73\n"
    "v_mul_f32 v74, v226, v74\n"
    "v_mfma_f32_16x16x32_bf16 v[92:95], a[88:91], v[172:175], v[92:95]\n"
    "v_mul_f32 v75, v226, v75\n"
    "v_cvt_pk_bf16_f32 v72, v72, v73\n"
    "v_mfma_f32_16x16x32_bf16 v[92:95], a[92:95], v[188:191], v[92:95]\n"
    "s_xor_b32 s91, s89, 20480\n"
    "buffer_load_dwordx4 v[68:71], v233, s[28:31], s91 offen offset:0 sc1\n"
    "v_mfma_f32_16x16x32_bf16 v[108:111], v[128:131], v[172:175], v[108:111]\n"
    "v_cvt_pk_bf16_f32 v73, v74, v75\n"
    "v_mul_f32 v88, v226, v88\n"
    "v_mfma_f32_16x16x32_bf16 v[108:111], v[132:135], v[188:191], v[108:111]\n"
    "v_mul_f32 v89, v226, v89\n"
    "v_mul_f32 v90, v226, v90\n"
    "v_mfma_f32_16x16x32_bf16 v[124:127], v[144:147], v[172:175], v[124:127]\n"
    "v_mul_f32 v91, v226, v91\n"
    "v_cvt_pk_bf16_f32 v74, v88, v89\n"
    "v_mfma_f32_16x16x32_bf16 v[124:127], v[148:151], v[188:191], v[124:127]\n"
    "s_waitcnt lgkmcnt(0)\n"
    "buffer_load_dwordx4 v[84:87], v233, s[28:31], s91 offen offset:1024 sc1\n"
    "v_add_f32 v208, v208, v209\n"
    "v_add_f32 v210, v210, v211\n"
    "v_add_f32 v208, v208, v210\n"
    "v_cmp_ngt_f32 vcc, 0x53800000, v208\n"
    "v_cvt_pk_bf16_f32 v75, v90, v91\n"
    "v_mul_f32 v104, v226, v104\n"
    "s_and_b64 vcc, vcc, s[64:65]\n"
    "s_cbranch_scc1 Lfall%=\n"
    "v_mul_f32 v105, v226, v105\n"
    "v_mul_f32 v106, v226, v106\n"
    "v_mul_f32 v107, v226, v107\n"
    "v_cvt_pk_bf16_f32 v104, v104, v105\n"
    "v_cvt_pk_bf16_f32 v105, v106, v107\n"
    "v_mul_f32 v120, v226, v120\n"
    "v_mul_f32 v121, v226, v121\n"
    "v_mul_f32 v122, v226, v122\n"
    "v_mul_f32 v123, v226, v123\n"
    "v_cvt_pk_bf16_f32 v106, v120, v121\n"
    "v_cvt_pk_bf16_f32 v107, v122, v123\n"
    "s_waitcnt vmcnt(13)\n"
    "v_mfma_f32_16x16x32_bf16 v[128:131], a[136:139], v[0:3], 0\n"
    "s_waitcnt vmcnt(12)\n"
    "ds_read_b64_tr_b16 a[40:41], v238 offset:256\n"
    "v_mfma_f32_16x16x32_bf16 v[128:131], a[168:171], v[16:19], v[128:131]\n"
    "s_waitcnt vmcnt(11)\n"
    "ds_read_b64_tr_b16 a[42:43], v238 offset:16640\n"
    "v_add_u32 v228, s51, v233\n"
    "v_mov_b32 v254, 2147418112\n"
    "v_mfma_f32_16x16x32_bf16 v[128:131], a[200:203], v[32:35], v[128:131]\n"
    "s_waitcnt vmcnt(10)\n"
    "ds_read_b64_tr_b16 a[74:75], v242 offset:256\n"
    "v_cmp_gt_u32 vcc, 2, v253\n"
    "v_add_u32 v229, s85, v233\n"
    "v_mfma_f32_16x16x32_bf16 v[128:131], a[232:235], v[48:51], v[128:131]\n"
    "s_waitcnt vmcnt(9)\n"
    "ds_read_b64_tr_b16 a[104:105], v242 offset:16640\n"
    "v_add_u32 v230, s86, v233\n"
    "v_add_u32 v231, s87, v233\n"
    "v_mfma_f32_16x16x32_bf16 v[128:131], a[8:11], v[64:67], v[128:131]\n"
    "ds_read_b64_tr_b16 a[44:45], v239 offset:256\n"
    "v_lshl_add_u32 v232, v253, 7, s84\n"
    "v_cndmask_b32 v232, v254, v232, vcc\n"
    "v_mfma_f32_16x16x32_bf16 v[132:135], a[12:15], v[64:67], 0\n"
    "ds_read_b64_tr_b16 a[46:47], v239 offset:16640\n"
    "buffer_load_dwordx4 v[100:103], v233, s[28:31], s91 offen offset:2048 sc1\n"
    "v_mul_f32 v12, v227, v12\n"
    "v_mfma_f32_16x16x32_bf16 v[132:135], a[236:239], v[48:51], v[132:135]\n"
    "ds_read_b64_tr_b16 a[78:79], v243 offset:256\n"
    "v_mul_f32 v13, v227, v13\n"
    "v_mfma_f32_16x16x32_bf16 v[132:135], a[204:207], v[32:35], v[132:135]\n"
    "ds_read_b64_tr_b16 a[108:109], v243 offset:16640\n"
    "v_mul_f32 v14, v227, v14\n"
    "v_mfma_f32_16x16x32_bf16 v[132:135], a[172:175], v[16:19], v[132:135]\n"
    "ds_read_b64_tr_b16 a[48:49], v238 offset:512\n"
    "v_mul_f32 v15, v227, v15\n"
    "v_mfma_f32_16x16x32_bf16 v[132:135], a[140:143], v[0:3], v[132:135]\n"
    "ds_read_b64_tr_b16 a[50:51], v238 offset:16896\n"
    "buffer_load_dwordx4 v[116:119], v233, s[28:31], s91 offen offset:3072 sc1\n"
    "v_cvt_pk_bf16_f32 v12, v12, v13\n"
    "v_mfma_f32_16x16x32_bf16 v[136:139], a[144:147], v[0:3], 0\n"
    "ds_read_b64_tr_b16 a[82:83], v242 offset:512\n"
    "v_cvt_pk_bf16_f32 v13, v14, v15\n"
    "v_mfma_f32_16x16x32_bf16 v[136:139], a[176:179], v[16:19], v[136:139]\n"
    "ds_read_b64_tr_b16 a[112:113], v242 offset:16896\n"
    "v_mul_f32 v28, v227, v28\n"
    "v_mfma_f32_16x16x32_bf16 v[136:139], a[208:211], v[32:35], v[136:139]\n"
    "ds_read_b64_tr_b16 a[52:53], v239 offset:512\n"
    "buffer_load_dword v250, v245, s[36:39], 0 offen sc1\n"
    "v_mul_f32 v29, v227, v29\n"
    "v_mfma_f32_16x16x32_bf16 v[136:139], a[240:243], v[48:51], v[136:139]\n"
    "ds_read_b64_tr_b16 a[54:55], v239 offset:16896\n"
    "buffer_load_dword v249, v244, s[36:39], 0 offen offset:128 sc1\n"
    "v_mul_f32 v30, v227, v30\n"
    "v_mfma_f32_16x16x32_bf16 v[136:139], a[16:19], v[64:67], v[136:139]\n"
    "s_waitcnt vmcnt(19)\n"
    "ds_read_b64_tr_b16 a[86:87], v243 offset:512\n"
    "ds_read_b64_tr_b16 a[72:73], v240 offset:16640\n"
    "v_mfma_f32_16x16x32_bf16 v[140:143], a[20:23], v[64:67], 0\n"
    "ds_read_b64_tr_b16 a[116:117], v243 offset:16896\n"
    "ds_read_b64_tr_b16 a[106:107], v238 offset:33024\n"
    "buffer_store_dwordx4 v[8:11], v233, s[28:31], s52 offen offset:0 sc1\n"
    "v_mfma_f32_16x16x32_bf16 v[140:143], a[244:247], v[48:51], v[140:143]\n"
    "ds_read_b64_tr_b16 a[56:57], v238 offset:768\n"
    "ds_read_b64_tr_b16 a[76:77], v241 offset:16640\n"
    "v_mfma_f32_16x16x32_bf16 v[140:143], a[212:215], v[32:35], v[140:143]\n"
    "ds_read_b64_tr_b16 a[58:59], v238 offset:17152\n"
    "ds_read_b64_tr_b16 a[110:111], v239 offset:33024\n"
    "v_mfma_f32_16x16x32_bf16 v[140:143], a[180:183], v[16:19], v[140:143]\n"
    "ds_read_b64_tr_b16 a[90:91], v242 offset:768\n"
    "ds_read_b64_tr_b16 a[80:81], v240 offset:16896\n"
    "v_mfma_f32_16x16x32_bf16 v[140:143], a[148:151], v[0:3], v[140:143]\n"
    "ds_read_b64_tr_b16 a[120:121], v242 offset:17152\n"
    "ds_read_b64_tr_b16 a[114:115], v238 offset:33280\n"
    "v_mfma_f32_16x16x32_bf16 v[144:147], a[152:155], v[0:3], 0\n"
    "ds_read_b64_tr_b16 a[60:61], v239 offset:768\n"
    "ds_read_b64_tr_b16 a[84:85], v241 offset:16896\n"
    "v_mfma_f32_16x16x32_bf16 v[144:147], a[184:187], v[16:19], v[144:147]\n"
    "ds_read_b64_tr_b16 a[62:63], v239 offset:17152\n"
    "ds_read_b64_tr_b16 a[118:119], v239 offset:33280\n"
    "v_mfma_f32_16x16x32_bf16 v[144:147], a[216:219], v[32:35], v[144:147]\n"
    "ds_read_b64_tr_b16 a[94:95], v243 offset:768\n"
    "ds_read_b64_tr_b16 a[88:89], v240 offset:17152\n"
    "buffer_store_dwordx4 v[40:43], v233, s[28:31], s52 offen offset:1024 sc1\n"
    "v_mfma_f32_16x16x32_bf16 v[144:147], a[248:251], v[48:51], v[144:147]\n"
    "ds_read_b64_tr_b16 a[124:125], v243 offset:17152\n"
    "ds_read_b64_tr_b16 a[122:123], v238 offset:33536\n"
    "v_mfma_f32_16x16x32_bf16 v[144:147], a[24:27], v[64:67], v[144:147]\n"
    "ds_read_b64_tr_b16 a[32:33], v238 offset:0\n"
    "ds_read_b64_tr_b16 a[92:93], v241 offset:17152\n"
    "v_mfma_f32_16x16x32_bf16 v[148:151], a[28:31], v[64:67], 0\n"
    "ds_read_b64_tr_b16 a[34:35], v238 offset:16384\n"
    "ds_read_b64_tr_b16 a[126:127], v239 offset:33536\n"
    "v_mfma_f32_16x16x32_bf16 v[148:151], a[252:255], v[48:51], v[148:151]\n"
    "ds_read_b64_tr_b16 a[66:67], v242 offset:0\n"
    "ds_read_b64_tr_b16 a[64:65], v240 offset:16384\n"
    "v_mfma_f32_16x16x32_bf16 v[148:151], a[220:223], v[32:35], v[148:151]\n"
    "ds_read_b64_tr_b16 a[96:97], v242 offset:16384\n"
    "ds_read_b64_tr_b16 a[98:99], v238 offset:32768\n"
    "v_mfma_f32_16x16x32_bf16 v[148:151], a[188:191], v[16:19], v[148:151]\n"
    "ds_read_b64_tr_b16 a[36:37], v239 offset:0\n"
    "ds_read_b64_tr_b16 a[68:69], v241 offset:16384\n"
    "v_mfma_f32_16x16x32_bf16 v[148:151], a[156:159], v[0:3], v[148:151]\n"
    "ds_read_b64_tr_b16 a[38:39], v239 offset:16384\n"
    "ds_read_b64_tr_b16 a[102:103], v239 offset:32768\n"
    "buffer_store_dwordx4 v[72:75], v233, s[28:31], s52 offen offset:2048 sc1\n"
    "v_mfma_f32_16x16x32_bf16 v[152:155], a[128:131], v[0:3], 0\n"
    "ds_read_b64_tr_b16 a[70:71], v243 offset:0\n"
    "v_mul_f32 v31, v227, v31\n"
    "v_mfma_f32_16x16x32_bf16 v[152:155], a[160:163], v[16:19], v[152:155]\n"
    "ds_read_b64_tr_b16 a[100:101], v243 offset:16384\n"
    "v_cvt_pk_bf16_f32 v14, v28, v29\n"
    "v_mfma_f32_16x16x32_bf16 v[152:155], a[192:195], v[32:35], v[152:155]\n"
    "v_cvt_pk_bf16_f32 v15, v30, v31\n"
    "v_mul_f32 v44, v227, v44\n"
    "v_mfma_f32_16x16x32_bf16 v[152:155], a[224:227], v[48:51], v[152:155]\n"
    "v_mul_f32 v45, v227, v45\n"
    "v_mul_f32 v46, v227, v46\n"
    "v_mfma_f32_16x16x32_bf16 v[152:155], a[0:3], v[64:67], v[152:155]\n"
    "v_mul_f32 v47, v227, v47\n"
    "v_cvt_pk_bf16_f32 v44, v44, v45\n"
    "v_mfma_f32_16x16x32_bf16 v[156:159], a[4:7], v[64:67], 0\n"
    "v_cvt_pk_bf16_f32 v45, v46, v47\n"
    "v_mul_f32 v60, v227, v60\n"
    "v_mfma_f32_16x16x32_bf16 v[156:159], a[228:231], v[48:51], v[156:159]\n"
    "buffer_store_dwordx4 v[104:107], v233, s[28:31], s52 offen offset:3072 sc1\n"
    "v_mul_f32 v61, v227, v61\n"
    "v_mul_f32 v62, v227, v62\n"
    "v_mfma_f32_16x16x32_bf16 v[156:159], a[196:199], v[32:35], v[156:159]\n"
    "v_mul_f32 v63, v227, v63\n"
    "v_cvt_pk_bf16_f32 v46, v60, v61\n"
    "v_mfma_f32_16x16x32_bf16 v[156:159], a[164:167], v[16:19], v[156:159]\n"
    "s_waitcnt vmcnt(14)\n"
    "v_cvt_pk_bf16_f32 v47, v62, v63\n"
    "v_mul_f32 v76, v227, v76\n"
    "v_mfma_f32_16x16x32_bf16 v[156:159], a[132:135], v[0:3], v[156:159]\n"
    "s_waitcnt lgkmcnt(15)\n"
    "v_mul_f32 v77, v227, v77\n"
    "v_mul_f32 v78, v227, v78\n"
    "v_mfma_f32_16x16x32_bf16 v[128:131], a[40:43], v[80:83], v[128:131]\n"
    "s_waitcnt lgkmcnt(15)\n"
    "v_mul_f32 v79, v227, v79\n"
    "v_cvt_pk_bf16_f32 v76, v76, v77\n"
    "v_mfma_f32_16x16x32_bf16 v[128:131], a[72:75], v[96:99], v[128:131]\n"
    "s_waitcnt lgkmcnt(15)\n"
    "v_cvt_pk_bf16_f32 v77, v78, v79\n"
    "v_mul_f32 v92, v227, v92\n"
    "v_mfma_f32_16x16x32_bf16 v[128:131], a[104:107], v[112:115], v[128:131]\n"
    "s_waitcnt lgkmcnt(15)\n"
    "v_mul_f32 v93, v227, v93\n"
    "v_mul_f32 v94, v227, v94\n"
    "v_mfma_f32_16x16x32_bf16 v[132:135], a[108:111], v[112:115], v[132:135]\n"
    "v_mul_f32 v95, v227, v95\n"
    "v_cvt_pk_bf16_f32 v78, v92, v93\n"
    "v_mfma_f32_16x16x32_bf16 v[132:135], a[76:79], v[96:99], v[132:135]\n"
    "v_cvt_pk_bf16_f32 v79, v94, v95\n"
    "v_mul_f32 v108, v227, v108\n"
    "v_mfma_f32_16x16x32_bf16 v[132:135], a[44:47], v[80:83], v[132:135]\n"
    "v_mul_f32 v109, v227, v109\n"
    "v_mul_f32 v110, v227, v110\n"
    "v_mfma_f32_16x16x32_bf16 v[136:139], a[48:51], v[80:83], v[136:139]\n"
    "s_waitcnt lgkmcnt(15)\n"
    "v_mul_f32 v111, v227, v111\n"
    "v_cvt_pk_bf16_f32 v108, v108, v109\n"
    "v_mfma_f32_16x16x32_bf16 v[136:139], a[80:83], v[96:99], v[136:139]\n"
    "s_waitcnt lgkmcnt(15)\n"
    "v_cvt_pk_bf16_f32 v109, v110, v111\n"
    "v_mul_f32 v124, v227, v124\n"
    "v_mfma_f32_16x16x32_bf16 v[136:139], a[112:115], v[112:115], v[136:139]\n"
    "s_waitcnt lgkmcnt(15)\n"
    "v_mul_f32 v125, v227, v125\n"
    "v_mul_f32 v126, v227, v126\n"
    "v_mfma_f32_16x16x32_bf16 v[140:143], a[116:119], v[112:115], v[140:143]\n"
    "v_mfma_f32_16x16x32_bf16 v[140:143], a[84:87], v[96:99], v[140:143]\n"
    "ds_write_b128 v228, v[128:131] offset:0\n"
    "v_mul_f32 v127, v227, v127\n"
    "v_mfma_f32_16x16x32_bf16 v[140:143], a[52:55], v[80:83], v[140:143]\n"
    "v_cvt_pk_bf16_f32 v110, v124, v125\n"
    "v_cvt_pk_bf16_f32 v111, v126, v127\n"
    "v_mfma_f32_16x16x32_bf16 v[144:147], a[56:59], v[80:83], v[144:147]\n"
    "s_waitcnt lgkmcnt(15)\n"
    "v_mfma_f32_16x16x32_bf16 v[144:147], a[88:91], v[96:99], v[144:147]\n"
    "s_waitcnt lgkmcnt(15)\n"
    "ds_write_b128 v228, v[132:135] offset:1024\n"
    "v_mfma_f32_16x16x32_bf16 v[144:147], a[120:123], v[112:115], v[144:147]\n"
    "s_waitcnt lgkmcnt(12)\n"
    "v_mfma_f32_16x16x32_bf16 v[148:151], a[124:127], v[112:115], v[148:151]\n"
    "v_mfma_f32_16x16x32_bf16 v[148:151], a[92:95], v[96:99], v[148:151]\n"
    "s_waitcnt vmcnt(4)\n"
    "ds_write_b128 v228, v[136:139] offset:2048\n"
    "v_cmp_eq_u32 vcc, s66, v249\n"
    "v_mfma_f32_16x16x32_bf16 v[148:151], a[60:63], v[80:83], v[148:151]\n"
    "s_waitcnt lgkmcnt(11)\n"
    "v_mfma_f32_16x16x32_bf16 v[152:155], a[32:35], v[80:83], v[152:155]\n"
    "s_and_b32 s55, vcc_lo, 4369\n"
    "v_mfma_f32_16x16x32_bf16 v[152:155], a[64:67], v[96:99], v[152:155]\n"
    "s_cmp_eq_u32 s55, 4369\n"
    "ds_write_b128 v228, v[140:143] offset:3072\n"
    "s_waitcnt lgkmcnt(10)\n"
    "v_mfma_f32_16x16x32_bf16 v[152:155], a[96:99], v[112:115], v[152:155]\n"
    "s_cbranch_scc0 Lsp1%=\n"
    "Lok1%=:\n"
    "s_waitcnt lgkmcnt(4)\n"
    "v_mfma_f32_16x16x32_bf16 v[156:159], a[100:103], v[112:115], v[156:159]\n"
    "s_xor_b32 s91, s89, 524288\n"
    "v_mfma_f32_16x16x32_bf16 v[156:159], a[68:71], v[96:99], v[156:159]\n"
    "ds_write_b128 v228, v[144:147] offset:16384\n"
    "v_mfma_f32_16x16x32_bf16 v[156:159], a[36:39], v[80:83], v[156:159]\n"
    "s_waitcnt vmcnt(6)\n"
    "buffer_load_dwordx4 v[192:195], v233, s[28:31], s91 offen offset:0 sc1\n"
    "v_mfma_f32_16x16x32_bf16 v[128:131], a[136:139], v[4:7], 0\n"
    "v_mfma_f32_16x16x32_bf16 v[128:131], a[168:171], v[20:23], v[128:131]\n"
    "ds_write_b128 v228, v[148:151] offset:17408\n"
    "v_mfma_f32_16x16x32_bf16 v[128:131], a[200:203], v[36:39], v[128:131]\n"
    "v_mfma_f32_16x16x32_bf16 v[128:131], a[232:235], v[52:55], v[128:131]\n"
    "buffer_load_dwordx4 v[196:199], v233, s[28:31], s91 offen offset:1024 sc1\n"
    "v_mfma_f32_16x16x32_bf16 v[128:131], a[8:11], v[68:71], v[128:131]\n"
    "v_mfma_f32_16x16x32_bf16 v[128:131], a[40:43], v[84:87], v[128:131]\n"
    "v_mfma_f32_16x16x32_bf16 v[128:131], a[72:75], v[100:103], v[128:131]\n"
    "v_mfma_f32_16x16x32_bf16 v[128:131], a[104:107], v[116:119], v[128:131]\n"
    "buffer_load_dwordx4 v[200:203], v233, s[28:31], s91 offen offset:2048 sc1\n"
    "v_mfma_f32_16x16x32_bf16 v[132:135], a[108:111], v[116:119], 0\n"
    "v_mfma_f32_16x16x32_bf16 v[132:135], a[76:79], v[100:103], v[132:135]\n"
    "s_waitcnt lgkmcnt(0)\n"
    "v_mfma_f32_16x16x32_bf16 v[132:135], a[44:47], v[84:87], v[132:135]\n"
    "s_barrier\n"
    "ds_read_b128 v[168:171], v229 offset:0\n"
    "ds_read_b128 v[172:175], v229 offset:1024\n"
    "v_mfma_f32_16x16x32_bf16 v[132:135], a[12:15], v[68:71], v[132:135]\n"
    "ds_read_b128 v[176:179], v230 offset:2048\n"
    "ds_read_b128 v[180:183], v230 offset:3072\n"
    "v_mfma_f32_16x16x32_bf16 v[132:135], a[236:239], v[52:55], v[132:135]\n"
    "ds_read_b128 v[184:187], v231 offset:16384\n"
    "ds_read_b128 v[188:191], v231 offset:17408\n"
    "v_mfma_f32_16x16x32_bf16 v[132:135], a[204:207], v[36:39], v[132:135]\n"
    "buffer_load_dwordx4 v[204:207], v233, s[28:31], s91 offen offset:3072 sc1\n"
    "v_mfma_f32_16x16x32_bf16 v[132:135], a[172:175], v[20:23], v[132:135]\n"
    "v_mfma_f32_16x16x32_bf16 v[132:135], a[140:143], v[4:7], v[132:135]\n"
    "ds_write_b128 v228, v[128:131] offset:32768\n"
    "v_mfma_f32_16x16x32_bf16 v[136:139], a[144:147], v[4:7], 0\n"
    "s_xor_b32 s91, s89, 540672\n"
    "v_mfma_f32_16x16x32_bf16 v[136:139], a[176:179], v[20:23], v[136:139]\n"
    "buffer_load_dwordx4 v[208:211], v233, s[28:31], s91 offen offset:0 sc1\n"
    "v_mfma_f32_16x16x32_bf16 v[136:139], a[208:211], v[36:39], v[136:139]\n"
    "v_mfma_f32_16x16x32_bf16 v[136:139], a[240:243], v[52:55], v[136:139]\n"
    "s_waitcnt lgkmcnt(4)\n"
    "v_add_f32 v168, v168, v176\n"
    "v_add_f32 v169, v169, v177\n"
    "v_mfma_f32_16x16x32_bf16 v[136:139], a[16:19], v[68:71], v[136:139]\n"
    "v_add_f32 v170, v170, v178\n"
    "v_add_f32 v171, v171, v179\n"
    "v_mfma_f32_16x16x32_bf16 v[136:139], a[48:51], v[84:87], v[136:139]\n"
    "buffer_load_dwordx4 v[212:215], v233, s[28:31], s91 offen offset:1024 sc1\n"
    "v_mfma_f32_16x16x32_bf16 v[136:139], a[80:83], v[100:103], v[136:139]\n"
    "s_waitcnt lgkmcnt(3)\n"
    "v_add_f32 v172, v172, v180\n"
    "v_add_f32 v173, v173, v181\n"
    "v_mfma_f32_16x16x32_bf16 v[136:139], a[112:115], v[116:119], v[136:139]\n"
    "ds_write_b128 v228, v[132:135] offset:33792\n"
    "v_add_f32 v174, v174, v182\n"
    "v_mfma_f32_16x16x32_bf16 v[140:143], a[116:119], v[116:119], 0\n"
    "s_waitcnt lgkmcnt(3)\n"
    "v_add_f32 v175, v175, v183\n"
    "v_add_f32 v152, v152, v184\n"
    "v_mfma_f32_16x16x32_bf16 v[140:143], a[84:87], v[100:103], v[140:143]\n"
    "buffer_load_dwordx4 v[216:219], v233, s[28:31], s91 offen offset:2048 sc1\n"
    "v_mfma_f32_16x16x32_bf16 v[140:143], a[52:55], v[84:87], v[140:143]\n"
    "v_add_f32 v153, v153, v185\n"
    "v_add_f32 v154, v154, v186\n"
    "v_mfma_f32_16x16x32_bf16 v[140:143], a[20:23], v[68:71], v[140:143]\n"
    "s_waitcnt lgkmcnt(2)\n"
    "v_add_f32 v155, v155, v187\n"
    "v_add_f32 v156, v156, v188\n"
    "v_mfma_f32_16x16x32_bf16 v[140:143], a[244:247], v[52:55], v[140:143]\n"
    "v_add_f32 v157, v157, v189\n"
    "v_add_f32 v158, v158, v190\n"
    "v_mfma_f32_16x16x32_bf16 v[140:143], a[212:215], v[36:39], v[140:143]\n"
    "buffer_load_dwordx4 v[220:223], v233, s[28:31], s91 offen offset:3072 sc1\n"
    "v_mfma_f32_16x16x32_bf16 v[140:143], a[180:183], v[20:23], v[140:143]\n"
    "v_add_f32 v159, v159, v191\n"
    "v_add_f32 v152, v152, v168\n"
    "v_mfma_f32_16x16x32_bf16 v[140:143], a[148:151], v[4:7], v[140:143]\n"
    "ds_write_b128 v228, v[136:139] offset:34816\n"
    "v_add_f32 v153, v153, v169\n"
    "v_mfma_f32_16x16x32_bf16 v[144:147], a[152:155], v[4:7], 0\n"
    "v_add_f32 v154, v154, v170\n"
    "v_add_f32 v155, v155, v171\n"
    "v_mfma_f32_16x16x32_bf16 v[144:147], a[184:187], v[20:23], v[144:147]\n"
    "s_waitcnt vmcnt(8)\n"
    "buffer_store_dword v246, v247, s[36:39], 0 offen offset:256 sc1\n"
    "v_mfma_f32_16x16x32_bf16 v[144:147], a[216:219], v[36:39], v[144:147]\n"
    "s_waitcnt vmcnt(14)\n"
    "v_add_f32 v156, v156, v172\n"
    "v_add_f32 v157, v157, v173\n"
    "v_cmp_ne_u32 vcc, v250, v246\n"
    "v_mfma_f32_16x16x32_bf16 v[144:147], a[248:251], v[52:55], v[144:147]\n"
    "v_add_f32 v158, v158, v174\n"
    "v_add_f32 v159, v159, v175\n"
    "v_mfma_f32_16x16x32_bf16 v[144:147], a[24:27], v[68:71], v[144:147]\n"
    "s_cbranch_vccnz Lrp%=\n"
    "Lrq%=:\n"
    "v_cvt_pk_bf16_f32 v24, v152, v153\n"
    "v_cvt_pk_bf16_f32 v25, v154, v155\n"
    "v_mfma_f32_16x16x32_bf16 v[144:147], a[56:59], v[84:87], v[144:147]\n"
    "s_xor_b32 s91, s89, 528384\n"
    "v_mfma_f32_16x16x32_bf16 v[144:147], a[88:91], v[100:103], v[144:147]\n"
    "v_cvt_pk_bf16_f32 v26, v156, v157\n"
    "v_cvt_pk_bf16_f32 v27, v158, v159\n"
    "v_mfma_f32_16x16x32_bf16 v[144:147], a[120:123], v[116:119], v[144:147]\n"
    "ds_write_b128 v228, v[140:143] offset:35840\n"
    "v_mfma_f32_16x16x32_bf16 v[148:151], a[124:127], v[116:119], 0\n"
    "buffer_load_dwordx4 v[0:3], v233, s[28:31], s91 offen offset:0 sc1\n"
    "v_mfma_f32_16x16x32_bf16 v[148:151], a[92:95], v[100:103], v[148:151]\n"
    "v_mfma_f32_16x16x32_bf16 v[148:151], a[60:63], v[84:87], v[148:151]\n"
    "v_mfma_f32_16x16x32_bf16 v[148:151], a[28:31], v[68:71], v[148:151]\n"
    "v_mfma_f32_16x16x32_bf16 v[148:151], a[252:255], v[52:55], v[148:151]\n"
    "v_mfma_f32_16x16x32_bf16 v[148:151], a[220:223], v[36:39], v[148:151]\n"
    "v_mfma_f32_16x16x32_bf16 v[148:151], a[188:191], v[20:23], v[148:151]\n"
    "buffer_load_dwordx4 v[16:19], v233, s[28:31], s91 offen offset:1024 sc1\n"
    "v_mfma_f32_16x16x32_bf16 v[148:151], a[156:159], v[4:7], v[148:151]\n"
    "ds_write_b128 v228, v[144:147] offset:49152\n"
    "v_mfma_f32_16x16x32_bf16 v[160:163], a[128:131], v[4:7], 0\n"
    "v_mfma_f32_16x16x32_bf16 v[160:163], a[160:163], v[20:23], v[160:163]\n"
    "v_mfma_f32_16x16x32_bf16 v[160:163], a[192:195], v[36:39], v[160:163]\n"
    "v_mfma_f32_16x16x32_bf16 v[160:163], a[224:227], v[52:55], v[160:163]\n"
    "v_mfma_f32_16x16x32_bf16 v[160:163], a[0:3], v[68:71], v[160:163]\n"
    "buffer_load_dwordx4 v[32:35], v233, s[28:31], s91 offen offset:2048 sc1\n"
    "v_mfma_f32_16x16x32_bf16 v[160:163], a[32:35], v[84:87], v[160:163]\n"
    "v_mfma_f32_16x16x32_bf16 v[160:163], a[64:67], v[100:103], v[160:163]\n"
    "v_mfma_f32_16x16x32_bf16 v[160:163], a[96:99], v[116:119], v[160:163]\n"
    "ds_write_b128 v228, v[148:151] offset:50176\n"
    "v_mfma_f32_16x16x32_bf16 v[164:167], a[100:103], v[116:119], 0\n"
    "v_mfma_f32_16x16x32_bf16 v[164:167], a[68:71], v[100:103], v[164:167]\n"
    "v_mfma_f32_16x16x32_bf16 v[164:167], a[36:39], v[84:87], v[164:167]\n"
    "buffer_load_dwordx4 v[48:51], v233, s[28:31], s91 offen offset:3072 sc1\n"
    "v_mfma_f32_16x16x32_bf16 v[164:167], a[4:7], v[68:71], v[164:167]\n"
    "v_mfma_f32_16x16x32_bf16 v[164:167], a[228:231], v[52:55], v[164:167]\n"
    "v_mfma_f32_16x16x32_bf16 v[164:167], a[196:199], v[36:39], v[164:167]\n"
    "v_mfma_f32_16x16x32_bf16 v[164:167], a[164:167], v[20:23], v[164:167]\n"
    "v_mfma_f32_16x16x32_bf16 v[164:167], a[132:135], v[4:7], v[164:167]\n"
    "s_waitcnt vmcnt(5)\n"
    "s_waitcnt lgkmcnt(0)\n"
    "v_mfma_f32_16x16x32_bf16 v[128:131], a[136:139], v[192:195], 0\n"
    "s_barrier\n"
    "ds_read_b128 v[168:171], v229 offset:32768\n"
    "ds_read_b128 v[172:175], v229 offset:33792\n"
    "v_mfma_f32_16x16x32_bf16 v[128:131], a[168:171], v[196:199], v[128:131]\n"
    "s_xor_b32 s91, s89, 544768\n"
    "ds_read_b128 v[176:179], v230 offset:34816\n"
    "ds_read_b128 v[180:183], v230 offset:35840\n"
    "v_mfma_f32_16x16x32_bf16 v[128:131], a[200:203], v[200:203], v[128:131]\n"
    "ds_read_b128 v[184:187], v231 offset:49152\n"
    "ds_read_b128 v[188:191], v231 offset:50176\n"
    "v_mfma_f32_16x16x32_bf16 v[128:131], a[232:235], v[204:207], v[128:131]\n"
    "buffer_load_dwordx4 v[64:67], v233, s[28:31], s91 offen offset:0 sc1\n"
    "v_mfma_f32_16x16x32_bf16 v[128:131], a[8:11], v[208:211], v[128:131]\n"
    "v_mfma_f32_16x16x32_bf16 v[128:131], a[40:43], v[212:215], v[128:131]\n"
    "v_mfma_f32_16x16x32_bf16 v[128:131], a[72:75], v[216:219], v[128:131]\n"
    "v_mfma_f32_16x16x32_bf16 v[128:131], a[104:107], v[220:223], v[128:131]\n"
    "buffer_load_dwordx4 v[80:83], v233, s[28:31], s91 offen offset:1024 sc1\n"
    "v_mfma_f32_16x16x32_bf16 v[132:135], a[108:111], v[220:223], 0\n"
    "v_mfma_f32_16x16x32_bf16 v[132:135], a[76:79], v[216:219], v[132:135]\n"
    "v_mfma_f32_16x16x32_bf16 v[132:135], a[44:47], v[212:215], v[132:135]\n"
    "s_waitcnt lgkmcnt(3)\n"
    "v_add_f32 v168, v168, v176\n"
    "v_add_f32 v169, v169, v177\n"
    "v_mfma_f32_16x16x32_bf16 v[132:135], a[12:15], v[208:211], v[132:135]\n"
    "v_add_f32 v170, v170, v178\n"
    "v_add_f32 v171, v171, v179\n"
    "v_mfma_f32_16x16x32_bf16 v[132:135], a[236:239], v[204:207], v[132:135]\n"
    "s_waitcnt lgkmcnt(2)\n"
    "v_add_f32 v172, v172, v180\n"
    "v_add_f32 v173, v173, v181\n"
    "v_mfma_f32_16x16x32_bf16 v[132:135], a[204:207], v[200:203], v[132:135]\n"
    "buffer_load_dwordx4 v[96:99], v233, s[28:31], s91 offen offset:2048 sc1\n"
    "v_mfma_f32_16x16x32_bf16 v[132:135], a[172:175], v[196:199], v[132:135]\n"
    "v_add_f32 v174, v174, v182\n"
    "v_add_f32 v175, v175, v183\n"
    "v_mfma_f32_16x16x32_bf16 v[132:135], a[140:143], v[192:195], v[132:135]\n"
    "s_waitcnt lgkmcnt(1)\n"
    "ds_write_b128 v228, v[128:131] offset:0\n"
    "v_add_f32 v160, v160, v184\n"
    "v_mfma_f32_16x16x32_bf16 v[136:139], a[144:147], v[192:195], 0\n"
    "v_add_f32 v161, v161, v185\n"
    "v_add_f32 v162, v162, v186\n"
    "v_mfma_f32_16x16x32_bf16 v[136:139], a[176:179], v[196:199], v[136:139]\n"
    "buffer_load_dwordx4 v[112:115], v233, s[28:31], s91 offen offset:3072 sc1\n"
    "v_mfma_f32_16x16x32_bf16 v[136:139], a[208:211], v[200:203], v[136:139]\n"
    "s_waitcnt lgkmcnt(1)\n"
    "v_add_f32 v163, v163, v187\n"
    "v_add_f32 v164, v164, v188\n"
    "v_mfma_f32_16x16x32_bf16 v[136:139], a[240:243], v[204:207], v[136:139]\n"
    "v_add_f32 v165, v165, v189\n"
    "v_add_f32 v166, v166, v190\n"
    "v_mfma_f32_16x16x32_bf16 v[136:139], a[16:19], v[208:211], v[136:139]\n"
    "v_add_f32 v167, v167, v191\n"
    "v_add_f32 v160, v160, v168\n"
    "v_mfma_f32_16x16x32_bf16 v[136:139], a[48:51], v[212:215], v[136:139]\n"
    "buffer_load_dword v249, v244, s[36:39], 0 offen offset:256 sc1\n"
    "v_mfma_f32_16x16x32_bf16 v[136:139], a[80:83], v[216:219], v[136:139]\n"
    "v_add_f32 v161, v161, v169\n"
    "v_add_f32 v162, v162, v170\n"
    "v_mfma_f32_16x16x32_bf16 v[136:139], a[112:115], v[220:223], v[136:139]\n"
    "ds_write_b128 v228, v[132:135] offset:1024\n"
    "v_add_f32 v163, v163, v171\n"
    "v_mfma_f32_16x16x32_bf16 v[140:143], a[116:119], v[220:223], 0\n"
    "v_add_f32 v164, v164, v172\n"
    "v_add_f32 v165, v165, v173\n"
    "v_mfma_f32_16x16x32_bf16 v[140:143], a[84:87], v[216:219], v[140:143]\n"
    "buffer_store_dwordx4 v[12:15], v233, s[28:31], s54 offen offset:0 sc1\n"
    "v_mfma_f32_16x16x32_bf16 v[140:143], a[52:55], v[212:215], v[140:143]\n"
    "v_add_f32 v166, v166, v174\n"
    "v_add_f32 v167, v167, v175\n"
    "v_mfma_f32_16x16x32_bf16 v[140:143], a[20:23], v[208:211], v[140:143]\n"
    "v_cvt_pk_bf16_f32 v56, v160, v161\n"
    "v_cvt_pk_bf16_f32 v57, v162, v163\n"
    "v_mfma_f32_16x16x32_bf16 v[140:143], a[244:247], v[204:207], v[140:143]\n"
    "v_cvt_pk_bf16_f32 v58, v164, v165\n"
    "v_cvt_pk_bf16_f32 v59, v166, v167\n"
    "v_mfma_f32_16x16x32_bf16 v[140:143], a[212:215], v[200:203], v[140:143]\n"
    "v_mfma_f32_16x16x32_bf16 v[140:143], a[180:183], v[196:199], v[140:143]\n"
    "v_mfma_f32_16x16x32_bf16 v[140:143], a[148:151], v[192:195], v[140:143]\n"
    "ds_write_b128 v228, v[136:139] offset:2048\n"
    "v_mfma_f32_16x16x32_bf16 v[144:147], a[152:155], v[192:195], 0\n"
    "buffer_store_dwordx4 v[44:47], v233, s[28:31], s54 offen offset:1024 sc1\n"
    "v_mfma_f32_16x16x32_bf16 v[144:147], a[184:187], v[196:199], v[144:147]\n"
    "v_mfma_f32_16x16x32_bf16 v[144:147], a[216:219], v[200:203], v[144:147]\n"
    "v_mfma_f32_16x16x32_bf16 v[144:147], a[248:251], v[204:207], v[144:147]\n"
    "v_mfma_f32_16x16x32_bf16 v[144:147], a[24:27], v[208:211], v[144:147]\n"
    "v_mfma_f32_16x16x32_bf16 v[144:147], a[56:59], v[212:215], v[144:147]\n"
    "v_mfma_f32_16x16x32_bf16 v[144:147], a[88:91], v[216:219], v[144:147]\n"
    "v_mfma_f32_16x16x32_bf16 v[144:147], a[120:123], v[220:223], v[144:147]\n"
    "ds_write_b128 v228, v[140:143] offset:3072\n"
    "v_mfma_f32_16x16x32_bf16 v[148:151], a[124:127], v[220:223], 0\n"
    "buffer_store_dwordx4 v[76:79], v233, s[28:31], s54 offen offset:2048 sc1\n"
    "v_mfma_f32_16x16x32_bf16 v[148:151], a[92:95], v[216:219], v[148:151]\n"
    "v_mfma_f32_16x16x32_bf16 v[148:151], a[60:63], v[212:215], v[148:151]\n"
    "v_mfma_f32_16x16x32_bf16 v[148:151], a[28:31], v[208:211], v[148:151]\n"
    "v_mfma_f32_16x16x32_bf16 v[148:151], a[252:255], v[204:207], v[148:151]\n"
    "v_mfma_f32_16x16x32_bf16 v[148:151], a[220:223], v[200:203], v[148:151]\n"
    "v_mfma_f32_16x16x32_bf16 v[148:151], a[188:191], v[196:199], v[148:151]\n"
    "v_mfma_f32_16x16x32_bf16 v[148:151], a[156:159], v[192:195], v[148:151]\n"
    "ds_write_b128 v228, v[144:147] offset:16384\n"
    "v_mfma_f32_16x16x32_bf16 v[152:155], a[128:131], v[192:195], 0\n"
    "buffer_store_dwordx4 v[108:111], v233, s[28:31], s54 offen offset:3072 sc1\n"
    "v_mfma_f32_16x16x32_bf16 v[152:155], a[160:163], v[196:199], v[152:155]\n"
    "v_mfma_f32_16x16x32_bf16 v[152:155], a[192:195], v[200:203], v[152:155]\n"
    "v_mfma_f32_16x16x32_bf16 v[152:155], a[224:227], v[204:207], v[152:155]\n"
    "v_mfma_f32_16x16x32_bf16 v[152:155], a[0:3], v[208:211], v[152:155]\n"
    "v_mfma_f32_16x16x32_bf16 v[152:155], a[32:35], v[212:215], v[152:155]\n"
    "v_mfma_f32_16x16x32_bf16 v[152:155], a[64:67], v[216:219], v[152:155]\n"
    "v_mfma_f32_16x16x32_bf16 v[152:155], a[96:99], v[220:223], v[152:155]\n"
    "ds_write_b128 v228, v[148:151] offset:17408\n"
    "v_mfma_f32_16x16x32_bf16 v[156:159], a[100:103], v[220:223], 0\n"
    "s_waitcnt vmcnt(4)\n"
    "v_cmp_eq_u32 vcc, s66, v249\n"
    "v_mfma_f32_16x16x32_bf16 v[156:159], a[68:71], v[216:219], v[156:159]\n"
    "v_mfma_f32_16x16x32_bf16 v[156:159], a[36:39], v[212:215], v[156:159]\n"
    "s_and_b32 s55, vcc_lo, 4369\n"
    "v_mfma_f32_16x16x32_bf16 v[156:159], a[4:7], v[208:211], v[156:159]\n"
    "s_cmp_eq_u32 s55, 4369\n"
    "v_mfma_f32_16x16x32_bf16 v[156:159], a[228:231], v[204:207], v[156:159]\n"
    "s_cbranch_scc0 Lsp2%=\n"
    "Lok2%=:\n"
    "v_mfma_f32_16x16x32_bf16 v[156:159], a[196:199], v[200:203], v[156:159]\n"
    "s_xor_b32 s91, s89, 1048576\n"
    "v_mfma_f32_16x16x32_bf16 v[156:159], a[164:167], v[196:199], v[156:159]\n"
    "buffer_load_dwordx4 v[4:7], v233, s[28:31], s91 offen offset:0 sc1\n"
    "v_mfma_f32_16x16x32_bf16 v[156:159], a[132:135], v[192:195], v[156:159]\n"
    "s_waitcnt vmcnt(6)\n"
    "s_waitcnt lgkmcnt(0)\n"
    "v_mfma_f32_16x16x32_bf16 v[128:131], a[136:139], v[0:3], 0\n"
    "s_barrier\n"
    "ds_read_b128 v[168:171], v229 offset:0\n"
    "ds_read_b128 v[172:175], v229 offset:1024\n"
    "v_mfma_f32_16x16x32_bf16 v[128:131], a[168:171], v[16:19], v[128:131]\n"
    "ds_read_b128 v[176:179], v230 offset:2048\n"
    "ds_read_b128 v[180:183], v230 offset:3072\n"
    "v_mfma_f32_16x16x32_bf16 v[128:131], a[200:203], v[32:35], v[128:131]\n"
    "ds_read_b128 v[184:187], v231 offset:16384\n"
    "ds_read_b128 v[188:191], v231 offset:17408\n"
    "v_mfma_f32_16x16x32_bf16 v[128:131], a[232:235], v[48:51], v[128:131]\n"
    "buffer_load_dwordx4 v[20:23], v233, s[28:31], s91 offen offset:1024 sc1\n"
    "v_mfma_f32_16x16x32_bf16 v[128:131], a[8:11], v[64:67], v[128:131]\n"
    "v_mfma_f32_16x16x32_bf16 v[128:131], a[40:43], v[80:83], v[128:131]\n"
    "v_mfma_f32_16x16x32_bf16 v[128:131], a[72:75], v[96:99], v[128:131]\n"
    "v_mfma_f32_16x16x32_bf16 v[128:131], a[104:107], v[112:115], v[128:131]\n"
    "buffer_load_dwordx4 v[36:39], v233, s[28:31], s91 offen offset:2048 sc1\n"
    "v_mfma_f32_16x16x32_bf16 v[132:135], a[108:111], v[112:115], 0\n"
    "v_mfma_f32_16x16x32_bf16 v[132:135], a[76:79], v[96:99], v[132:135]\n"
    "v_mfma_f32_16x16x32_bf16 v[132:135], a[44:47], v[80:83], v[132:135]\n"
    "s_waitcnt lgkmcnt(3)\n"
    "v_add_f32 v168, v168, v176\n"
    "v_add_f32 v169, v169, v177\n"
    "v_mfma_f32_16x16x32_bf16 v[132:135], a[12:15], v[64:67], v[132:135]\n"
    "v_add_f32 v170, v170, v178\n"
    "v_add_f32 v171, v171, v179\n"
    "v_mfma_f32_16x16x32_bf16 v[132:135], a[236:239], v[48:51], v[132:135]\n"
    "s_waitcnt lgkmcnt(2)\n"
    "v_add_f32 v172, v172, v180\n"
    "v_add_f32 v173, v173, v181\n"
    "v_mfma_f32_16x16x32_bf16 v[132:135], a[204:207], v[32:35], v[132:135]\n"
    "buffer_load_dwordx4 v[52:55], v233, s[28:31], s91 offen offset:3072 sc1\n"
    "v_mfma_f32_16x16x32_bf16 v[132:135], a[172:175], v[16:19], v[132:135]\n"
    "v_add_f32 v174, v174, v182\n"
    "v_add_f32 v175, v175, v183\n"
    "v_mfma_f32_16x16x32_bf16 v[132:135], a[140:143], v[0:3], v[132:135]\n"
    "s_waitcnt lgkmcnt(1)\n"
    "ds_write_b128 v228, v[128:131] offset:32768\n"
    "v_add_f32 v152, v152, v184\n"
    "v_mfma_f32_16x16x32_bf16 v[136:139], a[144:147], v[0:3], 0\n"
    "s_xor_b32 s91, s89, 1064960\n"
    "v_add_f32 v153, v153, v185\n"
    "v_add_f32 v154, v154, v186\n"
    "v_mfma_f32_16x16x32_bf16 v[136:139], a[176:179], v[16:19], v[136:139]\n"
    "buffer_load_dwordx4 v[68:71], v233, s[28:31], s91 offen offset:0 sc1\n"
    "v_mfma_f32_16x16x32_bf16 v[136:139], a[208:211], v[32:35], v[136:139]\n"
    "s_waitcnt lgkmcnt(1)\n"
    "v_add_f32 v155, v155, v187\n"
    "v_add_f32 v156, v156, v188\n"
    "v_mfma_f32_16x16x32_bf16 v[136:139], a[240:243], v[48:51], v[136:139]\n"
    "v_add_f32 v157, v157, v189\n"
    "v_add_f32 v158, v158, v190\n"
    "v_mfma_f32_16x16x32_bf16 v[136:139], a[16:19], v[64:67], v[136:139]\n"
    "v_add_f32 v159, v159, v191\n"
    "v_add_f32 v152, v152, v168\n"
    "v_mfma_f32_16x16x32_bf16 v[136:139], a[48:51], v[80:83], v[136:139]\n"
    "buffer_load_dwordx4 v[84:87], v233, s[28:31], s91 offen offset:1024 sc1\n"
    "v_mfma_f32_16x16x32_bf16 v[136:139], a[80:83], v[96:99], v[136:139]\n"
    "v_add_f32 v153, v153, v169\n"
    "v_add_f32 v154, v154, v170\n"
    "v_mfma_f32_16x16x32_bf16 v[136:139], a[112:115], v[112:115], v[136:139]\n"
    "ds_write_b128 v228, v[132:135] offset:33792\n"
    "v_add_f32 v155, v155, v171\n"
    "v_mfma_f32_16x16x32_bf16 v[140:143], a[116:119], v[112:115], 0\n"
    "v_add_f32 v156, v156, v172\n"
    "v_add_f32 v157, v157, v173\n"
    "v_mfma_f32_16x16x32_bf16 v[140:143], a[84:87], v[96:99], v[140:143]\n"
    "buffer_load_dwordx4 v[100:103], v233, s[28:31], s91 offen offset:2048 sc1\n"
    "v_mfma_f32_16x16x32_bf16 v[140:143], a[52:55], v[80:83], v[140:143]\n"
    "v_add_f32 v158, v158, v174\n"
    "v_add_f32 v159, v159, v175\n"
    "v_mfma_f32_16x16x32_bf16 v[140:143], a[20:23], v[64:67], v[140:143]\n"
    "v_cvt_pk_bf16_f32 v28, v152, v153\n"
    "v_cvt_pk_bf16_f32 v29, v154, v155\n"
    "v_mfma_f32_16x16x32_bf16 v[140:143], a[244:247], v[48:51], v[140:143]\n"
    "v_cvt_pk_bf16_f32 v30, v156, v157\n"
    "v_cvt_pk_bf16_f32 v31, v158, v159\n"
    "v_mfma_f32_16x16x32_bf16 v[140:143], a[212:215], v[32:35], v[140:143]\n"
    "buffer_load_dwordx4 v[116:119], v233, s[28:31], s91 offen offset:3072 sc1\n"
    "v_mfma_f32_16x16x32_bf16 v[140:143], a[180:183], v[16:19], v[140:143]\n"
    "v_mfma_f32_16x16x32_bf16 v[140:143], a[148:151], v[0:3], v[140:143]\n"
    "ds_write_b128 v228, v[136:139] offset:34816\n"
    "v_mfma_f32_16x16x32_bf16 v[144:147], a[152:155], v[0:3], 0\n"
    "v_mfma_f32_16x16x32_bf16 v[144:147], a[184:187], v[16:19], v[144:147]\n"
    "v_mfma_f32_16x16x32_bf16 v[144:147], a[216:219], v[32:35], v[144:147]\n"
    "v_mfma_f32_16x16x32_bf16 v[144:147], a[248:251], v[48:51], v[144:147]\n"
    "v_mfma_f32_16x16x32_bf16 v[144:147], a[24:27], v[64:67], v[144:147]\n"
    "s_waitcnt vmcnt(8)\n"
    "buffer_store_dword v246, v247, s[36:39], 0 offen offset:384 sc1\n"
    "v_mfma_f32_16x16x32_bf16 v[144:147], a[56:59], v[80:83], v[144:147]\n"
    "buffer_store_dwordx4 v[24:27], v233, s[32:35], s68 offen sc1\n"
    "v_mfma_f32_16x16x32_bf16 v[144:147], a[88:91], v[96:99], v[144:147]\n"
    "s_xor_b32 s91, s89, 1052672\n"
    "v_mfma_f32_16x16x32_bf16 v[144:147], a[120:123], v[112:115], v[144:147]\n"
    "ds_write_b128 v228, v[140:143] offset:35840\n"
    "v_mfma_f32_16x16x32_bf16 v[148:151], a[124:127], v[112:115], 0\n"
    "Lpg14%=:\n"
    "s_cbranch_execz Lwb2%=\n"
    "buffer_load_dwordx4 v[192:195], v233, s[28:31], s91 offen offset:0 sc1\n"
    "v_mfma_f32_16x16x32_bf16 v[148:151], a[92:95], v[96:99], v[148:151]\n"
    "v_mfma_f32_16x16x32_bf16 v[148:151], a[60:63], v[80:83], v[148:151]\n"
    "v_mfma_f32_16x16x32_bf16 v[148:151], a[28:31], v[64:67], v[148:151]\n"
    "v_mfma_f32_16x16x32_bf16 v[148:151], a[252:255], v[48:51], v[148:151]\n"
    "v_mfma_f32_16x16x32_bf16 v[148:151], a[220:223], v[32:35], v[148:151]\n"
    "v_mfma_f32_16x16x32_bf16 v[148:151], a[188:191], v[16:19], v[148:151]\n"
    "buffer_load_dwordx4 v[196:199], v233, s[28:31], s91 offen offset:1024 sc1\n"
    "v_mfma_f32_16x16x32_bf16 v[148:151], a[156:159], v[0:3], v[148:151]\n"
    "ds_write_b128 v228, v[144:147] offset:49152\n"
    "v_mfma_f32_16x16x32_bf16 v[160:163], a[128:131], v[0:3], 0\n"
    "v_mfma_f32_16x16x32_bf16 v[160:163], a[160:163], v[16:19], v[160:163]\n"
    "v_mfma_f32_16x16x32_bf16 v[160:163], a[192:195], v[32:35], v[160:163]\n"
    "v_mfma_f32_16x16x32_bf16 v[160:163], a[224:227], v[48:51], v[160:163]\n"
    "v_mfma_f32_16x16x32_bf16 v[160:163], a[0:3], v[64:67], v[160:163]\n"
    "buffer_load_dwordx4 v[200:203], v233, s[28:31], s91 offen offset:2048 sc1\n"
    "v_mfma_f32_16x16x32_bf16 v[160:163], a[32:35], v[80:83], v[160:163]\n"
    "v_mfma_f32_16x16x32_bf16 v[160:163], a[64:67], v[96:99], v[160:163]\n"
    "v_mfma_f32_16x16x32_bf16 v[160:163], a[96:99], v[112:115], v[160:163]\n"
    "ds_write_b128 v228, v[148:151] offset:50176\n"
    "v_mfma_f32_16x16x32_bf16 v[164:167], a[100:103], v[112:115], 0\n"
    "v_mfma_f32_16x16x32_bf16 v[164:167], a[68:71], v[96:99], v[164:167]\n"
    "v_mfma_f32_16x16x32_bf16 v[164:167], a[36:39], v[80:83], v[164:167]\n"
    "buffer_load_dwordx4 v[204:207], v233, s[28:31], s91 offen offset:3072 sc1\n"
    "v_mfma_f32_16x16x32_bf16 v[164:167], a[4:7], v[64:67], v[164:167]\n"
    "v_mfma_f32_16x16x32_bf16 v[164:167], a[228:231], v[48:51], v[164:167]\n"
    "v_mfma_f32_16x16x32_bf16 v[164:167], a[196:199], v[32:35], v[164:167]\n"
    "v_mfma_f32_16x16x32_bf16 v[164:167], a[164:167], v[16:19], v[164:167]\n"
    "v_mfma_f32_16x16x32_bf16 v[164:167], a[132:135], v[0:3], v[164:167]\n"
    "s_waitcnt vmcnt(6)\n"
    "s_waitcnt lgkmcnt(0)\n"
    "v_mfma_f32_16x16x32_bf16 v[128:131], a[136:139], v[4:7], 0\n"
    "s_barrier\n"
    "ds_read_b128 v[168:171], v229 offset:32768\n"
    "ds_read_b128 v[172:175], v229 offset:33792\n"
    "v_mfma_f32_16x16x32_bf16 v[128:131], a[168:171], v[20:23], v[128:131]\n"
    "s_xor_b32 s91, s89, 1069056\n"
    "ds_read_b128 v[176:179], v230 offset:34816\n"
    "ds_read_b128 v[180:183], v230 offset:35840\n"
    "v_mfma_f32_16x16x32_bf16 v[128:131], a[200:203], v[36:39], v[128:131]\n"
    "ds_read_b128 v[184:187], v231 offset:49152\n"
    "ds_read_b128 v[188:191], v231 offset:50176\n"
    "v_mfma_f32_16x16x32_bf16 v[128:131], a[232:235], v[52:55], v[128:131]\n"
    "buffer_load_dwordx4 v[208:211], v233, s[28:31], s91 offen offset:0 sc1\n"
    "v_mfma_f32_16x16x32_bf16 v[128:131], a[8:11], v[68:71], v[128:131]\n"
    "v_mfma_f32_16x16x32_bf16 v[128:131], a[40:43], v[84:87], v[128:131]\n"
    "v_mfma_f32_16x16x32_bf16 v[128:131], a[72:75], v[100:103], v[128:131]\n"
    "v_mfma_f32_16x16x32_bf16 v[128:131], a[104:107], v[116:119], v[128:131]\n"
    "buffer_load_dwordx4 v[212:215], v233, s[28:31], s91 offen offset:1024 sc1\n"
    "v_mfma_f32_16x16x32_bf16 v[132:135], a[108:111], v[116:119], 0\n"
    "v_mfma_f32_16x16x32_bf16 v[132:135], a[76:79], v[100:103], v[132:135]\n"
    "v_mfma_f32_16x16x32_bf16 v[132:135], a[44:47], v[84:87], v[132:135]\n"
    "s_waitcnt lgkmcnt(3)\n"
    "v_add_f32 v168, v168, v176\n"
    "v_add_f32 v169, v169, v177\n"
    "v_mfma_f32_16x16x32_bf16 v[132:135], a[12:15], v[68:71], v[132:135]\n"
    "v_add_f32 v170, v170, v178\n"
    "v_add_f32 v171, v171, v179\n"
    "v_mfma_f32_16x16x32_bf16 v[132:135], a[236:239], v[52:55], v[132:135]\n"
    "s_waitcnt lgkmcnt(2)\n"
    "v_add_f32 v172, v172, v180\n"
    "v_add_f32 v173, v173, v181\n"
    "v_mfma_f32_16x16x32_bf16 v[132:135], a[204:207], v[36:39], v[132:135]\n"
    "buffer_load_dwordx4 v[216:219], v233, s[28:31], s91 offen offset:2048 sc1\n"
    "v_mfma_f32_16x16x32_bf16 v[132:135], a[172:175], v[20:23], v[132:135]\n"
    "v_add_f32 v174, v174, v182\n"
    "v_add_f32 v175, v175, v183\n"
    "v_mfma_f32_16x16x32_bf16 v[132:135], a[140:143], v[4:7], v[132:135]\n"
    "s_waitcnt lgkmcnt(1)\n"
    "ds_write_b128 v228, v[128:131] offset:0\n"
    "v_add_f32 v160, v160, v184\n"
    "v_mfma_f32_16x16x32_bf16 v[136:139], a[144:147], v[4:7], 0\n"
    "v_add_f32 v161, v161, v185\n"
    "v_add_f32 v162, v162, v186\n"
    "v_mfma_f32_16x16x32_bf16 v[136:139], a[176:179], v[20:23], v[136:139]\n"
    "buffer_load_dwordx4 v[220:223], v233, s[28:31], s91 offen offset:3072 sc1\n"
    "v_mfma_f32_16x16x32_bf16 v[136:139], a[208:211], v[36:39], v[136:139]\n"
    "s_waitcnt lgkmcnt(1)\n"
    "v_add_f32 v163, v163, v187\n"
    "v_add_f32 v164, v164, v188\n"
    "v_mfma_f32_16x16x32_bf16 v[136:139], a[240:243], v[52:55], v[136:139]\n"
    "v_add_f32 v165, v165, v189\n"
    "v_add_f32 v166, v166, v190\n"
    "v_mfma_f32_16x16x32_bf16 v[136:139], a[16:19], v[68:71], v[136:139]\n"
    "v_add_f32 v167, v167, v191\n"
    "v_add_f32 v160, v160, v168\n"
    "v_mfma_f32_16x16x32_bf16 v[136:139], a[48:51], v[84:87], v[136:139]\n"
    "buffer_load_dword v249, v244, s[36:39], 0 offen offset:384 sc1\n"
    "v_mfma_f32_16x16x32_bf16 v[136:139], a[80:83], v[100:103], v[136:139]\n"
    "v_add_f32 v161, v161, v169\n"
    "v_add_f32 v162, v162, v170\n"
    "v_mfma_f32_16x16x32_bf16 v[136:139], a[112:115], v[116:119], v[136:139]\n"
    "ds_write_b128 v228, v[132:135] offset:1024\n"
    "v_add_f32 v163, v163, v171\n"
    "v_mfma_f32_16x16x32_bf16 v[140:143], a[116:119], v[116:119], 0\n"
    "v_add_f32 v164, v164, v172\n"
    "v_add_f32 v165, v165, v173\n"
    "v_mfma_f32_16x16x32_bf16 v[140:143], a[84:87], v[100:103], v[140:143]\n"
    "buffer_store_dwordx4 v[56:59], v233, s[32:35], s69 offen sc1\n"
    "v_mfma_f32_16x16x32_bf16 v[140:143], a[52:55], v[84:87], v[140:143]\n"
    "v_add_f32 v166, v166, v174\n"
    "v_add_f32 v167, v167, v175\n"
    "v_mfma_f32_16x16x32_bf16 v[140:143], a[20:23], v[68:71], v[140:143]\n"
    "v_cvt_pk_bf16_f32 v60, v160, v161\n"
    "v_cvt_pk_bf16_f32 v61, v162, v163\n"
    "v_mfma_f32_16x16x32_bf16 v[140:143], a[244:247], v[52:55], v[140:143]\n"
    "v_cvt_pk_bf16_f32 v62, v164, v165\n"
    "v_cvt_pk_bf16_f32 v63, v166, v167\n"
    "v_mfma_f32_16x16x32_bf16 v[140:143], a[212:215], v[36:39], v[140:143]\n"
    "v_mfma_f32_16x16x32_bf16 v[140:143], a[180:183], v[20:23], v[140:143]\n"
    "v_mfma_f32_16x16x32_bf16 v[140:143], a[148:151], v[4:7], v[140:143]\n"
    "ds_write_b128 v228, v[136:139] offset:2048\n"
    "v_mfma_f32_16x16x32_bf16 v[144:147], a[152:155], v[4:7], 0\n"
    "v_mfma_f32_16x16x32_bf16 v[144:147], a[184:187], v[20:23], v[144:147]\n"
    "buffer_store_dwordx4 v[28:31], v233, s[32:35], s70 offen sc1\n"
    "v_mfma_f32_16x16x32_bf16 v[144:147], a[216:219], v[36:39], v[144:147]\n"
    "v_mfma_f32_16x16x32_bf16 v[144:147], a[248:251], v[52:55], v[144:147]\n"
    "v_mfma_f32_16x16x32_bf16 v[144:147], a[24:27], v[68:71], v[144:147]\n"
    "v_mfma_f32_16x16x32_bf16 v[144:147], a[56:59], v[84:87], v[144:147]\n"
    "v_mfma_f32_16x16x32_bf16 v[144:147], a[88:91], v[100:103], v[144:147]\n"
    "v_mfma_f32_16x16x32_bf16 v[144:147], a[120:123], v[116:119], v[144:147]\n"
    "ds_write_b128 v228, v[140:143] offset:3072\n"
    "v_mfma_f32_16x16x32_bf16 v[148:151], a[124:127], v[116:119], 0\n"
    "v_mfma_f32_16x16x32_bf16 v[148:151], a[92:95], v[100:103], v[148:151]\n"
    "buffer_store_dwordx4 v[60:63], v233, s[32:35], s71 offen sc1\n"
    "v_mfma_f32_16x16x32_bf16 v[148:151], a[60:63], v[84:87], v[148:151]\n"
    "v_mfma_f32_16x16x32_bf16 v[148:151], a[28:31], v[68:71], v[148:151]\n"
    "v_mfma_f32_16x16x32_bf16 v[148:151], a[252:255], v[52:55], v[148:151]\n"
    "v_mfma_f32_16x16x32_bf16 v[148:151], a[220:223], v[36:39], v[148:151]\n"
    "v_mfma_f32_16x16x32_bf16 v[148:151], a[188:191], v[20:23], v[148:151]\n"
    "v_mfma_f32_16x16x32_bf16 v[148:151], a[156:159], v[4:7], v[148:151]\n"
    "ds_write_b128 v228, v[144:147] offset:16384\n"
    "v_mfma_f32_16x16x32_bf16 v[152:155], a[128:131], v[4:7], 0\n"
    "v_mfma_f32_16x16x32_bf16 v[152:155], a[160:163], v[20:23], v[152:155]\n"
    "v_mfma_f32_16x16x32_bf16 v[152:155], a[192:195], v[36:39], v[152:155]\n"
    "v_mfma_f32_16x16x32_bf16 v[152:155], a[224:227], v[52:55], v[152:155]\n"
    "v_mfma_f32_16x16x32_bf16 v[152:155], a[0:3], v[68:71], v[152:155]\n"
    "v_mfma_f32_16x16x32_bf16 v[152:155], a[32:35], v[84:87], v[152:155]\n"
    "v_mfma_f32_16x16x32_bf16 v[152:155], a[64:67], v[100:103], v[152:155]\n"
    "v_mfma_f32_16x16x32_bf16 v[152:155], a[96:99], v[116:119], v[152:155]\n"
    "s_waitcnt vmcnt(3)\n"
    "ds_write_b128 v228, v[148:151] offset:17408\n"
    "v_cmp_eq_u32 vcc, s66, v249\n"
    "v_mfma_f32_16x16x32_bf16 v[156:159], a[100:103], v[116:119], 0\n"
    "v_mfma_f32_16x16x32_bf16 v[156:159], a[68:71], v[100:103], v[156:159]\n"
    "s_and_b32 s55, vcc_lo, 4369\n"
    "v_mfma_f32_16x16x32_bf16 v[156:159], a[36:39], v[84:87], v[156:159]\n"
    "s_cmp_eq_u32 s55, 4369\n"
    "v_mfma_f32_16x16x32_bf16 v[156:159], a[4:7], v[68:71], v[156:159]\n"
    "s_cbranch_scc0 Lsp3%=\n"
    "Lok3%=:\n"
    "v_mfma_f32_16x16x32_bf16 v[156:159], a[228:231], v[52:55], v[156:159]\n"
    "s_xor_b32 s91, s89, 1572864\n"
    "v_mfma_f32_16x16x32_bf16 v[156:159], a[196:199], v[36:39], v[156:159]\n"
    "buffer_load_dwordx4 v[0:3], v233, s[28:31], s91 offen offset:0 sc1\n"
    "v_mfma_f32_16x16x32_bf16 v[156:159], a[164:167], v[20:23], v[156:159]\n"
    "v_mfma_f32_16x16x32_bf16 v[156:159], a[132:135], v[4:7], v[156:159]\n"
    "s_waitcnt vmcnt(5)\n"
    "s_waitcnt lgkmcnt(0)\n"
    "v_mfma_f32_16x16x32_bf16 v[128:131], a[136:139], v[192:195], 0\n"
    "s_barrier\n"
    "ds_read_b128 v[168:171], v229 offset:0\n"
    "ds_read_b128 v[172:175], v229 offset:1024\n"
    "v_mfma_f32_16x16x32_bf16 v[128:131], a[168:171], v[196:199], v[128:131]\n"
    "ds_read_b128 v[176:179], v230 offset:2048\n"
    "ds_read_b128 v[180:183], v230 offset:3072\n"
    "v_mfma_f32_16x16x32_bf16 v[128:131], a[200:203], v[200:203], v[128:131]\n"
    "ds_read_b128 v[184:187], v231 offset:16384\n"
    "ds_read_b128 v[188:191], v231 offset:17408\n"
    "v_mfma_f32_16x16x32_bf16 v[128:131], a[232:235], v[204:207], v[128:131]\n"
    "buffer_load_dwordx4 v[16:19], v233, s[28:31], s91 offen offset:1024 sc1\n"
    "v_mfma_f32_16x16x32_bf16 v[128:131], a[8:11], v[208:211], v[128:131]\n"
    "v_mfma_f32_16x16x32_bf16 v[128:131], a[40:43], v[212:215], v[128:131]\n"
    "v_mfma_f32_16x16x32_bf16 v[128:131], a[72:75], v[216:219], v[128:131]\n"
    "v_mfma_f32_16x16x32_bf16 v[128:131], a[104:107], v[220:223], v[128:131]\n"
    "buffer_load_dwordx4 v[32:35], v233, s[28:31], s91 offen offset:2048 sc1\n"
    "v_mfma_f32_16x16x32_bf16 v[132:135], a[108:111], v[220:223], 0\n"
    "v_mfma_f32_16x16x32_bf16 v[132:135], a[76:79], v[216:219], v[132:135]\n"
    "v_mfma_f32_16x16x32_bf16 v[132:135], a[44:47], v[212:215], v[132:135]\n"
    "s_waitcnt lgkmcnt(3)\n"
    "v_add_f32 v168, v168, v176\n"
    "v_add_f32 v169, v169, v177\n"
    "v_mfma_f32_16x16x32_bf16 v[132:135], a[12:15], v[208:211], v[132:135]\n"
    "v_add_f32 v170, v170, v178\n"
    "v_add_f32 v171, v171, v179\n"
    "v_mfma_f32_16x16x32_bf16 v[132:135], a[236:239], v[204:207], v[132:135]\n"
    "s_waitcnt lgkmcnt(2)\n"
    "v_add_f32 v172, v172, v180\n"
    "v_add_f32 v173, v173, v181\n"
    "v_mfma_f32_16x16x32_bf16 v[132:135], a[204:207], v[200:203], v[132:135]\n"
    "buffer_load_dwordx4 v[48:51], v233, s[28:31], s91 offen offset:3072 sc1\n"
    "v_mfma_f32_16x16x32_bf16 v[132:135], a[172:175], v[196:199], v[132:135]\n"
    "v_add_f32 v174, v174, v182\n"
    "v_add_f32 v175, v175, v183\n"
    "v_mfma_f32_16x16x32_bf16 v[132:135], a[140:143], v[192:195], v[132:135]\n"
    "s_waitcnt lgkmcnt(1)\n"
    "ds_write_b128 v228, v[128:131] offset:32768\n"
    "v_add_f32 v152, v152, v184\n"
    "v_mfma_f32_16x16x32_bf16 v[136:139], a[144:147], v[192:195], 0\n"
    "s_xor_b32 s91, s89, 1589248\n"
    "v_add_f32 v153, v153, v185\n"
    "v_add_f32 v154, v154, v186\n"
    "v_mfma_f32_16x16x32_bf16 v[136:139], a[176:179], v[196:199], v[136:139]\n"
    "buffer_load_dwordx4 v[64:67], v233, s[28:31], s91 offen offset:0 sc1\n"
    "v_mfma_f32_16x16x32_bf16 v[136:139], a[208:211], v[200:203], v[136:139]\n"
    "s_waitcnt lgkmcnt(1)\n"
    "v_add_f32 v155, v155, v187\n"
    "v_add_f32 v156, v156, v188\n"
    "v_mfma_f32_16x16x32_bf16 v[136:139], a[240:243], v[204:207], v[136:139]\n"
    "v_add_f32 v157, v157, v189\n"
    "v_add_f32 v158, v158, v190\n"
    "v_mfma_f32_16x16x32_bf16 v[136:139], a[16:19], v[208:211], v[136:139]\n"
    "v_add_f32 v159, v159, v191\n"
    "v_add_f32 v152, v152, v168\n"
    "v_mfma_f32_16x16x32_bf16 v[136:139], a[48:51], v[212:215], v[136:139]\n"
    "buffer_load_dwordx4 v[80:83], v233, s[28:31], s91 offen offset:1024 sc1\n"
    "v_mfma_f32_16x16x32_bf16 v[136:139], a[80:83], v[216:219], v[136:139]\n"
    "v_add_f32 v153, v153, v169\n"
    "v_add_f32 v154, v154, v170\n"
    "v_mfma_f32_16x16x32_bf16 v[136:139], a[112:115], v[220:223], v[136:139]\n"
    "ds_write_b128 v228, v[132:135] offset:33792\n"
    "v_add_f32 v155, v155, v171\n"
    "v_mfma_f32_16x16x32_bf16 v[140:143], a[116:119], v[220:223], 0\n"
    "v_add_f32 v156, v156, v172\n"
    "v_add_f32 v157, v157, v173\n"
    "v_mfma_f32_16x16x32_bf16 v[140:143], a[84:87], v[216:219], v[140:143]\n"
    "buffer_load_dwordx4 v[96:99], v233, s[28:31], s91 offen offset:2048 sc1\n"
    "v_mfma_f32_16x16x32_bf16 v[140:143], a[52:55], v[212:215], v[140:143]\n"
    "v_add_f32 v158, v158, v174\n"
    "v_add_f32 v159, v159, v175\n"
    "v_mfma_f32_16x16x32_bf16 v[140:143], a[20:23], v[208:211], v[140:143]\n"
    "v_cvt_pk_bf16_f32 v88, v152, v153\n"
    "v_cvt_pk_bf16_f32 v89, v154, v155\n"
    "v_mfma_f32_16x16x32_bf16 v[140:143], a[244:247], v[204:207], v[140:143]\n"
    "v_cvt_pk_bf16_f32 v90, v156, v157\n"
    "v_cvt_pk_bf16_f32 v91, v158, v159\n"
    "v_mfma_f32_16x16x32_bf16 v[140:143], a[212:215], v[200:203], v[140:143]\n"
    "buffer_load_dwordx4 v[112:115], v233, s[28:31], s91 offen offset:3072 sc1\n"
    "v_mfma_f32_16x16x32_bf16 v[140:143], a[180:183], v[196:199], v[140:143]\n"
    "v_mfma_f32_16x16x32_bf16 v[140:143], a[148:151], v[192:195], v[140:143]\n"
    "ds_write_b128 v228, v[136:139] offset:34816\n"
    "v_mfma_f32_16x16x32_bf16 v[144:147], a[152:155], v[192:195], 0\n"
    "v_cmp_eq_u32 vcc, 4, v253\n"
    "v_and_b32 v251, 7, v253\n"
    "v_mfma_f32_16x16x32_bf16 v[144:147], a[184:187], v[196:199], v[144:147]\n"
    "v_mfma_f32_16x16x32_bf16 v[144:147], a[216:219], v[200:203], v[144:147]\n"
    "v_cndmask_b32 v250, v254, v248, vcc\n"
    "v_lshlrev_b32 v251, 11, v251\n"
    "v_mfma_f32_16x16x32_bf16 v[144:147], a[248:251], v[204:207], v[144:147]\n"
    "s_waitcnt vmcnt(10)\n"
    "buffer_store_dword v246, v250, s[36:39], 0 offen sc1\n"
    "v_mfma_f32_16x16x32_bf16 v[144:147], a[24:27], v[208:211], v[144:147]\n"
    "s_lshr_b32 s52, s48, 2\n"
    "v_cmp_eq_u32 vcc, 5, v253\n"
    "v_bfe_u32 v252, v253, 3, 1\n"
    "v_mfma_f32_16x16x32_bf16 v[144:147], a[56:59], v[212:215], v[144:147]\n"
    "s_lshl_b32 s52, s52, 17\n"
    "v_mfma_f32_16x16x32_bf16 v[144:147], a[88:91], v[216:219], v[144:147]\n"
    "s_lshl_b32 s54, s49, 14\n"
    "v_cndmask_b32 v250, v254, v248, vcc\n"
    "v_lshl_add_u32 v251, v252, 10, v251\n"
    "v_mfma_f32_16x16x32_bf16 v[144:147], a[120:123], v[220:223], v[144:147]\n"
    "s_waitcnt vmcnt(9)\n"
    "ds_write_b128 v228, v[140:143] offset:35840\n"
    "buffer_store_dword v246, v250, s[36:39], 0 offen sc1\n"
    "v_mfma_f32_16x16x32_bf16 v[148:151], a[124:127], v[220:223], 0\n"
    "s_add_u32 s52, s52, s54\n"
    "buffer_store_dwordx4 v[88:91], v233, s[32:35], s72 offen sc1\n"
    "v_mfma_f32_16x16x32_bf16 v[148:151], a[92:95], v[216:219], v[148:151]\n"
    "s_xor_b32 s91, s89, 1576960\n"
    "v_mfma_f32_16x16x32_bf16 v[148:151], a[60:63], v[212:215], v[148:151]\n"
    "s_and_b32 s54, s48, 3\n"
    "buffer_load_dwordx4 v[4:7], v233, s[28:31], s91 offen offset:0 sc1\n"
    "v_mfma_f32_16x16x32_bf16 v[148:151], a[28:31], v[208:211], v[148:151]\n"
    "s_lshl_b32 s54, s54, 8\n"
    "v_bfe_u32 v252, v253, 4, 1\n"
    "v_lshl_add_u32 v251, v252, 5, v251\n"
    "v_mfma_f32_16x16x32_bf16 v[148:151], a[252:255], v[204:207], v[148:151]\n"
    "s_add_u32 s52, s52, s54\n"
    "v_lshrrev_b32 v252, 5, v253\n"
    "v_lshl_add_u32 v251, v252, 4, v251\n"
    "v_mfma_f32_16x16x32_bf16 v[148:151], a[220:223], v[200:203], v[148:151]\n"
    "s_lshl_b32 s54, s44, 6\n"
    "v_mfma_f32_16x16x32_bf16 v[148:151], a[188:191], v[196:199], v[148:151]\n"
    "s_add_u32 s92, s52, s54\n"
    "buffer_load_dwordx4 v[20:23], v233, s[28:31], s91 offen offset:1024 sc1\n"
    "v_mfma_f32_16x16x32_bf16 v[148:151], a[156:159], v[192:195], v[148:151]\n"
    "s_add_u32 s52, s92, 65536\n"
    "ds_write_b128 v228, v[144:147] offset:49152\n"
    "v_mov_b32 v8, 0\n"
    "v_mfma_f32_16x16x32_bf16 v[160:163], a[128:131], v[192:195], 0\n"
    "v_mov_b32 v9, 0\n"
    "v_mov_b32 v10, 0\n"
    "v_mfma_f32_16x16x32_bf16 v[160:163], a[160:163], v[196:199], v[160:163]\n"
    "v_mfma_f32_16x16x32_bf16 v[160:163], a[192:195], v[200:203], v[160:163]\n"
    "buffer_load_dwordx4 v[36:39], v233, s[28:31], s91 offen offset:2048 sc1\n"
    "v_mfma_f32_16x16x32_bf16 v[160:163], a[224:227], v[204:207], v[160:163]\n"
    "v_mov_b32 v11, 0\n"
    "v_mov_b32 v12, 0\n"
    "v_mfma_f32_16x16x32_bf16 v[160:163], a[0:3], v[208:211], v[160:163]\n"
    "v_mov_b32 v13, 0\n"
    "v_mov_b32 v14, 0\n"
    "v_mfma_f32_16x16x32_bf16 v[160:163], a[32:35], v[212:215], v[160:163]\n"
    "v_mfma_f32_16x16x32_bf16 v[160:163], a[64:67], v[216:219], v[160:163]\n"
    "buffer_load_dwordx4 v[52:55], v233, s[28:31], s91 offen offset:3072 sc1\n"
    "v_mfma_f32_16x16x32_bf16 v[160:163], a[96:99], v[220:223], v[160:163]\n"
    "ds_write_b128 v228, v[148:151] offset:50176\n"
    "v_mov_b32 v15, 0\n"
    "v_mfma_f32_16x16x32_bf16 v[164:167], a[100:103], v[220:223], 0\n"
    "v_mov_b32 v40, 0\n"
    "v_mov_b32 v41, 0\n"
    "v_mfma_f32_16x16x32_bf16 v[164:167], a[68:71], v[216:219], v[164:167]\n"
    "s_xor_b32 s91, s89, 1593344\n"
    "v_mfma_f32_16x16x32_bf16 v[164:167], a[36:39], v[212:215], v[164:167]\n"
    "buffer_load_dwordx4 v[68:71], v233, s[28:31], s91 offen offset:0 sc1\n"
    "v_mfma_f32_16x16x32_bf16 v[164:167], a[4:7], v[208:211], v[164:167]\n"
    "v_mov_b32 v42, 0\n"
    "v_mov_b32 v43, 0\n"
    "v_mfma_f32_16x16x32_bf16 v[164:167], a[228:231], v[204:207], v[164:167]\n"
    "v_mov_b32 v44, 0\n"
    "v_mov_b32 v45, 0\n"
    "v_mfma_f32_16x16x32_bf16 v[164:167], a[196:199], v[200:203], v[164:167]\n"
    "v_mfma_f32_16x16x32_bf16 v[164:167], a[164:167], v[196:199], v[164:167]\n"
    "buffer_load_dwordx4 v[84:87], v233, s[28:31], s91 offen offset:1024 sc1\n"
    "v_mfma_f32_16x16x32_bf16 v[164:167], a[132:135], v[192:195], v[164:167]\n"
    "s_waitcnt vmcnt(9)\n"
    "s_waitcnt lgkmcnt(0)\n"
    "v_mov_b32 v46, 0\n"
    "v_mfma_f32_16x16x32_bf16 v[128:131], a[136:139], v[0:3], 0\n"
    "s_barrier\n"
    "ds_read_b128 v[168:171], v229 offset:32768\n"
    "ds_read_b128 v[172:175], v229 offset:33792\n"
    "v_mfma_f32_16x16x32_bf16 v[128:131], a[168:171], v[16:19], v[128:131]\n"
    "ds_read_b128 v[176:179], v230 offset:34816\n"
    "ds_read_b128 v[180:183], v230 offset:35840\n"
    "v_mfma_f32_16x16x32_bf16 v[128:131], a[200:203], v[32:35], v[128:131]\n"
    "ds_read_b128 v[184:187], v231 offset:49152\n"
    "ds_read_b128 v[188:191], v231 offset:50176\n"
    "v_mfma_f32_16x16x32_bf16 v[128:131], a[232:235], v[48:51], v[128:131]\n"
    "buffer_load_dwordx4 v[100:103], v233, s[28:31], s91 offen offset:2048 sc1\n"
    "v_mfma_f32_16x16x32_bf16 v[128:131], a[8:11], v[64:67], v[128:131]\n"
    "v_mov_b32 v47, 0\n"
    "v_mfma_f32_16x16x32_bf16 v[128:131], a[40:43], v[80:83], v[128:131]\n"
    "v_mfma_f32_16x16x32_bf16 v[128:131], a[72:75], v[96:99], v[128:131]\n"
    "v_mfma_f32_16x16x32_bf16 v[128:131], a[104:107], v[112:115], v[128:131]\n"
    "buffer_load_dwordx4 v[116:119], v233, s[28:31], s91 offen offset:3072 sc1\n"
    "v_mfma_f32_16x16x32_bf16 v[132:135], a[108:111], v[112:115], 0\n"
    "v_mfma_f32_16x16x32_bf16 v[132:135], a[76:79], v[96:99], v[132:135]\n"
    "s_waitcnt lgkmcnt(3)\n"
    "v_add_f32 v168, v168, v176\n"
    "v_add_f32 v169, v169, v177\n"
    "v_mfma_f32_16x16x32_bf16 v[132:135], a[44:47], v[80:83], v[132:135]\n"
    "v_add_f32 v170, v170, v178\n"
    "v_add_f32 v171, v171, v179\n"
    "v_mfma_f32_16x16x32_bf16 v[132:135], a[12:15], v[64:67], v[132:135]\n"
    "s_waitcnt lgkmcnt(2)\n"
    "v_add_f32 v172, v172, v180\n"
    "v_add_f32 v173, v173, v181\n"
    "v_mfma_f32_16x16x32_bf16 v[132:135], a[236:239], v[48:51], v[132:135]\n"
    "v_add_f32 v174, v174, v182\n"
    "v_add_f32 v175, v175, v183\n"
    "v_mfma_f32_16x16x32_bf16 v[132:135], a[204:207], v[32:35], v[132:135]\n"
    "s_waitcnt lgkmcnt(1)\n"
    "v_add_f32 v160, v160, v184\n"
    "v_add_f32 v161, v161, v185\n"
    "v_mfma_f32_16x16x32_bf16 v[132:135], a[172:175], v[16:19], v[132:135]\n"
    "buffer_load_dword v249, v232, s[36:39], 0 offen offset:0 sc1\n"
    "v_add_f32 v162, v162, v186\n"
    "v_add_f32 v163, v163, v187\n"
    "v_mfma_f32_16x16x32_bf16 v[132:135], a[140:143], v[0:3], v[132:135]\n"
    "s_waitcnt lgkmcnt(0)\n"
    "ds_write_b128 v228, v[128:131] offset:0\n"
    "v_add_f32 v164, v164, v188\n"
    "v_mfma_f32_16x16x32_bf16 v[136:139], a[144:147], v[0:3], 0\n"
    "v_add_f32 v165, v165, v189\n"
    "v_add_f32 v166, v166, v190\n"
    "v_mfma_f32_16x16x32_bf16 v[136:139], a[176:179], v[16:19], v[136:139]\n"
    "v_add_f32 v167, v167, v191\n"
    "v_add_f32 v160, v160, v168\n"
    "v_mfma_f32_16x16x32_bf16 v[136:139], a[208:211], v[32:35], v[136:139]\n"
    "v_add_f32 v161, v161, v169\n"
    "v_add_f32 v162, v162, v170\n"
    "v_mfma_f32_16x16x32_bf16 v[136:139], a[240:243], v[48:51], v[136:139]\n"
    "v_add_f32 v163, v163, v171\n"
    "v_add_f32 v164, v164, v172\n"
    "v_mfma_f32_16x16x32_bf16 v[136:139], a[16:19], v[64:67], v[136:139]\n"
    "v_add_f32 v165, v165, v173\n"
    "v_add_f32 v166, v166, v174\n"
    "v_mfma_f32_16x16x32_bf16 v[136:139], a[48:51], v[80:83], v[136:139]\n"
    "v_add_f32 v167, v167, v175\n"
    "v_cvt_pk_bf16_f32 v160, v160, v161\n"
    "v_mfma_f32_16x16x32_bf16 v[136:139], a[80:83], v[96:99], v[136:139]\n"
    "v_cvt_pk_bf16_f32 v161, v162, v163\n"
    "v_cvt_pk_bf16_f32 v162, v164, v165\n"
    "v_mfma_f32_16x16x32_bf16 v[136:139], a[112:115], v[112:115], v[136:139]\n"
    "ds_write_b128 v228, v[132:135] offset:1024\n"
    "v_cvt_pk_bf16_f32 v163, v166, v167\n"
    "v_mfma_f32_16x16x32_bf16 v[140:143], a[116:119], v[112:115], 0\n"
    "buffer_store_dwordx4 v[160:163], v233, s[32:35], s73 offen sc1\n"
    "v_mfma_f32_16x16x32_bf16 v[140:143], a[84:87], v[96:99], v[140:143]\n"
    "v_mfma_f32_16x16x32_bf16 v[140:143], a[52:55], v[80:83], v[140:143]\n"
    "v_mfma_f32_16x16x32_bf16 v[140:143], a[20:23], v[64:67], v[140:143]\n"
    "v_mfma_f32_16x16x32_bf16 v[140:143], a[244:247], v[48:51], v[140:143]\n"
    "v_mfma_f32_16x16x32_bf16 v[140:143], a[212:215], v[32:35], v[140:143]\n"
    "v_mfma_f32_16x16x32_bf16 v[140:143], a[180:183], v[16:19], v[140:143]\n"
    "v_mfma_f32_16x16x32_bf16 v[140:143], a[148:151], v[0:3], v[140:143]\n"
    "ds_write_b128 v228, v[136:139] offset:2048\n"
    "v_mfma_f32_16x16x32_bf16 v[144:147], a[152:155], v[0:3], 0\n"
    "v_mfma_f32_16x16x32_bf16 v[144:147], a[184:187], v[16:19], v[144:147]\n"
    "v_mfma_f32_16x16x32_bf16 v[144:147], a[216:219], v[32:35], v[144:147]\n"
    "v_mfma_f32_16x16x32_bf16 v[144:147], a[248:251], v[48:51], v[144:147]\n"
    "v_mfma_f32_16x16x32_bf16 v[144:147], a[24:27], v[64:67], v[144:147]\n"
    "v_mfma_f32_16x16x32_bf16 v[144:147], a[56:59], v[80:83], v[144:147]\n"
    "v_mfma_f32_16x16x32_bf16 v[144:147], a[88:91], v[96:99], v[144:147]\n"
    "v_mfma_f32_16x16x32_bf16 v[144:147], a[120:123], v[112:115], v[144:147]\n"
    "ds_write_b128 v228, v[140:143] offset:3072\n"
    "v_mfma_f32_16x16x32_bf16 v[148:151], a[124:127], v[112:115], 0\n"
    "v_mfma_f32_16x16x32_bf16 v[148:151], a[92:95], v[96:99], v[148:151]\n"
    "v_mfma_f32_16x16x32_bf16 v[148:151], a[60:63], v[80:83], v[148:151]\n"
    "v_mfma_f32_16x16x32_bf16 v[148:151], a[28:31], v[64:67], v[148:151]\n"
    "v_mfma_f32_16x16x32_bf16 v[148:151], a[252:255], v[48:51], v[148:151]\n"
    "v_mfma_f32_16x16x32_bf16 v[148:151], a[220:223], v[32:35], v[148:151]\n"
    "v_mfma_f32_16x16x32_bf16 v[148:151], a[188:191], v[16:19], v[148:151]\n"
    "v_mfma_f32_16x16x32_bf16 v[148:151], a[156:159], v[0:3], v[148:151]\n"
    "ds_write_b128 v228, v[144:147] offset:16384\n"
    "v_mfma_f32_16x16x32_bf16 v[152:155], a[128:131], v[0:3], 0\n"
    "s_waitcnt vmcnt(1)\n"
    "v_cmp_eq_u32 vcc, s66, v249\n"
    "v_mfma_f32_16x16x32_bf16 v[152:155], a[160:163], v[16:19], v[152:155]\n"
    "v_mfma_f32_16x16x32_bf16 v[152:155], a[192:195], v[32:35], v[152:155]\n"
    "s_and_b32 s55, vcc_lo, 3\n"
    "v_mfma_f32_16x16x32_bf16 v[152:155], a[224:227], v[48:51], v[152:155]\n"
    "s_cmp_eq_u32 s55, 3\n"
    "v_mfma_f32_16x16x32_bf16 v[152:155], a[0:3], v[64:67], v[152:155]\n"
    "s_cbranch_scc0 Lsp4%=\n"
    "Lok4%=:\n"
    "v_mfma_f32_16x16x32_bf16 v[152:155], a[32:35], v[80:83], v[152:155]\n"
    "buffer_load_dword v224, v245, s[36:39], 0 offen sc1\n"
    "v_mfma_f32_16x16x32_bf16 v[152:155], a[64:67], v[96:99], v[152:155]\n"
    "s_add_u32 s53, s93, 0\n"
    "v_mfma_f32_16x16x32_bf16 v[152:155], a[96:99], v[112:115], v[152:155]\n"
    "ds_write_b128 v228, v[148:151] offset:17408\n"
    "v_mfma_f32_16x16x32_bf16 v[156:159], a[100:103], v[112:115], 0\n"
    "buffer_load_dwordx4 v[72:75], v233, s[32:35], s53 offen sc1\n"
    "v_mfma_f32_16x16x32_bf16 v[156:159], a[68:71], v[96:99], v[156:159]\n"
    "s_add_u32 s53, s93, 131072\n"
    "v_mfma_f32_16x16x32_bf16 v[156:159], a[36:39], v[80:83], v[156:159]\n"
    "buffer_load_dwordx4 v[76:79], v233, s[32:35], s53 offen sc1\n"
    "v_mfma_f32_16x16x32_bf16 v[156:159], a[4:7], v[64:67], v[156:159]\n"
    "s_add_u32 s53, s93, 262144\n"
    "v_mfma_f32_16x16x32_bf16 v[156:159], a[228:231], v[48:51], v[156:159]\n"
    "buffer_load_dwordx4 v[104:107], v233, s[32:35], s53 offen sc1\n"
    "v_mfma_f32_16x16x32_bf16 v[156:159], a[196:199], v[32:35], v[156:159]\n"
    "s_add_u32 s53, s93, 393216\n"
    "v_mfma_f32_16x16x32_bf16 v[156:159], a[164:167], v[16:19], v[156:159]\n"
    "buffer_load_dwordx4 v[108:111], v233, s[32:35], s53 offen sc1\n"
    "v_mfma_f32_16x16x32_bf16 v[156:159], a[132:135], v[0:3], v[156:159]\n"
    "s_waitcnt vmcnt(7)\n"
    "s_waitcnt lgkmcnt(0)\n"
    "v_mfma_f32_16x16x32_bf16 v[128:131], a[136:139], v[4:7], 0\n"
    "s_barrier\n"
    "ds_read_b128 v[168:171], v229 offset:0\n"
    "ds_read_b128 v[172:175], v229 offset:1024\n"
    "v_mfma_f32_16x16x32_bf16 v[128:131], a[168:171], v[20:23], v[128:131]\n"
    "ds_read_b128 v[176:179], v230 offset:2048\n"
    "ds_read_b128 v[180:183], v230 offset:3072\n"
    "v_cmp_eq_u32 vcc, 6, v253\n"
    "v_mfma_f32_16x16x32_bf16 v[128:131], a[200:203], v[36:39], v[128:131]\n"
    "ds_read_b128 v[184:187], v231 offset:16384\n"
    "ds_read_b128 v[188:191], v231 offset:17408\n"
    "v_mfma_f32_16x16x32_bf16 v[128:131], a[232:235], v[52:55], v[128:131]\n"
    "v_cndmask_b32 v250, v254, v248, vcc\n"
    "v_mfma_f32_16x16x32_bf16 v[128:131], a[8:11], v[68:71], v[128:131]\n"
    "s_waitcnt vmcnt(5)\n"
    "buffer_store_dword v246, v250, s[36:39], 0 offen sc1\n"
    "v_mfma_f32_16x16x32_bf16 v[128:131], a[40:43], v[84:87], v[128:131]\n"
    "v_mfma_f32_16x16x32_bf16 v[128:131], a[72:75], v[100:103], v[128:131]\n"
    "v_mfma_f32_16x16x32_bf16 v[128:131], a[104:107], v[116:119], v[128:131]\n"
    "v_mfma_f32_16x16x32_bf16 v[132:135], a[108:111], v[116:119], 0\n"
    "v_mfma_f32_16x16x32_bf16 v[132:135], a[76:79], v[100:103], v[132:135]\n"
    "s_waitcnt lgkmcnt(3)\n"
    "v_add_f32 v168, v168, v176\n"
    "v_add_f32 v169, v169, v177\n"
    "v_mfma_f32_16x16x32_bf16 v[132:135], a[44:47], v[84:87], v[132:135]\n"
    "v_add_f32 v170, v170, v178\n"
    "v_add_f32 v171, v171, v179\n"
    "v_mfma_f32_16x16x32_bf16 v[132:135], a[12:15], v[68:71], v[132:135]\n"
    "s_waitcnt lgkmcnt(2)\n"
    "v_add_f32 v172, v172, v180\n"
    "v_add_f32 v173, v173, v181\n"
    "v_mfma_f32_16x16x32_bf16 v[132:135], a[236:239], v[52:55], v[132:135]\n"
    "v_add_f32 v174, v174, v182\n"
    "v_add_f32 v175, v175, v183\n"
    "v_mfma_f32_16x16x32_bf16 v[132:135], a[204:207], v[36:39], v[132:135]\n"
    "s_waitcnt lgkmcnt(1)\n"
    "v_add_f32 v152, v152, v184\n"
    "v_add_f32 v153, v153, v185\n"
    "v_mfma_f32_16x16x32_bf16 v[132:135], a[172:175], v[20:23], v[132:135]\n"
    "buffer_load_dword v249, v232, s[36:39], 0 offen offset:256 sc1\n"
    "v_add_f32 v154, v154, v186\n"
    "v_add_f32 v155, v155, v187\n"
    "v_mfma_f32_16x16x32_bf16 v[132:135], a[140:143], v[4:7], v[132:135]\n"
    "s_waitcnt lgkmcnt(0)\n"
    "ds_write_b128 v228, v[128:131] offset:32768\n"
    "v_add_f32 v156, v156, v188\n"
    "v_mfma_f32_16x16x32_bf16 v[136:139], a[144:147], v[4:7], 0\n"
    "v_add_f32 v157, v157, v189\n"
    "v_add_f32 v158, v158, v190\n"
    "v_mfma_f32_16x16x32_bf16 v[136:139], a[176:179], v[20:23], v[136:139]\n"
    "v_add_f32 v159, v159, v191\n"
    "v_add_f32 v152, v152, v168\n"
    "v_mfma_f32_16x16x32_bf16 v[136:139], a[208:211], v[36:39], v[136:139]\n"
    "v_add_f32 v153, v153, v169\n"
    "v_add_f32 v154, v154, v170\n"
    "v_mfma_f32_16x16x32_bf16 v[136:139], a[240:243], v[52:55], v[136:139]\n"
    "v_add_f32 v155, v155, v171\n"
    "v_add_f32 v156, v156, v172\n"
    "v_mfma_f32_16x16x32_bf16 v[136:139], a[16:19], v[68:71], v[136:139]\n"
    "v_add_f32 v157, v157, v173\n"
    "v_add_f32 v158, v158, v174\n"
    "v_mfma_f32_16x16x32_bf16 v[136:139], a[48:51], v[84:87], v[136:139]\n"
    "v_add_f32 v159, v159, v175\n"
    "v_mfma_f32_16x16x32_bf16 v[136:139], a[80:83], v[100:103], v[136:139]\n"
    "v_mfma_f32_16x16x32_bf16 v[136:139], a[112:115], v[116:119], v[136:139]\n"
    "ds_write_b128 v228, v[132:135] offset:33792\n"
    "v_mfma_f32_16x16x32_bf16 v[140:143], a[116:119], v[116:119], 0\n"
    "v_mfma_f32_16x16x32_bf16 v[140:143], a[84:87], v[100:103], v[140:143]\n"
    "v_mfma_f32_16x16x32_bf16 v[140:143], a[52:55], v[84:87], v[140:143]\n"
    "v_mfma_f32_16x16x32_bf16 v[140:143], a[20:23], v[68:71], v[140:143]\n"
    "v_mfma_f32_16x16x32_bf16 v[140:143], a[244:247], v[52:55], v[140:143]\n"
    "v_mfma_f32_16x16x32_bf16 v[140:143], a[212:215], v[36:39], v[140:143]\n"
    "v_mfma_f32_16x16x32_bf16 v[140:143], a[180:183], v[20:23], v[140:143]\n"
    "s_waitcnt vmcnt(6)\n"
    "s_waitcnt vmcnt(2)\n"
    "v_cmp_ne_u32 vcc, v224, v246\n"
    "v_mfma_f32_16x16x32_bf16 v[140:143], a[148:151], v[4:7], v[140:143]\n"
    "ds_write_b128 v228, v[136:139] offset:34816\n"
    "v_lshlrev_b32 v92, 16, v72\n"
    "v_mfma_f32_16x16x32_bf16 v[144:147], a[152:155], v[4:7], 0\n"
    "s_cbranch_vccnz Lrr%=\n"
    "Lrs%=:\n"
    "v_and_b32 v93, 0xffff0000, v72\n"
    "v_mfma_f32_16x16x32_bf16 v[144:147], a[184:187], v[20:23], v[144:147]\n"
    "v_add_f32 v8, v8, v92\n"
    "v_add_f32 v9, v9, v93\n"
    "v_mfma_f32_16x16x32_bf16 v[144:147], a[216:219], v[36:39], v[144:147]\n"
    "v_lshlrev_b32 v94, 16, v73\n"
    "v_and_b32 v95, 0xffff0000, v73\n"
    "v_mfma_f32_16x16x32_bf16 v[144:147], a[248:251], v[52:55], v[144:147]\n"
    "v_add_f32 v10, v10, v94\n"
    "v_add_f32 v11, v11, v95\n"
    "v_mfma_f32_16x16x32_bf16 v[144:147], a[24:27], v[68:71], v[144:147]\n"
    "v_lshlrev_b32 v92, 16, v74\n"
    "v_and_b32 v93, 0xffff0000, v74\n"
    "v_mfma_f32_16x16x32_bf16 v[144:147], a[56:59], v[84:87], v[144:147]\n"
    "v_add_f32 v12, v12, v92\n"
    "v_add_f32 v13, v13, v93\n"
    "v_mfma_f32_16x16x32_bf16 v[144:147], a[88:91], v[100:103], v[144:147]\n"
    "v_lshlrev_b32 v94, 16, v75\n"
    "v_and_b32 v95, 0xffff0000, v75\n"
    "v_mfma_f32_16x16x32_bf16 v[144:147], a[120:123], v[116:119], v[144:147]\n"
    "ds_write_b128 v228, v[140:143] offset:35840\n"
    "v_add_f32 v14, v14, v94\n"
    "v_mfma_f32_16x16x32_bf16 v[148:151], a[124:127], v[116:119], 0\n"
    "v_add_f32 v15, v15, v95\n"
    "v_lshlrev_b32 v92, 16, v76\n"
    "v_mfma_f32_16x16x32_bf16 v[148:151], a[92:95], v[100:103], v[148:151]\n"
    "v_and_b32 v93, 0xffff0000, v76\n"
    "v_add_f32 v40, v40, v92\n"
    "v_mfma_f32_16x16x32_bf16 v[148:151], a[60:63], v[84:87], v[148:151]\n"
    "v_add_f32 v41, v41, v93\n"
    "v_lshlrev_b32 v94, 16, v77\n"
    "v_mfma_f32_16x16x32_bf16 v[148:151], a[28:31], v[68:71], v[148:151]\n"
    "v_and_b32 v95, 0xffff0000, v77\n"
    "v_add_f32 v42, v42, v94\n"
    "v_mfma_f32_16x16x32_bf16 v[148:151], a[252:255], v[52:55], v[148:151]\n"
    "v_add_f32 v43, v43, v95\n"
    "v_lshlrev_b32 v92, 16, v78\n"
    "v_mfma_f32_16x16x32_bf16 v[148:151], a[220:223], v[36:39], v[148:151]\n"
    "v_and_b32 v93, 0xffff0000, v78\n"
    "v_add_f32 v44, v44, v92\n"
    "v_mfma_f32_16x16x32_bf16 v[148:151], a[188:191], v[20:23], v[148:151]\n"
    "v_add_f32 v45, v45, v93\n"
    "v_lshlrev_b32 v94, 16, v79\n"
    "v_mfma_f32_16x16x32_bf16 v[148:151], a[156:159], v[4:7], v[148:151]\n"
    "ds_write_b128 v228, v[144:147] offset:49152\n"
    "v_and_b32 v95, 0xffff0000, v79\n"
    "v_mfma_f32_16x16x32_bf16 v[160:163], a[128:131], v[4:7], 0\n"
    "s_waitcnt vmcnt(0)\n"
    "v_cmp_eq_u32 vcc, s66, v249\n"
    "v_add_f32 v46, v46, v94\n"
    "v_mfma_f32_16x16x32_bf16 v[160:163], a[160:163], v[20:23], v[160:163]\n"
    "v_add_f32 v47, v47, v95\n"
    "v_lshlrev_b32 v92, 16, v104\n"
    "v_mfma_f32_16x16x32_bf16 v[160:163], a[192:195], v[36:39], v[160:163]\n"
    "s_and_b32 s55, vcc_lo, 1\n"
    "v_and_b32 v93, 0xffff0000, v104\n"
    "v_add_f32 v8, v8, v92\n"
    "v_mfma_f32_16x16x32_bf16 v[160:163], a[224:227], v[52:55], v[160:163]\n"
    "s_cmp_eq_u32 s55, 1\n"
    "v_add_f32 v9, v9, v93\n"
    "v_lshlrev_b32 v94, 16, v105\n"
    "v_mfma_f32_16x16x32_bf16 v[160:163], a[0:3], v[68:71], v[160:163]\n"
    "s_cbranch_scc0 Lsp5%=\n"
    "Lok5%=:\n"
    "v_and_b32 v95, 0xffff0000, v105\n"
    "v_mfma_f32_16x16x32_bf16 v[160:163], a[32:35], v[84:87], v[160:163]\n"
    "s_add_u32 s53, s93, 524288\n"
    "v_add_f32 v10, v10, v94\n"
    "v_add_f32 v11, v11, v95\n"
    "v_mfma_f32_16x16x32_bf16 v[160:163], a[64:67], v[100:103], v[160:163]\n"
    "buffer_load_dwordx4 v[120:123], v233, s[32:35], s53 offen sc1\n"
    "v_mfma_f32_16x16x32_bf16 v[160:163], a[96:99], v[116:119], v[160:163]\n"
    "s_add_u32 s53, s93, 655360\n"
    "ds_write_b128 v228, v[148:151] offset:50176\n"
    "v_lshlrev_b32 v92, 16, v106\n"
    "v_mfma_f32_16x16x32_bf16 v[164:167], a[100:103], v[116:119], 0\n"
    "buffer_load_dwordx4 v[124:127], v233, s[32:35], s53 offen sc1\n"
    "v_mfma_f32_16x16x32_bf16 v[164:167], a[68:71], v[100:103], v[164:167]\n"
    "v_and_b32 v93, 0xffff0000, v106\n"
    "v_add_f32 v12, v12, v92\n"
    "v_mfma_f32_16x16x32_bf16 v[164:167], a[36:39], v[84:87], v[164:167]\n"
    "Lpg15%=:\n"
    "s_cbranch_execz Lwb2%=\n"
    "buffer_load_dword v249, v245, s[36:39], 0 offen sc1\n"
    "v_mfma_f32_16x16x32_bf16 v[164:167], a[4:7], v[68:71], v[164:167]\n"
    "v_add_f32 v13, v13, v93\n"
    "v_lshlrev_b32 v94, 16, v107\n"
    "v_mfma_f32_16x16x32_bf16 v[164:167], a[228:231], v[52:55], v[164:167]\n"
    "v_and_b32 v95, 0xffff0000, v107\n"
    "v_add_f32 v14, v14, v94\n"
    "v_mfma_f32_16x16x32_bf16 v[164:167], a[196:199], v[36:39], v[164:167]\n"
    "v_add_f32 v15, v15, v95\n"
    "v_lshlrev_b32 v92, 16, v108\n"
    "v_mfma_f32_16x16x32_bf16 v[164:167], a[164:167], v[20:23], v[164:167]\n"
    "s_waitcnt lgkmcnt(0)\n"
    "v_and_b32 v93, 0xffff0000, v108\n"
    "v_add_f32 v40, v40, v92\n"
    "v_mfma_f32_16x16x32_bf16 v[164:167], a[132:135], v[4:7], v[164:167]\n"
    "s_barrier\n"
    "ds_read_b128 v[168:171], v229 offset:32768\n"
    "ds_read_b128 v[172:175], v229 offset:33792\n"
    "ds_read_b128 v[176:179], v230 offset:34816\n"
    "ds_read_b128 v[180:183], v230 offset:35840\n"
    "ds_read_b128 v[184:187], v231 offset:49152\n"
    "ds_read_b128 v[188:191], v231 offset:50176\n"
    "v_add_f32 v41, v41, v93\n"
    "v_lshlrev_b32 v94, 16, v109\n"
    "v_and_b32 v95, 0xffff0000, v109\n"
    "v_add_f32 v42, v42, v94\n"
    "v_add_f32 v43, v43, v95\n"
    "v_lshlrev_b32 v92, 16, v110\n"
    "v_and_b32 v93, 0xffff0000, v110\n"
    "v_add_f32 v44, v44, v92\n"
    "v_add_f32 v45, v45, v93\n"
    "v_lshlrev_b32 v94, 16, v111\n"
    "v_and_b32 v95, 0xffff0000, v111\n"
    "v_add_f32 v46, v46, v94\n"
    "v_add_f32 v47, v47, v95\n"
    "v_add_f32 v8, v8, v152\n"
    "v_add_f32 v9, v9, v153\n"
    "v_add_f32 v10, v10, v154\n"
    "v_add_f32 v11, v11, v155\n"
    "v_add_f32 v12, v12, v156\n"
    "v_add_f32 v13, v13, v157\n"
    "v_add_f32 v14, v14, v158\n"
    "v_add_f32 v15, v15, v159\n"
    "v_add_f32 v40, v40, v160\n"
    "v_add_f32 v41, v41, v161\n"
    "v_add_f32 v42, v42, v162\n"
    "v_add_f32 v43, v43, v163\n"
    "v_add_f32 v44, v44, v164\n"
    "v_add_f32 v45, v45, v165\n"
    "v_add_f32 v46, v46, v166\n"
    "v_add_f32 v47, v47, v167\n"
    "s_waitcnt lgkmcnt(5)\n"
    "v_add_f32 v40, v40, v168\n"
    "v_add_f32 v41, v41, v169\n"
    "v_add_f32 v42, v42, v170\n"
    "v_add_f32 v43, v43, v171\n"
    "s_waitcnt lgkmcnt(4)\n"
    "v_add_f32 v44, v44, v172\n"
    "v_add_f32 v45, v45, v173\n"
    "v_add_f32 v46, v46, v174\n"
    "v_add_f32 v47, v47, v175\n"
    "s_waitcnt lgkmcnt(3)\n"
    "v_add_f32 v40, v40, v176\n"
    "v_add_f32 v41, v41, v177\n"
    "v_add_f32 v42, v42, v178\n"
    "v_add_f32 v43, v43, v179\n"
    "s_waitcnt lgkmcnt(2)\n"
    "v_add_f32 v44, v44, v180\n"
    "v_add_f32 v45, v45, v181\n"
    "v_add_f32 v46, v46, v182\n"
    "v_add_f32 v47, v47, v183\n"
    "s_waitcnt lgkmcnt(1)\n"
    "v_add_f32 v40, v40, v184\n"
    "v_add_f32 v41, v41, v185\n"
    "v_add_f32 v42, v42, v186\n"
    "v_add_f32 v43, v43, v187\n"
    "s_waitcnt lgkmcnt(0)\n"
    "v_add_f32 v44, v44, v188\n"
    "v_add_f32 v45, v45, v189\n"
    "v_add_f32 v46, v46, v190\n"
    "v_add_f32 v47, v47, v191\n"
    "s_waitcnt vmcnt(1)\n"
    "v_lshlrev_b32 v168, 16, v120\n"
    "v_and_b32 v169, 0xffff0000, v120\n"
    "v_lshlrev_b32 v170, 16, v121\n"
    "v_and_b32 v171, 0xffff0000, v121\n"
    "v_lshlrev_b32 v172, 16, v122\n"
    "v_and_b32 v173, 0xffff0000, v122\n"
    "v_lshlrev_b32 v174, 16, v123\n"
    "v_and_b32 v175, 0xffff0000, v123\n"
    "v_add_f32 v8, v8, v168\n"
    "v_add_f32 v9, v9, v169\n"
    "v_add_f32 v10, v10, v170\n"
    "v_add_f32 v11, v11, v171\n"
    "v_add_f32 v12, v12, v172\n"
    "v_add_f32 v13, v13, v173\n"
    "v_add_f32 v14, v14, v174\n"
    "v_add_f32 v15, v15, v175\n"
    "v_cvt_pk_bf16_f32 v8, v8, v9\n"
    "v_cvt_pk_bf16_f32 v9, v10, v11\n"
    "v_cvt_pk_bf16_f32 v10, v12, v13\n"
    "v_cvt_pk_bf16_f32 v11, v14, v15\n"
    "s_nop 0\n"
    "v_permlane16_swap_b32 v8, v10\n"
    "v_permlane16_swap_b32 v9, v11\n"
    "buffer_store_dwordx4 v[8:11], v251, s[40:43], s92 offen\n"
    "v_lshlrev_b32 v176, 16, v124\n"
    "v_and_b32 v177, 0xffff0000, v124\n"
    "v_lshlrev_b32 v178, 16, v125\n"
    "v_and_b32 v179, 0xffff0000, v125\n"
    "v_lshlrev_b32 v180, 16, v126\n"
    "v_and_b32 v181, 0xffff0000, v126\n"
    "v_lshlrev_b32 v182, 16, v127\n"
    "v_and_b32 v183, 0xffff0000, v127\n"
    "v_add_f32 v40, v40, v176\n"
    "v_add_f32 v41, v41, v177\n"
    "v_add_f32 v42, v42, v178\n"
    "v_add_f32 v43, v43, v179\n"
    "v_add_f32 v44, v44, v180\n"
    "v_add_f32 v45, v45, v181\n"
    "v_add_f32 v46, v46, v182\n"
    "v_add_f32 v47, v47, v183\n"
    "v_cvt_pk_bf16_f32 v40, v40, v41\n"
    "v_cvt_pk_bf16_f32 v41, v42, v43\n"
    "v_cvt_pk_bf16_f32 v42, v44, v45\n"
    "v_cvt_pk_bf16_f32 v43, v46, v47\n"
    "s_nop 0\n"
    "v_permlane16_swap_b32 v40, v42\n"
    "v_permlane16_swap_b32 v41, v43\n"
    "buffer_store_dwordx4 v[40:43], v251, s[40:43], s52 offen\n"
    "s_waitcnt vmcnt(2)\n"
    "v_cmp_ne_u32 vcc, v249, v246\n"
    "s_nop 1\n"
    "s_cbranch_vccz Ldone%=\n"
    "buffer_load_dword v246, v245, s[36:39], 0 offen sc1\n"
    "s_waitcnt vmcnt(0)\n"
    "buffer_store_dword v246, v247, s[36:39], 0 offen offset:0 sc1\n"
    "buffer_store_dword v246, v247, s[36:39], 0 offen offset:128 sc1\n"
    "buffer_store_dword v246, v247, s[36:39], 0 offen offset:256 sc1\n"
    "buffer_store_dword v246, v247, s[36:39], 0 offen offset:384 sc1\n"
    "buffer_store_dword v246, v248, s[36:39], 0 offen sc1\n"
    "s_waitcnt vmcnt(0)\n"
    "Ldone%=:\n"
    "s_endpgm\n"
    "Lpa1x%=:\n"
    "s_min_i32 s11, s11, 20\n"
    "Lpc1x%=:\n"
    "s_nop 7\n"
    "s_sub_u32 s11, s11, 3\n"
    "s_cmp_gt_i32 s11, 2\n"
    "s_cbranch_scc1 Lpc1x%=\n"
    "s_branch Lpb1x%=\n"
    "Lpa2x%=:\n"
    "s_min_i32 s11, s11, 20\n"
    "Lpc2x%=:\n"
    "s_nop 7\n"
    "s_sub_u32 s11, s11, 3\n"
    "s_cmp_gt_i32 s11, 2\n"
    "s_cbranch_scc1 Lpc2x%=\n"
    "s_branch Lpb2x%=\n"
    "Lpa3x%=:\n"
    "s_min_i32 s11, s11, 20\n"
    "Lpc3x%=:\n"
    "s_nop 7\n"
    "s_sub_u32 s11, s11, 3\n"
    "s_cmp_gt_i32 s11, 2\n"
    "s_cbranch_scc1 Lpc3x%=\n"
    "s_branch Lpb3x%=\n"
    "Lpa4x%=:\n"
    "s_min_i32 s11, s11, 20\n"
    "Lpc4x%=:\n"
    "s_nop 7\n"
    "s_sub_u32 s11, s11, 3\n"
    "s_cmp_gt_i32 s11, 2\n"
    "s_cbranch_scc1 Lpc4x%=\n"
    "s_branch Lpb4x%=\n"
    "Lpa5x%=:\n"
    "s_min_i32 s11, s11, 20\n"
    "Lpc5x%=:\n"
    "s_nop 7\n"
    "s_sub_u32 s11, s11, 3\n"
    "s_cmp_gt_i32 s11, 2\n"
    "s_cbranch_scc1 Lpc5x%=\n"
    "s_branch Lpb5x%=\n"
    "Lpa6x%=:\n"
    "s_min_i32 s11, s11, 20\n"
    "Lpc6x%=:\n"
    "s_nop 7\n"
    "s_sub_u32 s11, s11, 3\n"
    "s_cmp_gt_i32 s11, 2\n"
    "s_cbranch_scc1 Lpc6x%=\n"
    "s_branch Lpb6x%=\n"
    "Lpa7x%=:\n"
    "s_min_i32 s11, s11, 20\n"
    "Lpc7x%=:\n"
    "s_nop 7\n"
    "s_sub_u32 s11, s11, 3\n"
    "s_cmp_gt_i32 s11, 2\n"
    "s_cbranch_scc1 Lpc7x%=\n"
    "s_branch Lpb7x%=\n"
    "Lpa8x%=:\n"
    "s_min_i32 s11, s11, 20\n"
    "Lpc8x%=:\n"
    "s_nop 7\n"
    "s_sub_u32 s11, s11, 3\n"
    "s_cmp_gt_i32 s11, 2\n"
    "s_cbranch_scc1 Lpc8x%=\n"
    "s_branch Lpb8x%=\n"
    "Lpa9x%=:\n"
    "s_min_i32 s11, s11, 20\n"
    "Lpc9x%=:\n"
    "s_nop 7\n"
    "s_sub_u32 s11, s11, 3\n"
    "s_cmp_gt_i32 s11, 2\n"
    "s_cbranch_scc1 Lpc9x%=\n"
    "s_branch Lpb9x%=\n"
    "Lpa10x%=:\n"
    "s_min_i32 s11, s11, 20\n"
    "Lpc10x%=:\n"
    "s_nop 7\n"
    "s_sub_u32 s11, s11, 3\n"
    "s_cmp_gt_i32 s11, 2\n"
    "s_cbranch_scc1 Lpc10x%=\n"
    "s_branch Lpb10x%=\n"
    "Lpa11x%=:\n"
    "s_min_i32 s11, s11, 20\n"
    "Lpc11x%=:\n"
    "s_nop 7\n"
    "s_sub_u32 s11, s11, 3\n"
    "s_cmp_gt_i32 s11, 2\n"
    "s_cbranch_scc1 Lpc11x%=\n"
    "s_branch Lpb11x%=\n"
    "Lpa12x%=:\n"
    "s_min_i32 s11, s11, 20\n"
    "Lpc12x%=:\n"
    "s_nop 7\n"
    "s_sub_u32 s11, s11, 3\n"
    "s_cmp_gt_i32 s11, 2\n"
    "s_cbranch_scc1 Lpc12x%=\n"
    "s_branch Lpb12x%=\n"
    "Lend%=:\n"
    "Lsp0%=:\n"
    "s_mov_b32 s67, 0\n"
    "Lsq0%=:\n"
    "buffer_load_dword v249, v244, s[36:39], 0 offen offset:0 sc1\n"
    "s_add_u32 s67, s67, 1\n"
    "s_waitcnt vmcnt(0)\n"
    "v_cmp_eq_u32 vcc, s66, v249\n"
    "s_cmp_gt_u32 s67, 0x40000\n"
    "s_cbranch_scc1 Lok0%=\n"
    "s_and_b32 s55, vcc_lo, 4369\n"
    "s_cmp_eq_u32 s55, 4369\n"
    "s_cbranch_scc0 Lsq0%=\n"
    "s_branch Lok0%=\n"
    "Lsp1%=:\n"
    "s_mov_b32 s67, 0\n"
    "Lsq1%=:\n"
    "buffer_load_dword v249, v244, s[36:39], 0 offen offset:128 sc1\n"
    "s_add_u32 s67, s67, 1\n"
    "s_waitcnt vmcnt(0)\n"
    "v_cmp_eq_u32 vcc, s66, v249\n"
    "s_cmp_gt_u32 s67, 0x40000\n"
    "s_cbranch_scc1 Lok1%=\n"
    "s_and_b32 s55, vcc_lo, 4369\n"
    "s_cmp_eq_u32 s55, 4369\n"
    "s_cbranch_scc0 Lsq1%=\n"
    "s_branch Lok1%=\n"
    "Lrp%=:\n"
    "buffer_load_dword v246, v245, s[36:39], 0 offen sc1\n"
    "s_waitcnt vmcnt(0)\n"
    "buffer_store_dword v246, v247, s[36:39], 0 offen offset:0 sc1\n"
    "buffer_store_dword v246, v247, s[36:39], 0 offen offset:128 sc1\n"
    "buffer_store_dword v246, v247, s[36:39], 0 offen offset:256 sc1\n"
    "s_waitcnt vmcnt(0)\n"
    "s_branch Lrq%=\n"
    "Lsp2%=:\n"
    "s_mov_b32 s67, 0\n"
    "Lsq2%=:\n"
    "buffer_load_dword v249, v244, s[36:39], 0 offen offset:256 sc1\n"
    "s_add_u32 s67, s67, 1\n"
    "s_waitcnt vmcnt(0)\n"
    "v_cmp_eq_u32 vcc, s66, v249\n"
    "s_cmp_gt_u32 s67, 0x40000\n"
    "s_cbranch_scc1 Lok2%=\n"
    "s_and_b32 s55, vcc_lo, 4369\n"
    "s_cmp_eq_u32 s55, 4369\n"
    "s_cbranch_scc0 Lsq2%=\n"
    "s_branch Lok2%=\n"
    "Lsp3%=:\n"
    "s_mov_b32 s67, 0\n"
    "Lsq3%=:\n"
    "buffer_load_dword v249, v244, s[36:39], 0 offen offset:384 sc1\n"
    "s_add_u32 s67, s67, 1\n"
    "s_waitcnt vmcnt(0)\n"
    "v_cmp_eq_u32 vcc, s66, v249\n"
    "s_cmp_gt_u32 s67, 0x40000\n"
    "s_cbranch_scc1 Lok3%=\n"
    "s_and_b32 s55, vcc_lo, 4369\n"
    "s_cmp_eq_u32 s55, 4369\n"
    "s_cbranch_scc0 Lsq3%=\n"
    "s_branch Lok3%=\n"
    "Lsp4%=:\n"
    "s_mov_b32 s67, 0\n"
    "Lsq4%=:\n"
    "buffer_load_dword v249, v232, s[36:39], 0 offen offset:0 sc1\n"
    "s_add_u32 s67, s67, 1\n"
    "s_waitcnt vmcnt(0)\n"
    "v_cmp_eq_u32 vcc, s66, v249\n"
    "s_cmp_gt_u32 s67, 0x40000\n"
    "s_cbranch_scc1 Lok4%=\n"
    "s_and_b32 s55, vcc_lo, 3\n"
    "s_cmp_eq_u32 s55, 3\n"
    "s_cbranch_scc0 Lsq4%=\n"
    "s_branch Lok4%=\n"
    "Lrr%=:\n"
    "buffer_load_dword v246, v245, s[36:39], 0 offen sc1\n"
    "s_waitcnt vmcnt(0)\n"
    "buffer_store_dword v246, v248, s[36:39], 0 offen sc1\n"
    "s_waitcnt vmcnt(0)\n"
    "s_branch Lrs%=\n"
    "Lsp5%=:\n"
    "s_mov_b32 s67, 0\n"
    "Lsq5%=:\n"
    "buffer_load_dword v249, v232, s[36:39], 0 offen offset:256 sc1\n"
    "s_add_u32 s67, s67, 1\n"
    "s_waitcnt vmcnt(0)\n"
    "v_cmp_eq_u32 vcc, s66, v249\n"
    "s_cmp_gt_u32 s67, 0x40000\n"
    "s_cbranch_scc1 Lok5%=\n"
    "s_and_b32 s55, vcc_lo, 1\n"
    "s_cmp_eq_u32 s55, 1\n"
    "s_cbranch_scc0 Lsq5%=\n"
    "s_branch Lok5%=\n"
    "Lfall%=:\n"
    "s_waitcnt vmcnt(0) lgkmcnt(0)\n"
    "s_nop 15\n"
    "s_barrier\n"
    "s_mov_b64 s[64:65], 0\n"
    "s_add_u32 s56, s51, 49152\n"
    "s_add_u32 s58, s61, 49152\n"
    "s_and_b32 s58, s58, 0x3ffff\n"
    "s_add_u32 s57, s51, 98304\n"
    "s_add_u32 s59, s61, 32768\n"
    "s_and_b32 s59, s59, 0x3ffff\n"
    "v_mov_b32 v224, 0\n"
    "v_mov_b32 v226, 0\n"
    "v_and_b32 v228, 0xffff7fff, v228\n"
    "v_and_b32 v229, 0xffff7fff, v229\n"
    "v_and_b32 v230, 0xffff7fff, v230\n"
    "v_and_b32 v249, 15, v253\n"
    "v_lshlrev_b32 v249, 8, v249\n"
    "v_and_b32 v250, 48, v253\n"
    "v_add_u32 v249, v249, v250\n"
    "s_mov_b32 s14, 0\n"
    "v_mov_b32 v225, 2147418112\n"
    "v_mov_b32 v208, 0xff800000\n"
    "v_mov_b32 v212, 0xff800000\n"
    "v_mov_b32 v216, 0xff800000\n"
    "v_mov_b32 v220, 0xff800000\n"
    "s_mov_b32 s52, 0\n"
    "Lmx%=:\n"
    "buffer_load_dwordx4 a[64:67], v249, s[16:19], s52 offen offset:0\n"
    "buffer_load_dwordx4 a[68:71], v249, s[16:19], s52 offen offset:64\n"
    "buffer_load_dwordx4 a[72:75], v249, s[16:19], s52 offen offset:128\n"
    "buffer_load_dwordx4 a[76:79], v249, s[16:19], s52 offen offset:192\n"
    "s_waitcnt vmcnt(0)\n"
    "v_mfma_f32_16x16x32_bf16 v[192:195], a[64:67], a[0:3], 0\n"
    "v_mfma_f32_16x16x32_bf16 v[192:195], a[68:71], a[4:7], v[192:195]\n"
    "v_mfma_f32_16x16x32_bf16 v[192:195], a[72:75], a[8:11], v[192:195]\n"
    "v_mfma_f32_16x16x32_bf16 v[192:195], a[76:79], a[12:15], v[192:195]\n"
    "v_mfma_f32_16x16x32_bf16 v[196:199], a[64:67], a[16:19], 0\n"
    "v_mfma_f32_16x16x32_bf16 v[196:199], a[68:71], a[20:23], v[196:199]\n"
    "v_mfma_f32_16x16x32_bf16 v[196:199], a[72:75], a[24:27], v[196:199]\n"
    "v_mfma_f32_16x16x32_bf16 v[196:199], a[76:79], a[28:31], v[196:199]\n"
    "v_mfma_f32_16x16x32_bf16 v[200:203], a[64:67], a[32:35], 0\n"
    "v_mfma_f32_16x16x32_bf16 v[200:203], a[68:71], a[36:39], v[200:203]\n"
    "v_mfma_f32_16x16x32_bf16 v[200:203], a[72:75], a[40:43], v[200:203]\n"
    "v_mfma_f32_16x16x32_bf16 v[200:203], a[76:79], a[44:47], v[200:203]\n"
    "v_mfma_f32_16x16x32_bf16 v[204:207], a[64:67], a[48:51], 0\n"
    "v_mfma_f32_16x16x32_bf16 v[204:207], a[68:71], a[52:55], v[204:207]\n"
    "v_mfma_f32_16x16x32_bf16 v[204:207], a[72:75], a[56:59], v[204:207]\n"
    "v_mfma_f32_16x16x32_bf16 v[204:207], a[76:79], a[60:63], v[204:207]\n"
    "s_nop 7\n"
    "v_max3_f32 v208, v208, v192, v193\n"
    "v_max3_f32 v208, v208, v194, v195\n"
    "v_max3_f32 v212, v212, v196, v197\n"
    "v_max3_f32 v212, v212, v198, v199\n"
    "v_max3_f32 v216, v216, v200, v201\n"
    "v_max3_f32 v216, v216, v202, v203\n"
    "v_max3_f32 v220, v220, v204, v205\n"
    "v_max3_f32 v220, v220, v206, v207\n"
    "s_add_u32 s52, s52, 4096\n"
    "s_cmp_lt_u32 s52, 262144\n"
    "s_cbranch_scc1 Lmx%=\n"
    "s_add_u32 m0, s51, 0\n"
    "s_add_u32 s52, s61, 0\n"
    "s_and_b32 s52, s52, 0x3ffff\n"
    "buffer_load_dwordx4 v231, s[16:19], s52 offen offset:0 nt lds\n"
    "buffer_load_dwordx4 v232, s[16:19], s52 offen offset:1024 nt lds\n"
    "buffer_load_dwordx4 v231, s[16:19], s52 offen offset:2048 nt lds\n"
    "buffer_load_dwordx4 v232, s[16:19], s52 offen offset:3072 nt lds\n"
    "s_add_u32 m0, s51, 16384\n"
    "s_add_u32 s52, s61, 16384\n"
    "s_and_b32 s52, s52, 0x3ffff\n"
    "buffer_load_dwordx4 v231, s[16:19], s52 offen offset:0 nt lds\n"
    "buffer_load_dwordx4 v232, s[16:19], s52 offen offset:1024 nt lds\n"
    "buffer_load_dwordx4 v231, s[16:19], s52 offen offset:2048 nt lds\n"
    "buffer_load_dwordx4 v232, s[16:19], s52 offen offset:3072 nt lds\n"
    "s_add_u32 m0, s51, 65536\n"
    "s_add_u32 s52, s61, 0\n"
    "s_and_b32 s52, s52, 0x3ffff\n"
    "buffer_load_dwordx4 v231, s[20:23], s52 offen offset:0 nt lds\n"
    "buffer_load_dwordx4 v232, s[20:23], s52 offen offset:1024 nt lds\n"
    "buffer_load_dwordx4 v231, s[20:23], s52 offen offset:2048 nt lds\n"
    "buffer_load_dwordx4 v232, s[20:23], s52 offen offset:3072 nt lds\n"
    "s_add_u32 m0, s51, 32768\n"
    "s_add_u32 s52, s61, 32768\n"
    "s_and_b32 s52, s52, 0x3ffff\n"
    "buffer_load_dwordx4 v231, s[16:19], s52 offen offset:0 nt lds\n"
    "buffer_load_dwordx4 v232, s[16:19], s52 offen offset:1024 nt lds\n"
    "buffer_load_dwordx4 v231, s[16:19], s52 offen offset:2048 nt lds\n"
    "buffer_load_dwordx4 v232, s[16:19], s52 offen offset:3072 nt lds\n"
    "s_add_u32 m0, s51, 81920\n"
    "s_add_u32 s52, s61, 16384\n"
    "s_and_b32 s52, s52, 0x3ffff\n"
    "buffer_load_dwordx4 v231, s[20:23], s52 offen offset:0 nt lds\n"
    "buffer_load_dwordx4 v232, s[20:23], s52 offen offset:1024 nt lds\n"
    "buffer_load_dwordx4 v231, s[20:23], s52 offen offset:2048 nt lds\n"
    "buffer_load_dwordx4 v232, s[20:23], s52 offen offset:3072 nt lds\n"
    "s_add_u32 m0, s51, 49152\n"
    "s_add_u32 s52, s61, 49152\n"
    "s_and_b32 s52, s52, 0x3ffff\n"
    "buffer_load_dwordx4 v231, s[16:19], s52 offen offset:0 nt lds\n"
    "buffer_load_dwordx4 v232, s[16:19], s52 offen offset:1024 nt lds\n"
    "s_waitcnt vmcnt(0)\n"
    "s_barrier\n"
    "ds_read_b128 a[64:67], v228 offset:0\n"
    "ds_read_b128 a[68:71], v228 offset:256\n"
    "ds_read_b128 a[72:75], v228 offset:512\n"
    "ds_read_b128 a[76:79], v228 offset:768\n"
    "ds_read_b128 a[80:83], v228 offset:4096\n"
    "ds_read_b128 a[84:87], v228 offset:4352\n"
    "ds_read_b128 a[88:91], v228 offset:4608\n"
    "ds_read_b128 a[92:95], v228 offset:4864\n"
    "v_mov_b32 v209, v208\n"
    "v_mov_b32 v213, v212\n"
    "v_mov_b32 v217, v216\n"
    "v_mov_b32 v221, v220\n"
    "v_permlane16_swap_b32 v208, v209\n"
    "v_permlane16_swap_b32 v212, v213\n"
    "v_permlane16_swap_b32 v216, v217\n"
    "v_permlane16_swap_b32 v220, v221\n"
    "v_max_f32 v208, v208, v209\n"
    "v_max_f32 v212, v212, v213\n"
    "v_max_f32 v216, v216, v217\n"
    "v_max_f32 v220, v220, v221\n"
    "v_mov_b32 v209, v208\n"
    "v_mov_b32 v213, v212\n"
    "v_mov_b32 v217, v216\n"
    "v_mov_b32 v221, v220\n"
    "v_permlane32_swap_b32 v208, v209\n"
    "v_permlane32_swap_b32 v212, v213\n"
    "v_permlane32_swap_b32 v216, v217\n"
    "v_permlane32_swap_b32 v220, v221\n"
    "v_min_f32_e64 v210, -v208, -v209\n"
    "v_min_f32_e64 v214, -v212, -v213\n"
    "v_min_f32_e64 v218, -v216, -v217\n"
    "v_min_f32_e64 v222, -v220, -v221\n"
    "v_mov_b32 v208, v210\n"
    "v_mov_b32 v212, v214\n"
    "v_mov_b32 v216, v218\n"
    "v_mov_b32 v220, v222\n"
    "v_mov_b32 v209, v210\n"
    "v_mov_b32 v213, v214\n"
    "v_mov_b32 v217, v218\n"
    "v_mov_b32 v221, v222\n"
    "v_mov_b32 v211, v210\n"
    "v_mov_b32 v215, v214\n"
    "v_mov_b32 v219, v218\n"
    "s_waitcnt lgkmcnt(7)\n"
    "v_mfma_f32_16x16x32_bf16 v[192:195], a[64:67], a[0:3], v[208:211]\n"
    "s_waitcnt lgkmcnt(6)\n"
    "v_mov_b32 v223, v222\n"
    "v_mfma_f32_16x16x32_bf16 v[192:195], a[68:71], a[4:7], v[192:195]\n"
    "s_waitcnt lgkmcnt(5)\n"
    "v_mfma_f32_16x16x32_bf16 v[192:195], a[72:75], a[8:11], v[192:195]\n"
    "s_waitcnt lgkmcnt(4)\n"
    "v_mfma_f32_16x16x32_bf16 v[192:195], a[76:79], a[12:15], v[192:195]\n"
    "v_mfma_f32_16x16x32_bf16 v[196:199], a[64:67], a[16:19], v[212:215]\n"
    "v_mfma_f32_16x16x32_bf16 v[196:199], a[68:71], a[20:23], v[196:199]\n"
    "v_mfma_f32_16x16x32_bf16 v[196:199], a[72:75], a[24:27], v[196:199]\n"
    "v_mfma_f32_16x16x32_bf16 v[196:199], a[76:79], a[28:31], v[196:199]\n"
    "v_mfma_f32_16x16x32_bf16 v[200:203], a[64:67], a[32:35], v[216:219]\n"
    "v_mfma_f32_16x16x32_bf16 v[200:203], a[68:71], a[36:39], v[200:203]\n"
    "v_mfma_f32_16x16x32_bf16 v[200:203], a[72:75], a[40:43], v[200:203]\n"
    "v_mfma_f32_16x16x32_bf16 v[200:203], a[76:79], a[44:47], v[200:203]\n"
    "v_exp_f32 v192, v192\n"
    "v_mfma_f32_16x16x32_bf16 v[204:207], a[64:67], a[48:51], v[220:223]\n"
    "ds_read_b128 a[64:67], v228 offset:8192\n"
    "v_mfma_f32_16x16x32_bf16 v[204:207], a[68:71], a[52:55], v[204:207]\n"
    "ds_read_b128 a[68:71], v228 offset:8448\n"
    "v_mfma_f32_16x16x32_bf16 v[204:207], a[72:75], a[56:59], v[204:207]\n"
    "ds_read_b128 a[72:75], v228 offset:8704\n"
    "v_mfma_f32_16x16x32_bf16 v[204:207], a[76:79], a[60:63], v[204:207]\n"
    "s_waitcnt lgkmcnt(6)\n"
    "ds_read_b128 a[76:79], v228 offset:8960\n"
    "v_exp_f32 v193, v193\n"
    "v_exp_f32 v194, v194\n"
    "v_exp_f32 v195, v195\n"
    "v_add_f32 v224, v224, v192\n"
    "v_add_f32 v224, v224, v193\n"
    "v_add_f32 v224, v224, v194\n"
    "v_add_f32 v224, v224, v195\n"
    "v_cvt_pk_bf16_f32 v128, v192, v193\n"
    "v_cvt_pk_bf16_f32 v129, v194, v195\n"
    "v_mfma_f32_16x16x32_bf16 v[192:195], a[80:83], a[0:3], v[208:211]\n"
    "s_waitcnt lgkmcnt(6)\n"
    "v_exp_f32 v196, v196\n"
    "v_mfma_f32_16x16x32_bf16 v[192:195], a[84:87], a[4:7], v[192:195]\n"
    "s_waitcnt lgkmcnt(5)\n"
    "v_exp_f32 v197, v197\n"
    "v_mfma_f32_16x16x32_bf16 v[192:195], a[88:91], a[8:11], v[192:195]\n"
    "s_waitcnt lgkmcnt(4)\n"
    "v_exp_f32 v198, v198\n"
    "v_mfma_f32_16x16x32_bf16 v[192:195], a[92:95], a[12:15], v[192:195]\n"
    "v_exp_f32 v199, v199\n"
    "v_cvt_pk_bf16_f32 v132, v196, v197\n"
    "v_cvt_pk_bf16_f32 v133, v198, v199\n"
    "v_mfma_f32_16x16x32_bf16 v[196:199], a[80:83], a[16:19], v[212:215]\n"
    "v_exp_f32 v200, v200\n"
    "v_mfma_f32_16x16x32_bf16 v[196:199], a[84:87], a[20:23], v[196:199]\n"
    "v_exp_f32 v201, v201\n"
    "v_mfma_f32_16x16x32_bf16 v[196:199], a[88:91], a[24:27], v[196:199]\n"
    "v_exp_f32 v202, v202\n"
    "v_mfma_f32_16x16x32_bf16 v[196:199], a[92:95], a[28:31], v[196:199]\n"
    "v_exp_f32 v203, v203\n"
    "v_add_f32 v226, v226, v200\n"
    "v_add_f32 v226, v226, v201\n"
    "v_add_f32 v226, v226, v202\n"
    "v_add_f32 v226, v226, v203\n"
    "v_cvt_pk_bf16_f32 v136, v200, v201\n"
    "v_cvt_pk_bf16_f32 v137, v202, v203\n"
    "v_mfma_f32_16x16x32_bf16 v[200:203], a[80:83], a[32:35], v[216:219]\n"
    "v_exp_f32 v204, v204\n"
    "v_mfma_f32_16x16x32_bf16 v[200:203], a[84:87], a[36:39], v[200:203]\n"
    "v_exp_f32 v205, v205\n"
    "v_mfma_f32_16x16x32_bf16 v[200:203], a[88:91], a[40:43], v[200:203]\n"
    "v_exp_f32 v206, v206\n"
    "v_mfma_f32_16x16x32_bf16 v[200:203], a[92:95], a[44:47], v[200:203]\n"
    "v_exp_f32 v207, v207\n"
    "v_cvt_pk_bf16_f32 v140, v204, v205\n"
    "v_cvt_pk_bf16_f32 v141, v206, v207\n"
    "v_mfma_f32_16x16x32_bf16 v[204:207], a[80:83], a[48:51], v[220:223]\n"
    "ds_read_b128 a[80:83], v228 offset:12288\n"
    "v_mfma_f32_16x16x32_bf16 v[204:207], a[84:87], a[52:55], v[204:207]\n"
    "ds_read_b128 a[84:87], v228 offset:12544\n"
    "v_mfma_f32_16x16x32_bf16 v[204:207], a[88:91], a[56:59], v[204:207]\n"
    "ds_read_b128 a[88:91], v228 offset:12800\n"
    "v_mfma_f32_16x16x32_bf16 v[204:207], a[92:95], a[60:63], v[204:207]\n"
    "s_waitcnt lgkmcnt(6)\n"
    "ds_read_b128 a[92:95], v228 offset:13056\n"
    "v_exp_f32 v192, v192\n"
    "v_exp_f32 v193, v193\n"
    "v_exp_f32 v194, v194\n"
    "v_exp_f32 v195, v195\n"
    "v_add_f32 v224, v224, v192\n"
    "v_add_f32 v224, v224, v193\n"
    "v_add_f32 v224, v224, v194\n"
    "v_add_f32 v224, v224, v195\n"
    "v_cvt_pk_bf16_f32 v130, v192, v193\n"
    "v_cvt_pk_bf16_f32 v131, v194, v195\n"
    "v_mfma_f32_16x16x32_bf16 v[192:195], a[64:67], a[0:3], v[208:211]\n"
    "s_waitcnt lgkmcnt(6)\n"
    "v_exp_f32 v196, v196\n"
    "v_mfma_f32_16x16x32_bf16 v[192:195], a[68:71], a[4:7], v[192:195]\n"
    "s_waitcnt lgkmcnt(5)\n"
    "v_exp_f32 v197, v197\n"
    "v_mfma_f32_16x16x32_bf16 v[192:195], a[72:75], a[8:11], v[192:195]\n"
    "s_waitcnt lgkmcnt(4)\n"
    "v_exp_f32 v198, v198\n"
    "v_mfma_f32_16x16x32_bf16 v[192:195], a[76:79], a[12:15], v[192:195]\n"
    "v_exp_f32 v199, v199\n"
    "v_cvt_pk_bf16_f32 v134, v196, v197\n"
    "v_cvt_pk_bf16_f32 v135, v198, v199\n"
    "v_mfma_f32_16x16x32_bf16 v[196:199], a[64:67], a[16:19], v[212:215]\n"
    "v_exp_f32 v200, v200\n"
    "v_mfma_f32_16x16x32_bf16 v[196:199], a[68:71], a[20:23], v[196:199]\n"
    "v_exp_f32 v201, v201\n"
    "v_mfma_f32_16x16x32_bf16 v[196:199], a[72:75], a[24:27], v[196:199]\n"
    "v_exp_f32 v202, v202\n"
    "v_mfma_f32_16x16x32_bf16 v[196:199], a[76:79], a[28:31], v[196:199]\n"
    "v_exp_f32 v203, v203\n"
    "v_add_f32 v226, v226, v200\n"
    "v_add_f32 v226, v226, v201\n"
    "v_add_f32 v226, v226, v202\n"
    "v_add_f32 v226, v226, v203\n"
    "v_cvt_pk_bf16_f32 v138, v200, v201\n"
    "v_cvt_pk_bf16_f32 v139, v202, v203\n"
    "v_mfma_f32_16x16x32_bf16 v[200:203], a[64:67], a[32:35], v[216:219]\n"
    "v_exp_f32 v204, v204\n"
    "v_mfma_f32_16x16x32_bf16 v[200:203], a[68:71], a[36:39], v[200:203]\n"
    "v_exp_f32 v205, v205\n"
    "v_mfma_f32_16x16x32_bf16 v[200:203], a[72:75], a[40:43], v[200:203]\n"
    "v_exp_f32 v206, v206\n"
    "v_mfma_f32_16x16x32_bf16 v[200:203], a[76:79], a[44:47], v[200:203]\n"
    "v_exp_f32 v207, v207\n"
    "v_cvt_pk_bf16_f32 v142, v204, v205\n"
    "v_cvt_pk_bf16_f32 v143, v206, v207\n"
    "v_mfma_f32_16x16x32_bf16 v[204:207], a[64:67], a[48:51], v[220:223]\n"
    "ds_read_b128 a[64:67], v228 offset:16384\n"
    "v_mfma_f32_16x16x32_bf16 v[204:207], a[68:71], a[52:55], v[204:207]\n"
    "ds_read_b128 a[68:71], v228 offset:16640\n"
    "v_mfma_f32_16x16x32_bf16 v[204:207], a[72:75], a[56:59], v[204:207]\n"
    "ds_read_b128 a[72:75], v228 offset:16896\n"
    "v_mfma_f32_16x16x32_bf16 v[204:207], a[76:79], a[60:63], v[204:207]\n"
    "s_waitcnt lgkmcnt(6)\n"
    "ds_read_b128 a[76:79], v228 offset:17152\n"
    "v_exp_f32 v192, v192\n"
    "v_exp_f32 v193, v193\n"
    "v_exp_f32 v194, v194\n"
    "v_exp_f32 v195, v195\n"
    "v_add_f32 v224, v224, v192\n"
    "v_add_f32 v224, v224, v193\n"
    "v_add_f32 v224, v224, v194\n"
    "v_add_f32 v224, v224, v195\n"
    "v_cvt_pk_bf16_f32 v144, v192, v193\n"
    "v_cvt_pk_bf16_f32 v145, v194, v195\n"
    "v_mfma_f32_16x16x32_bf16 v[192:195], a[80:83], a[0:3], v[208:211]\n"
    "s_waitcnt lgkmcnt(6)\n"
    "v_exp_f32 v196, v196\n"
    "v_mfma_f32_16x16x32_bf16 v[192:195], a[84:87], a[4:7], v[192:195]\n"
    "s_waitcnt lgkmcnt(5)\n"
    "v_exp_f32 v197, v197\n"
    "v_mfma_f32_16x16x32_bf16 v[192:195], a[88:91], a[8:11], v[192:195]\n"
    "s_waitcnt lgkmcnt(4)\n"
    "v_exp_f32 v198, v198\n"
    "v_mfma_f32_16x16x32_bf16 v[192:195], a[92:95], a[12:15], v[192:195]\n"
    "v_exp_f32 v199, v199\n"
    "v_cvt_pk_bf16_f32 v148, v196, v197\n"
    "v_cvt_pk_bf16_f32 v149, v198, v199\n"
    "v_mfma_f32_16x16x32_bf16 v[196:199], a[80:83], a[16:19], v[212:215]\n"
    "v_exp_f32 v200, v200\n"
    "v_mfma_f32_16x16x32_bf16 v[196:199], a[84:87], a[20:23], v[196:199]\n"
    "v_exp_f32 v201, v201\n"
    "v_mfma_f32_16x16x32_bf16 v[196:199], a[88:91], a[24:27], v[196:199]\n"
    "v_exp_f32 v202, v202\n"
    "v_mfma_f32_16x16x32_bf16 v[196:199], a[92:95], a[28:31], v[196:199]\n"
    "v_exp_f32 v203, v203\n"
    "v_add_f32 v226, v226, v200\n"
    "v_add_f32 v226, v226, v201\n"
    "v_add_f32 v226, v226, v202\n"
    "v_add_f32 v226, v226, v203\n"
    "v_cvt_pk_bf16_f32 v152, v200, v201\n"
    "v_cvt_pk_bf16_f32 v153, v202, v203\n"
    "v_mfma_f32_16x16x32_bf16 v[200:203], a[80:83], a[32:35], v[216:219]\n"
    "v_exp_f32 v204, v204\n"
    "v_mfma_f32_16x16x32_bf16 v[200:203], a[84:87], a[36:39], v[200:203]\n"
    "v_exp_f32 v205, v205\n"
    "v_mfma_f32_16x16x32_bf16 v[200:203], a[88:91], a[40:43], v[200:203]\n"
    "v_exp_f32 v206, v206\n"
    "v_mfma_f32_16x16x32_bf16 v[200:203], a[92:95], a[44:47], v[200:203]\n"
    "v_exp_f32 v207, v207\n"
    "v_cvt_pk_bf16_f32 v156, v204, v205\n"
    "v_cvt_pk_bf16_f32 v157, v206, v207\n"
    "v_mfma_f32_16x16x32_bf16 v[204:207], a[80:83], a[48:51], v[220:223]\n"
    "ds_read_b128 a[80:83], v228 offset:20480\n"
    "ds_read_b64_tr_b16 a[96:97], v229 offset:0\n"
    "v_mfma_f32_16x16x32_bf16 v[204:207], a[84:87], a[52:55], v[204:207]\n"
    "ds_read_b64_tr_b16 a[98:99], v229 offset:4096\n"
    "ds_read_b64_tr_b16 a[100:101], v229 offset:8192\n"
    "v_mfma_f32_16x16x32_bf16 v[204:207], a[88:91], a[56:59], v[204:207]\n"
    "ds_read_b64_tr_b16 a[102:103], v229 offset:12288\n"
    "v_mfma_f32_16x16x32_bf16 v[204:207], a[92:95], a[60:63], v[204:207]\n"
    "v_exp_f32 v192, v192\n"
    "v_exp_f32 v193, v193\n"
    "v_exp_f32 v194, v194\n"
    "v_exp_f32 v195, v195\n"
    "v_add_f32 v224, v224, v192\n"
    "v_add_f32 v224, v224, v193\n"
    "v_add_f32 v224, v224, v194\n"
    "v_add_f32 v224, v224, v195\n"
    "v_cvt_pk_bf16_f32 v146, v192, v193\n"
    "v_cvt_pk_bf16_f32 v147, v194, v195\n"
    "v_exp_f32 v196, v196\n"
    "v_exp_f32 v197, v197\n"
    "v_exp_f32 v198, v198\n"
    "v_exp_f32 v199, v199\n"
    "v_cvt_pk_bf16_f32 v150, v196, v197\n"
    "v_cvt_pk_bf16_f32 v151, v198, v199\n"
    "v_exp_f32 v200, v200\n"
    "v_exp_f32 v201, v201\n"
    "v_exp_f32 v202, v202\n"
    "v_exp_f32 v203, v203\n"
    "v_add_f32 v226, v226, v200\n"
    "v_add_f32 v226, v226, v201\n"
    "v_add_f32 v226, v226, v202\n"
    "v_add_f32 v226, v226, v203\n"
    "v_cvt_pk_bf16_f32 v154, v200, v201\n"
    "s_waitcnt lgkmcnt(0)\n"
    "s_barrier\n"
    "s_nop 7\n"
    "s_branch Lit0%=\n"
    ".p2align 12\n"
    : "+v"(tid)
    : "s"(__builtin_amdgcn_kernarg_segment_ptr()), "s"(blockIdx.x), "s"((unsigned)(size_t)lds)
    : "v1", "v2", "v3", "v4", "v5", "v6", "v7", "v8", "v9", "v10", "v11", "v12", "v13", "v14", "v15", "v16", "v17", "v18", "v19", "v20", "v21", "v22", "v23", "v24", "v25", "v26", "v27", "v28", "v29", "v30", "v31", "v32", "v33", "v34", "v35", "v36", "v37", "v38", "v39", "v40", "v41", "v42", "v43", "v44", "v45", "v46", "v47", "v48", "v49", "v50", "v51", "v52", "v53", "v54", "v55", "v56", "v57", "v58", "v59", "v60", "v61", "v62", "v63", "v64", "v65", "v66", "v67", "v68", "v69", "v70", "v71", "v72", "v73", "v74", "v75", "v76", "v77", "v78", "v79", "v80", "v81", "v82", "v83", "v84", "v85", "v86", "v87", "v88", "v89", "v90", "v91", "v92", "v93", "v94", "v95", "v96", "v97", "v98", "v99", "v100", "v101", "v102", "v103", "v104", "v105", "v106", "v107", "v108", "v109", "v110", "v111", "v112", "v113", "v114", "v115", "v116", "v117", "v118", "v119", "v120", "v121", "v122", "v123", "v124", "v125", "v126", "v127", "v128", "v129", "v130", "v131", "v132", "v133", "v134", "v135", "v136", "v137", "v138", "v139", "v140", "v141", "v142", "v143", "v144", "v145", "v146", "v147", "v148", "v149", "v150", "v151", "v152", "v153", "v154", "v155", "v156", "v157", "v158", "v159", "v160", "v161", "v162", "v163", "v164", "v165", "v166", "v167", "v168", "v169", "v170", "v171", "v172", "v173", "v174", "v175", "v176", "v177", "v178", "v179", "v180", "v181", "v182", "v183", "v184", "v185", "v186", "v187", "v188", "v189", "v190", "v191", "v192", "v193", "v194", "v195", "v196", "v197", "v198", "v199", "v200", "v201", "v202", "v203", "v204", "v205", "v206", "v207", "v208", "v209", "v210", "v211", "v212", "v213", "v214", "v215", "v216", "v217", "v218", "v219", "v220", "v221", "v222", "v223", "v224", "v225", "v226", "v227", "v228", "v229", "v230", "v231", "v232", "v233", "v234", "v235", "v236", "v237", "v238", "v239", "v240", "v241", "v242", "v243", "v244", "v245", "v246", "v247", "v248", "v249", "v250", "v251", "v252", "v253", "v254", "v255", "a0", "a1", "a2", "a3", "a4", "a5", "a6", "a7", "a8", "a9", "a10", "a11", "a12", "a13", "a14", "a15", "a16", "a17", "a18", "a19", "a20", "a21", "a22", "a23", "a24", "a25", "a26", "a27", "a28", "a29", "a30", "a31", "a32", "a33", "a34", "a35", "a36", "a37", "a38", "a39", "a40", "a41", "a42", "a43", "a44", "a45", "a46", "a47", "a48", "a49", "a50", "a51", "a52", "a53", "a54", "a55", "a56", "a57", "a58", "a59", "a60", "a61", "a62", "a63", "a64", "a65", "a66", "a67", "a68", "a69", "a70", "a71", "a72", "a73", "a74", "a75", "a76", "a77", "a78", "a79", "a80", "a81", "a82", "a83", "a84", "a85", "a86", "a87", "a88", "a89", "a90", "a91", "a92", "a93", "a94", "a95", "a96", "a97", "a98", "a99", "a100", "a101", "a102", "a103", "a104", "a105", "a106", "a107", "a108", "a109", "a110", "a111", "a112", "a113", "a114", "a115", "a116", "a117", "a118", "a119", "a120", "a121", "a122", "a123", "a124", "a125", "a126", "a127", "a128", "a129", "a130", "a131", "a132", "a133", "a134", "a135", "a136", "a137", "a138", "a139", "a140", "a141", "a142", "a143", "a144", "a145", "a146", "a147", "a148", "a149", "a150", "a151", "a152", "a153", "a154", "a155", "a156", "a157", "a158", "a159", "a160", "a161", "a162", "a163", "a164", "a165", "a166", "a167", "a168", "a169", "a170", "a171", "a172", "a173", "a174", "a175", "a176", "a177", "a178", "a179", "a180", "a181", "a182", "a183", "a184", "a185", "a186", "a187", "a188", "a189", "a190", "a191", "a192", "a193", "a194", "a195", "a196", "a197", "a198", "a199", "a200", "a201", "a202", "a203", "a204", "a205", "a206", "a207", "a208", "a209", "a210", "a211", "a212", "a213", "a214", "a215", "a216", "a217", "a218", "a219", "a220", "a221", "a222", "a223", "a224", "a225", "a226", "a227", "a228", "a229", "a230", "a231", "a232", "a233", "a234", "a235", "a236", "a237", "a238", "a239", "a240", "a241", "a242", "a243", "a244", "a245", "a246", "a247", "a248", "a249", "a250", "a251", "a252", "a253", "a254", "a255", "s4", "s5", "s6", "s7", "s8", "s9", "s10", "s11", "s12", "s13", "s14", "s15", "s16", "s17", "s18", "s19", "s20", "s21", "s22", "s23", "s24", "s25", "s26", "s27", "s28", "s29", "s30", "s31", "s32", "s33", "s34", "s35", "s36", "s37", "s38", "s39", "s40", "s41", "s42", "s43", "s44", "s45", "s46", "s47", "s48", "s49", "s50", "s51", "s52", "s53", "s54", "s55", "s56", "s57", "s58", "s59", "s60", "s61", "s62", "s63", "s64", "s65", "s66", "s67", "s68", "s69", "s70", "s71", "s72", "s73", "s74", "s75", "s76", "s77", "s78", "s79", "s80", "s81", "s82", "s83", "s84", "s85", "s86", "s87", "s88", "s89", "s90", "s91", "s92", "s93", "s94", "s95", "s96", "s97", "s98", "s99", "vcc", "scc", "memory");
}

extern "C" void kernel_launch(void* const* d_in, const int* in_sizes, int n_in, void* d_out, int out_size, void* d_ws, size_t ws_size, hipStream_t stream) {
  mha_fused<<<256, 256, 0, stream>>>(d_in[0], d_in[1], d_in[2], d_in[3], d_in[4], d_out, d_ws);
}
```
